```python
import torch

def _softmax(x, axis=-1):
    return torch.softmax(x, dim=axis)
def _gelu(x):
    return torch.nn.functional.gelu(x, approximate="tanh")
def _silu(x):
    return torch.nn.functional.silu(x)
def _sigmoid(x):
    return torch.sigmoid(x)
def _layernorm(x, axis=-1, eps=1e-5):
    m = x.mean(dim=axis, keepdim=True)
    v = x.var(dim=axis, keepdim=True, unbiased=False)
    return (x - m) / torch.sqrt(v + eps)
def _scatter_add(tgt, idx, src, axis=0):
    return tgt.clone().index_add_(axis, idx, src)
def _segment_sum(src, seg, n):
    out = torch.zeros((n,) + src.shape[1:], dtype=src.dtype)
    return out.index_add_(0, seg, src)
def _pad(x, pw):
    flat = tuple(v for pair in reversed(pw) for v in pair)
    return torch.nn.functional.pad(x, flat)

kernel_name = 'standalone__mha_block_cached__near_ridge'

def setup_inputs():

    _w = (lambda a: (lambda t: t.to(torch.bfloat16) if t.is_floating_point() else t)(torch.as_tensor(a)))
    B, Sq, D = 8, 192, 576
    H, Sk, Dh = 32, 512, 128
    return {
        "x": _w((lambda *s: torch.randn(*s, dtype=torch.bfloat16) * 0.8)(B, Sq, D)),
        "k_cache": _w((lambda *s: torch.randn(*s, dtype=torch.bfloat16) * 0.8)(B, H, Sk, Dh)),
        "v_cache": _w((lambda *s: torch.randn(*s, dtype=torch.bfloat16) * 0.05)(B, H, Sk, Dh)),
        "wq": _w((lambda *s: torch.randn(*s, dtype=torch.bfloat16) * 0.8)(D, H * Dh) * (D ** -0.5)),
        "wo": _w((lambda *s: torch.randn(*s, dtype=torch.bfloat16) * 0.05)(H * Dh, D) * ((H * Dh) ** -0.5)),
    }

def reference(x, k_cache, v_cache, wq, wo):
    B, Sq, D = x.shape
    H, Dh = 32, 128
    q = (lambda x, *d: x.permute(*d))((x @ wq).reshape(B, Sq, H, Dh), 0, 2, 1, 3)
    scores = torch.einsum("bhqd,bhkd->bhqk", q, k_cache) * (Dh ** -0.5)
    p = _softmax(scores, axis=-1)
    o = torch.einsum("bhqk,bhkd->bhqd", p, v_cache)
    return (lambda x, *d: x.permute(*d))(o, 0, 2, 1, 3).reshape(B, Sq, H * Dh) @ wo
```

```cpp
#include <hip/hip_runtime.h>
#include <hip/hip_bf16.h>
typedef __hip_bfloat16 bf16;
constexpr int B=8, SQ=192, DM=576, H=32, SK=512, DH=128;
__device__ inline float b2f(bf16 v){ return __bfloat162float(v); }
__global__ void gemm_naive(const bf16* A, const bf16* Bm, bf16* C, int M, int N, int K){
  __shared__ float As[16][17], Bs[16][17];
  int tx=threadIdx.x&15, ty=threadIdx.x>>4;
  int row=blockIdx.y*16+ty, col=blockIdx.x*16+tx;
  float acc=0;
  for(int k0=0;k0<K;k0+=16){
    As[ty][tx]=b2f(A[(long)row*K+k0+tx]);
    Bs[ty][tx]=b2f(Bm[(long)(k0+ty)*N+col]);
    __syncthreads();
    for(int k=0;k<16;k++) acc+=As[ty][k]*Bs[k][tx];
    __syncthreads();
  }
  C[(long)row*N+col]=__float2bfloat16(acc);
}
__global__ void attn_naive(const bf16* Q, const bf16* Kc, const bf16* Vc, bf16* O){
  int q=blockIdx.x, h=blockIdx.y, b=blockIdx.z, t=threadIdx.x;
  __shared__ float qs[DH]; __shared__ float s[SK]; __shared__ float red[128];
  qs[t]=b2f(Q[((long)(b*SQ+q))*H*DH+h*DH+t]);
  __syncthreads();
  const bf16* Kp=Kc+((long)(b*H+h))*SK*DH; const bf16* Vp=Vc+((long)(b*H+h))*SK*DH;
  float mx=-1e30f;
  for(int k=t;k<SK;k+=128){ float a=0; for(int d=0;d<DH;d++) a+=qs[d]*b2f(Kp[(long)k*DH+d]); a*=0.08838834764831845f; s[k]=a; mx=fmaxf(mx,a);}
  red[t]=mx; __syncthreads();
  for(int o=64;o>0;o>>=1){ if(t<o) red[t]=fmaxf(red[t],red[t+o]); __syncthreads(); }
  mx=red[0]; __syncthreads();
  float sm=0; for(int k=t;k<SK;k+=128){ float e=__expf(s[k]-mx); s[k]=e; sm+=e; }
  red[t]=sm; __syncthreads();
  for(int o=64;o>0;o>>=1){ if(t<o) red[t]+=red[t+o]; __syncthreads(); }
  sm=red[0];
  float a=0; for(int k=0;k<SK;k++) a+=s[k]*b2f(Vp[(long)k*DH+t]);
  O[((long)(b*SQ+q))*H*DH+h*DH+t]=__float2bfloat16(a/sm);
}
extern "C" void kernel_launch(void* const* d_in, const int* in_sizes, int n_in, void* d_out, int out_size, void* d_ws, size_t ws_size, hipStream_t stream){
  const bf16* x=(const bf16*)d_in[0]; const bf16* kc=(const bf16*)d_in[1]; const bf16* vc=(const bf16*)d_in[2];
  const bf16* wq=(const bf16*)d_in[3]; const bf16* wo=(const bf16*)d_in[4];
  bf16* q=(bf16*)d_ws; bf16* o=q+(long)B*SQ*H*DH;
  gemm_naive<<<dim3(H*DH/16,B*SQ/16),256,0,stream>>>(x,wq,q,B*SQ,H*DH,DM);
  attn_naive<<<dim3(SQ,H,B),128,0,stream>>>(q,kc,vc,o);
  gemm_naive<<<dim3(DM/16,B*SQ/16),256,0,stream>>>(o,wo,(bf16*)d_out,B*SQ,DM,H*DH);
}
```

```cpp
#include <hip/hip_runtime.h>
#include <hip/hip_bf16.h>
#include <type_traits>

typedef __bf16 bf16x8 __attribute__((ext_vector_type(8)));
typedef __bf16 bf16x4 __attribute__((ext_vector_type(4)));
typedef __bf16 bf16x2 __attribute__((ext_vector_type(2)));
typedef float f32x16 __attribute__((ext_vector_type(16)));
typedef float f32x4 __attribute__((ext_vector_type(4)));
typedef float f32x2 __attribute__((ext_vector_type(2)));
typedef unsigned u32x4 __attribute__((ext_vector_type(4)));
typedef unsigned u32x2 __attribute__((ext_vector_type(2)));
typedef __attribute__((address_space(3))) char lds_char;
typedef __attribute__((address_space(3))) bf16x4 lds_bf16x4;
typedef __attribute__((address_space(3))) bf16x8 lds_bf16x8;
typedef __attribute__((address_space(3))) f32x4 lds_f32x4;
typedef __attribute__((address_space(3))) f32x2 lds_f32x2;
typedef __attribute__((address_space(3))) u32x4 lds_u32x4;

#define DI __device__ __forceinline__
#define TNOW() 0ull
#define WREC(i, v) do {} while (0)
#define TBAR(accw) bar()
#define TVM(N, accw) wait_vm<N>()
#define STAMP(i) do {} while (0)
#define MFMA32(a, b, c) __builtin_amdgcn_mfma_f32_32x32x16_bf16((a), (b), (c), 0, 0, 0)

constexpr int NB = 8, SQ = 192, DM = 576, NH = 32, SK = 512, DH = 128;
constexpr float QSCALE = 0.08838834764831845f * 1.4426950408889634f;
constexpr float THR = 8.0f;

constexpr unsigned QS_BYTES = 40960, QS_W = 24576;
constexpr unsigned KV3 = 122880, SPARE = 155648, LDS_TOTAL = 163840;
DI constexpr unsigned kv_slot(int s) { return (unsigned)((s + 3) & 3) * QS_BYTES; }

DI unsigned rfl(unsigned x) { return __builtin_amdgcn_readfirstlane(x); }
DI unsigned cvtpk(float lo, float hi) { f32x2 v = {lo, hi}; bf16x2 b = __builtin_convertvector(v, bf16x2); return __builtin_bit_cast(unsigned, b); }
DI bf16x8 pack8(const f32x16& x, int s) {
  u32x4 p = {cvtpk(x[8 * s], x[8 * s + 1]), cvtpk(x[8 * s + 2], x[8 * s + 3]), cvtpk(x[8 * s + 4], x[8 * s + 5]), cvtpk(x[8 * s + 6], x[8 * s + 7])};
  return __builtin_bit_cast(bf16x8, p);
}
DI bf16x8 lds_rd128(lds_char* base, unsigned off) { return *(lds_bf16x8*)(base + off); }
DI bf16x8 lds_tr2(lds_char* base, unsigned off0, unsigned off1) {
  bf16x4 lo = __builtin_amdgcn_ds_read_tr16_b64_v4bf16((lds_bf16x4*)(base + off0));
  bf16x4 hi = __builtin_amdgcn_ds_read_tr16_b64_v4bf16((lds_bf16x4*)(base + off1));
  return __builtin_shufflevector(lo, hi, 0, 1, 2, 3, 4, 5, 6, 7);
}
DI void dma16(__amdgpu_buffer_rsrc_t r, unsigned voff, unsigned soff, unsigned m0v) {
  asm volatile("s_mov_b32 m0, %3\n\ts_nop 0\n\tbuffer_load_dwordx4 %0, %1, %2 offen lds" ::"v"(voff), "s"(r), "s"(soff), "s"(m0v) : "memory");
}
DI void dma16nt(__amdgpu_buffer_rsrc_t r, unsigned voff, unsigned soff, unsigned m0v) {
  asm volatile("s_mov_b32 m0, %3\n\ts_nop 0\n\tbuffer_load_dwordx4 %0, %1, %2 offen nt lds" ::"v"(voff), "s"(r), "s"(soff), "s"(m0v) : "memory");
}
template <int N> DI void wait_bar() { asm volatile("s_waitcnt vmcnt(%0) lgkmcnt(0)\n\ts_barrier" ::"i"(N) : "memory"); }
DI float pl32_max(float v) {
  auto r = __builtin_amdgcn_permlane32_swap(__float_as_uint(v), __float_as_uint(v), false, false);
  return fmaxf(__uint_as_float(r[0]), __uint_as_float(r[1]));
}
DI float pl32_sum(float v) {
  auto r = __builtin_amdgcn_permlane32_swap(__float_as_uint(v), __float_as_uint(v), false, false);
  return __uint_as_float(r[0]) + __uint_as_float(r[1]);
}

struct P1Ctx {
  __amdgpu_buffer_rsrc_t rx, rw, rk, rv;
  unsigned vx, vw, vk, vv;
  unsigned sx, sw, skv;
  unsigned ldsb;
};

DI void issue_q(const P1Ctx& c, unsigned lw, int i, unsigned st) {
  const unsigned slot = c.ldsb + (unsigned)(i % 3) * QS_BYTES;
#pragma unroll
  for (int p = 0; p < 3; ++p) dma16(c.rx, c.vx, c.sx + (unsigned)(8 * 8 * p) * 1152u + st * 128u, slot + (lw + 8 * p) * 1024u);
#pragma unroll
  for (int p = 0; p < 2; ++p) dma16(c.rw, c.vw, c.sw + (st * 64 + 32 * p) * 8192u, slot + QS_W + (lw + 8 * p) * 1024u);
}
DI void issue_kv1(const P1Ctx& c, unsigned p, int s) {
  const unsigned dst = c.ldsb + kv_slot(s) + p * 1024u;
  const unsigned so = c.skv + ((p >> 3) & 1) * 65536u + (p & 7) * 1024u + (unsigned)s * 8192u;
  if (p < 16) dma16nt(c.rk, c.vk ^ ((p & 3) << 6), so, dst);
  else dma16nt(c.rv, c.vv, so, dst);
}
template <int N> DI void wait_vm() { asm volatile("s_waitcnt vmcnt(%0)" ::"i"(N) : "memory"); }
DI void bar() { __builtin_amdgcn_sched_barrier(0); asm volatile("s_waitcnt lgkmcnt(0)\n\ts_barrier" ::: "memory"); __builtin_amdgcn_sched_barrier(0); }
#define SB() __builtin_amdgcn_sched_barrier(0)
DI unsigned qblk(unsigned blk) { return blk < 40 ? 2 * QS_BYTES + blk * 1024 : SPARE + (blk - 40) * 1024; }

__global__ __launch_bounds__(768, 3) void attn_kernel(const __hip_bfloat16* __restrict__ x, const __hip_bfloat16* __restrict__ kc,
                                                      const __hip_bfloat16* __restrict__ vc, const __hip_bfloat16* __restrict__ wq,
                                                      __hip_bfloat16* __restrict__ obuf, unsigned long long* diag) {
  STAMP(0);
  __shared__ __attribute__((aligned(1024))) char smem[LDS_TOTAL];
  lds_char* lds = (lds_char*)smem;
  const unsigned tid = threadIdx.x, lane = tid & 63, w = rfl(tid >> 6);
  const unsigned r = lane & 31, h = lane >> 5;
  const unsigned xcd = blockIdx.x & 7, jj = blockIdx.x >> 3;
  const unsigned b = 4 * (xcd & 1) + (jj & 3), hd = 8 * (xcd >> 1) + (jj >> 2);
  const unsigned t = w >> 1, j = w & 1;
  const unsigned g1 = (lane >> 4) & 1, qq = (lane & 15) >> 2, pp = lane & 3, sp = ((pp & 1) << 1) | (pp >> 1);

  P1Ctx c;
  c.ldsb = (unsigned)(size_t)smem;
  c.rx = __builtin_amdgcn_make_buffer_rsrc((void*)x, (short)0, NB * SQ * DM * 2, 0x00020000);
  c.rw = __builtin_amdgcn_make_buffer_rsrc((void*)wq, (short)0, DM * NH * DH * 2, 0x00020000);
  c.rk = __builtin_amdgcn_make_buffer_rsrc((void*)kc, (short)0, NB * NH * SK * DH * 2, 0x00020000);
  c.rv = __builtin_amdgcn_make_buffer_rsrc((void*)vc, (short)0, NB * NH * SK * DH * 2, 0x00020000);
  c.skv = (b * NH + hd) * (SK * DH * 2);
  c.vk = (lane >> 4) * 256 + ((lane & 15) ^ (lane >> 4)) * 16;
  c.vv = (lane >> 4) * 256 + ((lane & 15) ^ ((lane >> 4) << 2)) * 16;

  const unsigned koff = ((jj >> 2) + 2 * (jj & 3)) % 9;
  if (w >= 4) {
    const unsigned lw = w - 4;
    c.vx = (lane >> 3) * 1152 + (((lane & 7) ^ (lane >> 4) ^ ((lw & 1) << 2)) * 16);
    c.sx = (b * SQ + 8 * lw) * 1152u;
    c.vw = (lane >> 4) * 8192 + (((lane & 15) ^ ((lane >> 4) << 2)) * 16);
    c.sw = 4 * lw * 8192u + hd * 256u;
    auto stg = [&](int i) { unsigned v = koff + i; return v >= 9 ? v - 9 : v; };
    auto kv4 = [&](int s) {
#pragma unroll
      for (int p = 0; p < 4; ++p) issue_kv1(c, lw * 4 + p, s);
    };
    issue_q(c, lw, 0, stg(0)); issue_q(c, lw, 1, stg(1));
    wait_vm<5>(); bar(); issue_q(c, lw, 2, stg(2));
    wait_vm<5>(); bar(); issue_q(c, lw, 3, stg(3));
    wait_vm<5>(); bar(); issue_q(c, lw, 4, stg(4)); kv4(0);
    wait_vm<9>(); bar(); issue_q(c, lw, 5, stg(5));
    wait_vm<9>(); bar(); issue_q(c, lw, 6, stg(6));
    wait_vm<5>(); bar(); issue_q(c, lw, 7, stg(7));
    wait_vm<5>(); bar(); issue_q(c, lw, 8, stg(8));
    wait_vm<5>(); bar(); kv4(1);
    wait_vm<4>(); bar(); kv4(2);
    bar();
    bar();
  } else {
    const unsigned qh = w >> 1, dh = w & 1;
    const unsigned xa0 = (96 * qh + r) * 128 + ((h ^ ((r >> 1) & 7)) << 4);
    const unsigned wa0 = QS_W + (8 * h + qq) * 256 + ((4 * ((2 * dh) ^ qq) + 2 * g1 + (sp >> 1)) << 4) + (sp & 1) * 8;
    f32x16 qacc[3][2];
#pragma unroll
    for (int a = 0; a < 3; ++a)
#pragma unroll
      for (int i = 0; i < 16; ++i) { qacc[a][0][i] = 0.f; qacc[a][1][i] = 0.f; }
    {
      bf16x8 bA[3], aA[2], bB[3], aB[2];
      unsigned xb, wb;
      auto rd = [&](bf16x8 (&bb)[3], bf16x8 (&aa)[2], int s) {
        const unsigned xs = xb ^ (s << 5), wb1 = wb ^ 64;
        aa[0] = lds_tr2(lds, wb + s * 4096, wb + s * 4096 + 1024);
        bb[0] = lds_rd128(lds, xs); bb[1] = lds_rd128(lds, xs + 4096); bb[2] = lds_rd128(lds, xs + 8192);
        aa[1] = lds_tr2(lds, wb1 + s * 4096, wb1 + s * 4096 + 1024);
      };
      auto mm = [&](bf16x8 (&bb)[3], bf16x8 (&aa)[2]) {
        qacc[0][0] = MFMA32(aa[0], bb[0], qacc[0][0]);
        qacc[1][0] = MFMA32(aa[0], bb[1], qacc[1][0]);
        qacc[2][0] = MFMA32(aa[0], bb[2], qacc[2][0]);
        qacc[0][1] = MFMA32(aa[1], bb[0], qacc[0][1]);
        qacc[1][1] = MFMA32(aa[1], bb[1], qacc[1][1]);
        qacc[2][1] = MFMA32(aa[1], bb[2], qacc[2][1]);
      };
      bar();
      STAMP(1);
      xb = xa0; wb = wa0;
      asm volatile("" : "+v"(xb), "+v"(wb));
      rd(bA, aA, 0);
#pragma unroll 1
      for (int st = 0; st < 8; ++st) {
        SB(); rd(bB, aB, 1); SB(); mm(bA, aA);
        SB(); rd(bA, aA, 2); SB(); mm(bB, aB);
        SB(); rd(bB, aB, 3); SB(); mm(bA, aA);
        bar();
        const unsigned so = (unsigned)((st + 1) % 3) * QS_BYTES;
        xb = xa0 + so; wb = wa0 + so;
        asm volatile("" : "+v"(xb), "+v"(wb));
        rd(bA, aA, 0);
        SB(); mm(bB, aB);
      }
      SB(); rd(bB, aB, 1); SB(); mm(bA, aA);
      SB(); rd(bA, aA, 2); SB(); mm(bB, aB);
      SB(); rd(bB, aB, 3); SB(); mm(bA, aA);
      SB(); mm(bB, aB);
    }
    bar();
    STAMP(4);
#pragma unroll
    for (int a = 0; a < 3; ++a)
#pragma unroll
      for (int d = 0; d < 2; ++d) {
#pragma unroll
        for (int i = 0; i < 16; ++i) qacc[a][d][i] *= QSCALE;
#pragma unroll
        for (int s2 = 0; s2 < 2; ++s2) {
          const unsigned blk = (3 * qh + a) * 8 + 4 * dh + 2 * d + s2;
          *(lds_bf16x8*)(lds + qblk(blk) + lane * 16) = pack8(qacc[a][d], s2);
        }
      }
    bar();
  }
  bf16x8 qf[8];
#pragma unroll
  for (int i = 0; i < 8; ++i) qf[i] = lds_rd128(lds, qblk(t * 8 + i) + lane * 16);

  const unsigned grp = w >> 2;
  f32x16 oacc[4];
#pragma unroll
  for (int d = 0; d < 4; ++d)
#pragma unroll
    for (int i = 0; i < 16; ++i) oacc[d][i] = 0.f;
  float m = -__builtin_inff(), l0 = 0.f, l1 = 0.f;
  const unsigned ka0 = j * 8192 + r * 256 + ((h ^ (r & 15)) << 4);
  const unsigned va0 = (16384 + j * 8192 + (4 * h + qq) * 256 + ((4 * qq + 2 * g1 + (sp >> 1)) << 4) + (sp & 1) * 8) ^ (j << 7);
  const unsigned vkx = c.vk ^ ((w & 3) << 6);
  const unsigned sp0 = c.skv + ((w >> 3) & 1) * 65536u + (w & 7) * 1024u;
  const unsigned sp1 = c.skv + (((w + 12) >> 3) & 1) * 65536u + ((w + 12) & 7) * 1024u;
  const unsigned sp2 = c.skv + (((w + 24) >> 3) & 1) * 65536u + ((w + 24) & 7) * 1024u;

  bf16x8 kf[4];
  auto step = [&](auto G_, auto MODE_, int s) {
    constexpr int G = decltype(G_)::value, MODE = decltype(MODE_)::value;
    constexpr int NP = G == 2 ? 2 : 3;
    unsigned vb = va0 + kv_slot(s), kc0 = ka0 + kv_slot(s), kb = ka0 + kv_slot(s + 1);
    asm volatile("" : "+v"(kb), "+v"(vb), "+v"(kc0));
    __builtin_amdgcn_s_setprio(1);
    f32x16 sc;
    bf16x8 vf[4][2];
    {
      bf16x8 kg[4];
#pragma unroll
      for (int ks = 0; ks < 4; ++ks) kg[ks] = lds_rd128(lds, kc0 ^ ((ks + 4) << 5));
      SB();
#pragma unroll
      for (int i = 0; i < 16; ++i) sc[i] = 0.f;
#pragma unroll
      for (int ks = 0; ks < 4; ++ks) sc = MFMA32(kf[ks], qf[ks], sc);
      SB();
#pragma unroll
      for (int d = 0; d < 4; ++d) { const unsigned va = vb ^ (d << 6); vf[d][0] = lds_tr2(lds, va, va + 2048); }
      SB();
#pragma unroll
      for (int ks = 0; ks < 4; ++ks) sc = MFMA32(kg[ks], qf[ks + 4], sc);
      SB();
#pragma unroll
      for (int d = 0; d < 4; ++d) { const unsigned va = vb ^ (d << 6); vf[d][1] = lds_tr2(lds, va + 4096, va + 4096 + 2048); }
      SB();
    }
    __builtin_amdgcn_s_setprio(0);
    if constexpr (G == 1) { if constexpr (MODE <= 1) wait_vm<NP>(); else if constexpr (MODE == 2) wait_vm<0>(); }
    bar();
    {
      const float t0 = fmaxf(fmaxf(sc[0], sc[1]), sc[2]), t1 = fmaxf(fmaxf(sc[3], sc[4]), sc[5]), t2 = fmaxf(fmaxf(sc[6], sc[7]), sc[8]);
      const float t3 = fmaxf(fmaxf(sc[9], sc[10]), sc[11]), t4 = fmaxf(fmaxf(sc[12], sc[13]), sc[14]);
      float tm = fmaxf(fmaxf(fmaxf(t0, t1), t2), fmaxf(fmaxf(t3, t4), sc[15]));
      tm = pl32_max(tm);
      if (__any(tm > m + THR)) {
        const float mn = fmaxf(m, tm);
        const float al = __builtin_amdgcn_exp2f(m - mn);
        l0 *= al; l1 *= al; m = mn;
#pragma unroll
        for (int d = 0; d < 4; ++d)
#pragma unroll
          for (int i = 0; i < 16; ++i) oacc[d][i] *= al;
      }
    }
#pragma unroll
    for (int i = 0; i < 16; ++i) sc[i] -= m;
    SB();
#pragma unroll
    for (int i = 0; i < 16; ++i) sc[i] = __builtin_amdgcn_exp2f(sc[i]);
    bf16x8 p0 = pack8(sc, 0), p1 = pack8(sc, 1);
#pragma unroll
    for (int i = 0; i < 16; i += 2) { l0 += sc[i]; l1 += sc[i + 1]; }
    asm volatile("" : "+v"(p0), "+v"(p1), "+v"(l0), "+v"(l1));
    if constexpr (G == 0) { if constexpr (MODE <= 1) wait_vm<NP>(); else if constexpr (MODE == 2) wait_vm<0>(); }
    bar();
    __builtin_amdgcn_s_setprio(1);
#pragma unroll
    for (int d = 0; d < 4; ++d) oacc[d] = MFMA32(vf[d][0], p0, oacc[d]);
    SB();
    if constexpr (MODE != 3) {
#pragma unroll
      for (int ks = 0; ks < 4; ++ks) kf[ks] = lds_rd128(lds, kb ^ (ks << 5));
    }
    SB();
#pragma unroll
    for (int d = 0; d < 4; ++d) oacc[d] = MFMA32(vf[d][1], p1, oacc[d]);
    __builtin_amdgcn_s_setprio(0);
    SB();
    if constexpr (MODE == 0) {
      const unsigned dst = c.ldsb + kv_slot(s + 3) + w * 1024u, sofs = (unsigned)(s + 3) * 8192u;
      dma16nt(c.rk, vkx, sp0 + sofs, dst);
      if constexpr (G == 0) dma16nt(c.rk, vkx, sp1 + sofs, dst + 12288u); else dma16nt(c.rv, c.vv, sp1 + sofs, dst + 12288u);
      if constexpr (G != 2) dma16nt(c.rv, c.vv, sp2 + sofs, dst + 24576u);
    }
    if constexpr (G == 2) { if constexpr (MODE == 0) wait_vm<NP>(); else if constexpr (MODE == 1) wait_vm<0>(); }
    bar();
  };
  auto run = [&](auto G_) {
    constexpr int G = decltype(G_)::value;
    {
      unsigned kb0 = ka0 + kv_slot(0);
      asm volatile("" : "+v"(kb0));
#pragma unroll
      for (int ks = 0; ks < 4; ++ks) kf[ks] = lds_rd128(lds, kb0 ^ (ks << 5));
    }
    if constexpr (G >= 1) bar();
    if constexpr (G == 2) { wait_vm<4>(); bar(); }
#pragma unroll 1
    for (int s = 0; s < 5; ++s) step(G_, std::integral_constant<int, 0>{}, s);
    step(G_, std::integral_constant<int, 1>{}, 5);
    step(G_, std::integral_constant<int, 2>{}, 6);
    step(G_, std::integral_constant<int, 3>{}, 7);
    if constexpr (G == 0) bar();
    if constexpr (G <= 1) bar();
  };
  bar(); STAMP(5);
  if (grp == 0) run(std::integral_constant<int, 0>{});
  else if (grp == 1) run(std::integral_constant<int, 1>{});
  else run(std::integral_constant<int, 2>{});

  float l = pl32_sum(l0 + l1);
  STAMP(13);
  {
    const unsigned xw = w * 8192 + lane * 16;
#pragma unroll
    for (int dd = 0; dd < 2; ++dd)
#pragma unroll
      for (int g = 0; g < 4; ++g) {
        f32x4 v = {oacc[2 + dd][4 * g], oacc[2 + dd][4 * g + 1], oacc[2 + dd][4 * g + 2], oacc[2 + dd][4 * g + 3]};
        *(lds_f32x4*)(lds + xw + (dd * 4 + g) * 1024) = v;
      }
    if (h == 0) { f32x2 ml = {m, l}; *(lds_f32x2*)(lds + SPARE + w * 256 + r * 8) = ml; }
    bar();
    f32x2 o = *(lds_f32x2*)(lds + SPARE + (w ^ 1) * 256 + r * 8);
    const float mn = fmaxf(m, o[0]);
    const float f = __builtin_amdgcn_exp2f(m - mn), fo = __builtin_amdgcn_exp2f(o[0] - mn);
    const float inv = 1.0f / (l * f + o[1] * fo);
    const float sa = f * inv, sb = fo * inv;
    const unsigned xr = (w ^ 1) * 8192 + lane * 16;
#pragma unroll
    for (int dd = 0; dd < 2; ++dd)
#pragma unroll
      for (int g = 0; g < 4; ++g) {
        f32x4 v = *(lds_f32x4*)(lds + xr + (dd * 4 + g) * 1024);
#pragma unroll
        for (int e = 0; e < 4; ++e) oacc[dd][4 * g + e] = oacc[dd][4 * g + e] * sa + v[e] * sb;
      }
    char* op = (char*)obuf + ((size_t)((b * NH + hd) * 6 + t) * 8 + 4 * j) * 1024 + lane * 16;
    *(bf16x8*)(op) = pack8(oacc[0], 0);
    *(bf16x8*)(op + 1024) = pack8(oacc[0], 1);
    *(bf16x8*)(op + 2048) = pack8(oacc[1], 0);
    *(bf16x8*)(op + 3072) = pack8(oacc[1], 1);
  }
}

constexpr int P2_SLOTS = 5;
__global__ __launch_bounds__(768, 3) void oproj_kernel(const __hip_bfloat16* __restrict__ obuf, const __hip_bfloat16* __restrict__ wo,
                                                       __hip_bfloat16* __restrict__ out, unsigned long long* diag) {
  STAMP(16);
  __shared__ __attribute__((aligned(1024))) char smem[P2_SLOTS * 32768];
  lds_char* lds = (lds_char*)smem;
  const unsigned tid = threadIdx.x, lane = tid & 63, w = rfl(tid >> 6);
  const unsigned r = lane & 31, h = lane >> 5;
  const unsigned b = blockIdx.x & 7, ti = blockIdx.x >> 3;
  const unsigned rt = ti / 9, ct = ti % 9;
  const unsigned ldsb = (unsigned)(size_t)smem;
  f32x16 acc;
#pragma unroll
  for (int i = 0; i < 16; ++i) acc[i] = 0.f;
  const unsigned nt = w & 1, qt = (w >> 1) & 1, kh = (w >> 2) & 1;
  if (w >= 8) {
    const unsigned lw = w - 8;
    const bool isO = lw < 2;
    __amdgpu_buffer_rsrc_t rs = isO ? __builtin_amdgcn_make_buffer_rsrc((void*)obuf, (short)0, NB * SQ * NH * DH * 2, 0x00020000)
                                    : __builtin_amdgcn_make_buffer_rsrc((void*)wo, (short)0, NH * DH * DM * 2, 0x00020000);
    const unsigned vo = isO ? lane * 16 : (lane >> 3) * 1152 + (((lane & 7) ^ (((lane >> 4) & 1) << 2)) * 16);
    const unsigned s0 = isO ? (b * NH * 6 + 2 * rt) * 8192u + lw * 8192u : 64 * (lw - 2) * 1152u + ct * 128u;
    const unsigned sst = isO ? 6 * 8192u : 128 * 1152u;
    const unsigned spc = isO ? 1024u : 8 * 1152u;
    auto issue = [&](int hd) {
      const unsigned dst = ldsb + (unsigned)(hd % P2_SLOTS) * 32768u + lw * 8192u;
#pragma unroll
      for (int i = 0; i < 8; ++i) dma16(rs, vo, s0 + (unsigned)hd * sst + i * spc, dst + i * 1024u);
    };
    unsigned long long tw = 0, tb = 0, t0 = TNOW();
    issue(0); issue(1); issue(2); issue(3);
#pragma unroll 1
    for (int hd = 0; hd < NH; ++hd) {
      { unsigned long long _a = TNOW();
      if (hd <= NH - 4) wait_vm<24>(); else if (hd == NH - 3) wait_vm<16>(); else if (hd == NH - 2) wait_vm<8>(); else wait_vm<0>();
      tw += TNOW() - _a; }
      TBAR(tb);
      if (hd + 4 < NH) issue(hd + 4);
    }
    WREC(10, TNOW() - t0); WREC(11, tw); WREC(12, tb);
  } else {
    const unsigned g1 = (lane >> 4) & 1, qq = (lane & 15) >> 2, pp = lane & 3, sp = ((pp & 1) << 1) | (pp >> 1);
    const unsigned ba0 = qt * 8192 + kh * 4096 + lane * 16;
    const unsigned aa0 = 16384 + (64 * kh + 8 * h + qq) * 128 + (((4 * nt + 2 * g1 + (sp >> 1)) ^ (((qq >> 1) & 1) << 2)) << 4) + (sp & 1) * 8;
    unsigned long long tb = 0, t0 = TNOW();
#pragma unroll 1
    for (int hd = 0; hd < NH; ++hd) {
      TBAR(tb);
      const unsigned so = (unsigned)(hd % P2_SLOTS) * 32768u;
      unsigned bb = ba0 + so, ab = aa0 + so;
      asm volatile("" : "+v"(bb), "+v"(ab));
#pragma unroll
      for (int ks = 0; ks < 4; ++ks) {
        bf16x8 bo = lds_rd128(lds, bb + ks * 1024);
        bf16x8 a = lds_tr2(lds, ab + ks * 2048, ab + ks * 2048 + 512);
        acc = MFMA32(a, bo, acc);
      }
    }
    WREC(10, TNOW() - t0); WREC(12, tb);
  }
  bar();
  STAMP(21);
  if (w < 8) {
    const unsigned base = kh * (64 * 272) + (32 * qt + r) * 272 + (32 * nt + 8 * h) * 4;
#pragma unroll
    for (int s = 0; s < 2; ++s)
#pragma unroll
      for (int g = 0; g < 2; ++g) {
        f32x4 v = {acc[8 * s + 4 * g], acc[8 * s + 4 * g + 1], acc[8 * s + 4 * g + 2], acc[8 * s + 4 * g + 3]};
        *(lds_f32x4*)(lds + base + (16 * s + 4 * g) * 4) = v;
      }
  }
  bar();
  if (w < 8) {
    const unsigned row = tid >> 3, c8 = tid & 7;
    const unsigned a0 = row * 272 + c8 * 32;
    f32x4 u0 = *(lds_f32x4*)(lds + a0), u1 = *(lds_f32x4*)(lds + a0 + 16);
    f32x4 v0 = *(lds_f32x4*)(lds + 64 * 272 + a0), v1 = *(lds_f32x4*)(lds + 64 * 272 + a0 + 16);
    u0 += v0; u1 += v1;
    u32x4 o = {cvtpk(u0[0], u0[1]), cvtpk(u0[2], u0[3]), cvtpk(u1[0], u1[1]), cvtpk(u1[2], u1[3])};
    *(u32x4*)((char*)out + ((size_t)(b * SQ + rt * 64 + row) * DM + ct * 64 + c8 * 8) * 2) = o;
  }
}

extern "C" void kernel_launch(void* const* d_in, const int* in_sizes, int n_in, void* d_out, int out_size, void* d_ws, size_t ws_size,
                              hipStream_t stream) {
  const __hip_bfloat16* x = (const __hip_bfloat16*)d_in[0];
  const __hip_bfloat16* kc = (const __hip_bfloat16*)d_in[1];
  const __hip_bfloat16* vc = (const __hip_bfloat16*)d_in[2];
  const __hip_bfloat16* wq = (const __hip_bfloat16*)d_in[3];
  const __hip_bfloat16* wo = (const __hip_bfloat16*)d_in[4];
  __hip_bfloat16* obuf = (__hip_bfloat16*)d_ws;
  unsigned long long* diag = (unsigned long long*)((char*)d_ws + (64u << 20));
  attn_kernel<<<256, 768, 0, stream>>>(x, kc, vc, wq, obuf, diag);
  oproj_kernel<<<216, 768, 0, stream>>>(obuf, wo, (__hip_bfloat16*)d_out, diag);
}
```

```cpp
#include <hip/hip_runtime.h>
#include <hip/hip_bf16.h>
#include <type_traits>

typedef __bf16 bf16x8 __attribute__((ext_vector_type(8)));
typedef __bf16 bf16x4 __attribute__((ext_vector_type(4)));
typedef __bf16 bf16x2 __attribute__((ext_vector_type(2)));
typedef float f32x16 __attribute__((ext_vector_type(16)));
typedef float f32x4 __attribute__((ext_vector_type(4)));
typedef float f32x2 __attribute__((ext_vector_type(2)));
typedef unsigned u32x4 __attribute__((ext_vector_type(4)));
typedef unsigned u32x2 __attribute__((ext_vector_type(2)));
typedef __attribute__((address_space(3))) char lds_char;
typedef __attribute__((address_space(3))) bf16x4 lds_bf16x4;
typedef __attribute__((address_space(3))) bf16x8 lds_bf16x8;
typedef __attribute__((address_space(3))) f32x4 lds_f32x4;
typedef __attribute__((address_space(3))) f32x2 lds_f32x2;
typedef __attribute__((address_space(1))) unsigned long long gu64;

#define DI __device__ __forceinline__
#define MFMA32(a, b, c) __builtin_amdgcn_mfma_f32_32x32x16_bf16((a), (b), (c), 0, 0, 0)
#define MFMA16(a, b, c) __builtin_amdgcn_mfma_f32_16x16x32_bf16((a), (b), (c), 0, 0, 0)
#define SB() __builtin_amdgcn_sched_barrier(0)
template <int V> using IC = std::integral_constant<int, V>;

#define STAMPW(wv, i) do {} while (0)
#define STAMP(i) do {} while (0)
#define WREC(i, v) do {} while (0)
#define TNOW() 0ull

constexpr int NB = 8, SQ = 192, DM = 576, NH = 32, SK = 512, DH = 128;
constexpr float QSCALE = 0.08838834764831845f * 1.4426950408889634f;
constexpr float THR = 8.0f;

constexpr size_t WS_O = 0;
constexpr size_t WS_PART = 16u << 20;
constexpr size_t WS_FLAG = 32u << 20;
constexpr unsigned PART_BLK = 24 * 144 * 4;
constexpr unsigned O_BYTES = NB * NH * 6 * 8 * 1024, PART_BYTES = 8 * 8 * 4 * 4 * PART_BLK;

constexpr unsigned QS_BYTES = 40960, QS_W = 24576;
constexpr unsigned SPARE = 155648, LDS_TOTAL = 163840, ML_OFF = 98304;
DI constexpr unsigned kv_slot(int s) { return (unsigned)((s + 3) & 3) * QS_BYTES; }
DI unsigned qblk(unsigned blk) { return blk < 40 ? 2 * QS_BYTES + blk * 1024 : SPARE + (blk - 40) * 1024; }
constexpr unsigned P2_OSLOT = 12288, P2_WSLOT = 18432, P2_WBASE = 53248, IMG_STRIDE = 592, IMG_BYTES = 96 * IMG_STRIDE;
DI unsigned p2_oslot(unsigned i) { return (i & 3) * P2_OSLOT; }
DI unsigned p2_wslot(unsigned i) { return P2_WBASE + ((i + 3) % 6) * P2_WSLOT; }

DI unsigned rfl(unsigned x) { return __builtin_amdgcn_readfirstlane(x); }
DI unsigned cvtpk(float lo, float hi) { f32x2 v = {lo, hi}; bf16x2 b = __builtin_convertvector(v, bf16x2); return __builtin_bit_cast(unsigned, b); }
DI bf16x8 pack8(const f32x16& x, int s) {
  u32x4 p = {cvtpk(x[8 * s], x[8 * s + 1]), cvtpk(x[8 * s + 2], x[8 * s + 3]), cvtpk(x[8 * s + 4], x[8 * s + 5]), cvtpk(x[8 * s + 6], x[8 * s + 7])};
  return __builtin_bit_cast(bf16x8, p);
}
DI bf16x8 lds_rd128(lds_char* base, unsigned off) { return *(lds_bf16x8*)(base + off); }
DI bf16x8 lds_tr2(lds_char* base, unsigned off0, unsigned off1) {
  bf16x4 lo = __builtin_amdgcn_ds_read_tr16_b64_v4bf16((lds_bf16x4*)(base + off0));
  bf16x4 hi = __builtin_amdgcn_ds_read_tr16_b64_v4bf16((lds_bf16x4*)(base + off1));
  return __builtin_shufflevector(lo, hi, 0, 1, 2, 3, 4, 5, 6, 7);
}
template <int POL> DI void dma16(__amdgpu_buffer_rsrc_t r, unsigned voff, unsigned soff, unsigned m0v) {
  if constexpr (POL == 0) asm volatile("s_mov_b32 m0, %3\n\ts_nop 0\n\tbuffer_load_dwordx4 %0, %1, %2 offen lds" ::"v"(voff), "s"(r), "s"(soff), "s"(m0v) : "memory");
  else if constexpr (POL == 1) asm volatile("s_mov_b32 m0, %3\n\ts_nop 0\n\tbuffer_load_dwordx4 %0, %1, %2 offen nt lds" ::"v"(voff), "s"(r), "s"(soff), "s"(m0v) : "memory");
  else asm volatile("s_mov_b32 m0, %3\n\ts_nop 0\n\tbuffer_load_dwordx4 %0, %1, %2 offen sc1 lds" ::"v"(voff), "s"(r), "s"(soff), "s"(m0v) : "memory");
}
template <int N> DI void wait_vm() { asm volatile("s_waitcnt vmcnt(%0)" ::"i"(N) : "memory"); }
DI void bar() { SB(); asm volatile("s_waitcnt lgkmcnt(0)\n\ts_barrier" ::: "memory"); SB(); }
DI float pl32_max(float v) {
  auto r = __builtin_amdgcn_permlane32_swap(__float_as_uint(v), __float_as_uint(v), false, false);
  return fmaxf(__uint_as_float(r[0]), __uint_as_float(r[1]));
}
DI float pl32_sum(float v) {
  auto r = __builtin_amdgcn_permlane32_swap(__float_as_uint(v), __float_as_uint(v), false, false);
  return __uint_as_float(r[0]) + __uint_as_float(r[1]);
}
DI unsigned lane_id() { unsigned l; asm volatile("v_mbcnt_lo_u32_b32 %0, -1, 0\n\tv_mbcnt_hi_u32_b32 %0, -1, %0" : "=v"(l)); return l; }
DI void poll_flags(gu64* flags, unsigned idx0, unsigned stride, unsigned n, unsigned skip, unsigned long long tag, unsigned lane) {
  const bool mine = lane < n && lane != skip;
  gu64* p = flags + (size_t)(idx0 + (mine ? lane : 0) * stride) * 16;
  for (unsigned spins = 0; spins < 400000u; ++spins) {
    const unsigned long long v = __hip_atomic_load(p, __ATOMIC_RELAXED, __HIP_MEMORY_SCOPE_AGENT);
    if (__all(!mine || v == tag)) break;
    __builtin_amdgcn_s_sleep(1);
  }
}

#define POL_X 0
#define POL_WQ 0
#define O_ST_AUX 16
#define POL_O 2
#define POL_WO 0

__global__ __launch_bounds__(768, 3) void mha_kernel(const __hip_bfloat16* __restrict__ x, const __hip_bfloat16* __restrict__ kc,
                                                     const __hip_bfloat16* __restrict__ vc, const __hip_bfloat16* __restrict__ wq,
                                                     const __hip_bfloat16* __restrict__ wo, __hip_bfloat16* __restrict__ out,
                                                     char* __restrict__ ws, unsigned long long* diag) {
  STAMP(0);
  __shared__ __attribute__((aligned(1024))) char smem[LDS_TOTAL];
  lds_char* lds = (lds_char*)smem;
  const unsigned ldsb = (unsigned)(size_t)smem;
  const unsigned tid = threadIdx.x, w = rfl(tid >> 6);
  const unsigned xcd = blockIdx.x & 7, jj = blockIdx.x >> 3;
  const unsigned b = 4 * (xcd & 1) + (jj & 3), hd = 8 * (xcd >> 1) + (jj >> 2);
  gu64* flags = (gu64*)(ws + WS_FLAG);
  unsigned long long epoch;
  auto read_epoch = [&]() {
    asm volatile("s_load_dwordx2 %0, %1, 0x0\n\ts_waitcnt lgkmcnt(0)" : "=s"(epoch) : "s"((const char*)(ws + WS_FLAG) + (512 + b) * 128) : "memory");
  };

  bf16x8 qf[8];
  f32x16 oacc[4];
  float m, l0, l1;
  {
    const unsigned lane = tid & 63, r = lane & 31, h = lane >> 5;
    const unsigned t = w >> 1, j = w & 1;
    const unsigned g1 = (lane >> 4) & 1, qq = (lane & 15) >> 2, pp = lane & 3, sp = ((pp & 1) << 1) | (pp >> 1);
    const __amdgpu_buffer_rsrc_t rk = __builtin_amdgcn_make_buffer_rsrc((void*)kc, (short)0, NB * NH * SK * DH * 2, 0x00020000);
    const __amdgpu_buffer_rsrc_t rv = __builtin_amdgcn_make_buffer_rsrc((void*)vc, (short)0, NB * NH * SK * DH * 2, 0x00020000);
    const unsigned skv = (b * NH + hd) * (SK * DH * 2);
    const unsigned vk = (lane >> 4) * 256 + ((lane & 15) ^ (lane >> 4)) * 16;
    const unsigned vv = (lane >> 4) * 256 + ((lane & 15) ^ ((lane >> 4) << 2)) * 16;

    const unsigned koff = 0;
    if (w >= 4) {
      const unsigned lw = w - 4;
      const __amdgpu_buffer_rsrc_t rx = __builtin_amdgcn_make_buffer_rsrc((void*)x, (short)0, NB * SQ * DM * 2, 0x00020000);
      const __amdgpu_buffer_rsrc_t rw = __builtin_amdgcn_make_buffer_rsrc((void*)wq, (short)0, DM * NH * DH * 2, 0x00020000);
      const unsigned vx = (lane >> 3) * 1152 + (((lane & 7) ^ (lane >> 4) ^ ((lw & 1) << 2)) * 16);
      const unsigned sx = (b * SQ + 8 * lw) * 1152u;
      const unsigned vw = (lane >> 4) * 8192 + (((lane & 15) ^ ((lane >> 4) << 2)) * 16);
      const unsigned sw = 4 * lw * 8192u + hd * 256u;
      auto issue_q = [&](int i) {
        unsigned st = koff + i; st = st >= 9 ? st - 9 : st;
        const unsigned slot = ldsb + (unsigned)(i % 3) * QS_BYTES;
#pragma unroll
        for (int p = 0; p < 3; ++p) dma16<POL_X>(rx, vx, sx + (unsigned)(64 * p) * 1152u + st * 128u, slot + (lw + 8 * p) * 1024u);
#pragma unroll
        for (int p = 0; p < 2; ++p) dma16<POL_WQ>(rw, vw, sw + (st * 64 + 32 * p) * 8192u, slot + QS_W + (lw + 8 * p) * 1024u);
      };
      auto kv4 = [&](int s) {
        const unsigned dst = ldsb + kv_slot(s) + lw * 4096u;
        const unsigned so = skv + ((lw >> 1) & 1) * 65536u + (lw & 1) * 4096u + (unsigned)s * 8192u;
#pragma unroll
        for (int p = 0; p < 4; ++p) {
          if (lw < 4) dma16<1>(rk, vk ^ (p << 6), so + p * 1024u, dst + p * 1024u);
          else dma16<1>(rv, vv, so + p * 1024u, dst + p * 1024u);
        }
      };
      issue_q(0); issue_q(1);
      wait_vm<5>(); bar(); issue_q(2);
      wait_vm<5>(); bar(); issue_q(3);
      wait_vm<5>(); bar(); issue_q(4); kv4(0);
      wait_vm<9>(); bar(); issue_q(5);
      wait_vm<9>(); bar(); issue_q(6);
      wait_vm<5>(); bar(); issue_q(7);
      wait_vm<5>(); bar(); issue_q(8);
      wait_vm<5>(); bar(); kv4(1);
      wait_vm<4>(); bar(); kv4(2);
      read_epoch();
      bar();
      bar();
    } else {
      read_epoch();
      const unsigned qh = w >> 1, dh = w & 1;
      const unsigned xa0 = (96 * qh + r) * 128 + ((h ^ ((r >> 1) & 7)) << 4);
      const unsigned wa0 = QS_W + (8 * h + qq) * 256 + ((4 * ((2 * dh) ^ qq) + 2 * g1 + (sp >> 1)) << 4) + (sp & 1) * 8;
      f32x16 qacc[3][2];
#pragma unroll
      for (int a = 0; a < 3; ++a)
#pragma unroll
        for (int i = 0; i < 16; ++i) { qacc[a][0][i] = 0.f; qacc[a][1][i] = 0.f; }
      bf16x8 bA[3], aA[2], bB[3], aB[2];
      unsigned xb, wb;
      auto rd = [&](bf16x8 (&bb)[3], bf16x8 (&aa)[2], int s) {
        const unsigned xs = xb ^ (s << 5), wb1 = wb ^ 64;
        aa[0] = lds_tr2(lds, wb + s * 4096, wb + s * 4096 + 1024);
        bb[0] = lds_rd128(lds, xs); bb[1] = lds_rd128(lds, xs + 4096); bb[2] = lds_rd128(lds, xs + 8192);
        aa[1] = lds_tr2(lds, wb1 + s * 4096, wb1 + s * 4096 + 1024);
      };
      auto mm = [&](bf16x8 (&bb)[3], bf16x8 (&aa)[2]) {
        qacc[0][0] = MFMA32(aa[0], bb[0], qacc[0][0]);
        qacc[1][0] = MFMA32(aa[0], bb[1], qacc[1][0]);
        qacc[2][0] = MFMA32(aa[0], bb[2], qacc[2][0]);
        qacc[0][1] = MFMA32(aa[1], bb[0], qacc[0][1]);
        qacc[1][1] = MFMA32(aa[1], bb[1], qacc[1][1]);
        qacc[2][1] = MFMA32(aa[1], bb[2], qacc[2][1]);
      };
      bar();
      STAMP(1);
      xb = xa0; wb = wa0;
      asm volatile("" : "+v"(xb), "+v"(wb));
      rd(bA, aA, 0);
#pragma unroll 1
      for (int st = 0; st < 8; ++st) {
        SB(); rd(bB, aB, 1); SB(); mm(bA, aA);
        SB(); rd(bA, aA, 2); SB(); mm(bB, aB);
        SB(); rd(bB, aB, 3); SB(); mm(bA, aA);
        bar();
        const unsigned so = (unsigned)((st + 1) % 3) * QS_BYTES;
        xb = xa0 + so; wb = wa0 + so;
        asm volatile("" : "+v"(xb), "+v"(wb));
        rd(bA, aA, 0);
        SB(); mm(bB, aB);
      }
      SB(); rd(bB, aB, 1); SB(); mm(bA, aA);
      SB(); rd(bA, aA, 2); SB(); mm(bB, aB);
      SB(); rd(bB, aB, 3); SB(); mm(bA, aA);
      SB(); mm(bB, aB);
      bar();
      STAMP(4);
#pragma unroll
      for (int a = 0; a < 3; ++a)
#pragma unroll
        for (int d = 0; d < 2; ++d) {
#pragma unroll
          for (int i = 0; i < 16; ++i) qacc[a][d][i] *= QSCALE;
#pragma unroll
          for (int s2 = 0; s2 < 2; ++s2) {
            const unsigned blk = (3 * qh + a) * 8 + 4 * dh + 2 * d + s2;
            *(lds_bf16x8*)(lds + qblk(blk) + lane * 16) = pack8(qacc[a][d], s2);
          }
        }
      bar();
    }
#pragma unroll
    for (int i = 0; i < 8; ++i) qf[i] = lds_rd128(lds, qblk(t * 8 + i) + lane * 16);

    const unsigned grp = w >> 2;
#pragma unroll
    for (int d = 0; d < 4; ++d)
#pragma unroll
      for (int i = 0; i < 16; ++i) oacc[d][i] = 0.f;
    m = -__builtin_inff(); l0 = 0.f; l1 = 0.f;
    const unsigned ka0 = j * 8192 + r * 256 + ((h ^ (r & 15)) << 4);
    const unsigned va0 = (16384 + j * 8192 + (4 * h + qq) * 256 + ((4 * qq + 2 * g1 + (sp >> 1)) << 4) + (sp & 1) * 8) ^ (j << 7);
    const unsigned vkx = vk ^ ((w & 3) << 6);
    const unsigned sp0 = skv + ((w >> 3) & 1) * 65536u + (w & 7) * 1024u;
    const unsigned sp1 = skv + (((w + 12) >> 3) & 1) * 65536u + ((w + 12) & 7) * 1024u;
    const unsigned sp2 = skv + (((w + 24) >> 3) & 1) * 65536u + ((w + 24) & 7) * 1024u;

    bf16x8 kf[4];
    auto step = [&](auto G_, auto MODE_, int s) {
      constexpr int G = decltype(G_)::value, MODE = decltype(MODE_)::value;
      constexpr int NP = G == 2 ? 2 : 3;
      unsigned vb = va0 + kv_slot(s), kc0 = ka0 + kv_slot(s), kb = ka0 + kv_slot(s + 1);
      asm volatile("" : "+v"(kb), "+v"(vb), "+v"(kc0));
      __builtin_amdgcn_s_setprio(1);
      f32x16 sc;
      bf16x8 vf[4][2];
      {
        bf16x8 kg[4];
#pragma unroll
        for (int ks = 0; ks < 4; ++ks) kg[ks] = lds_rd128(lds, kc0 ^ ((ks + 4) << 5));
        SB();
#pragma unroll
        for (int i = 0; i < 16; ++i) sc[i] = 0.f;
#pragma unroll
        for (int ks = 0; ks < 4; ++ks) sc = MFMA32(kf[ks], qf[ks], sc);
        SB();
#pragma unroll
        for (int d = 0; d < 4; ++d) { const unsigned va = vb ^ (d << 6); vf[d][0] = lds_tr2(lds, va, va + 2048); }
        SB();
#pragma unroll
        for (int ks = 0; ks < 4; ++ks) sc = MFMA32(kg[ks], qf[ks + 4], sc);
        SB();
#pragma unroll
        for (int d = 0; d < 4; ++d) { const unsigned va = vb ^ (d << 6); vf[d][1] = lds_tr2(lds, va + 4096, va + 4096 + 2048); }
        SB();
      }
      __builtin_amdgcn_s_setprio(0);
      if constexpr (G == 1) { if constexpr (MODE <= 1) wait_vm<NP>(); else if constexpr (MODE == 2) wait_vm<0>(); }
      bar();
      {
        const float t0 = fmaxf(fmaxf(sc[0], sc[1]), sc[2]), t1 = fmaxf(fmaxf(sc[3], sc[4]), sc[5]), t2 = fmaxf(fmaxf(sc[6], sc[7]), sc[8]);
        const float t3 = fmaxf(fmaxf(sc[9], sc[10]), sc[11]), t4 = fmaxf(fmaxf(sc[12], sc[13]), sc[14]);
        float tm = fmaxf(fmaxf(fmaxf(t0, t1), t2), fmaxf(fmaxf(t3, t4), sc[15]));
        tm = pl32_max(tm);
        if (__any(tm > m + THR)) {
          const float mn = fmaxf(m, tm);
          const float al = __builtin_amdgcn_exp2f(m - mn);
          l0 *= al; l1 *= al; m = mn;
#pragma unroll
          for (int d = 0; d < 4; ++d)
#pragma unroll
            for (int i = 0; i < 16; ++i) oacc[d][i] *= al;
        }
      }
#pragma unroll
      for (int i = 0; i < 16; ++i) sc[i] -= m;
      SB();
#pragma unroll
      for (int i = 0; i < 16; ++i) sc[i] = __builtin_amdgcn_exp2f(sc[i]);
      bf16x8 p0 = pack8(sc, 0), p1 = pack8(sc, 1);
#pragma unroll
      for (int i = 0; i < 16; i += 2) { l0 += sc[i]; l1 += sc[i + 1]; }
      asm volatile("" : "+v"(p0), "+v"(p1), "+v"(l0), "+v"(l1));
      if constexpr (G == 0) { if constexpr (MODE <= 1) wait_vm<NP>(); else if constexpr (MODE == 2) wait_vm<0>(); }
      bar();
      __builtin_amdgcn_s_setprio(1);
#pragma unroll
      for (int d = 0; d < 4; ++d) oacc[d] = MFMA32(vf[d][0], p0, oacc[d]);
      SB();
      if constexpr (MODE != 3) {
#pragma unroll
        for (int ks = 0; ks < 4; ++ks) kf[ks] = lds_rd128(lds, kb ^ (ks << 5));
      }
      SB();
#pragma unroll
      for (int d = 0; d < 4; ++d) oacc[d] = MFMA32(vf[d][1], p1, oacc[d]);
      __builtin_amdgcn_s_setprio(0);
      SB();
      if constexpr (MODE == 0) {
        const unsigned dst = ldsb + kv_slot(s + 3) + w * 1024u, sofs = (unsigned)(s + 3) * 8192u;
        dma16<1>(rk, vkx, sp0 + sofs, dst);
        if constexpr (G == 0) dma16<1>(rk, vkx, sp1 + sofs, dst + 12288u); else dma16<1>(rv, vv, sp1 + sofs, dst + 12288u);
        if constexpr (G != 2) dma16<1>(rv, vv, sp2 + sofs, dst + 24576u);
      }
      if constexpr (G == 2) { if constexpr (MODE == 0) wait_vm<NP>(); else if constexpr (MODE == 1) wait_vm<0>(); }
      bar();
    };
    auto run = [&](auto G_) {
      constexpr int G = decltype(G_)::value;
      {
        unsigned kb0 = ka0 + kv_slot(0);
        asm volatile("" : "+v"(kb0));
#pragma unroll
        for (int ks = 0; ks < 4; ++ks) kf[ks] = lds_rd128(lds, kb0 ^ (ks << 5));
      }
      if constexpr (G >= 1) bar();
      if constexpr (G == 2) { wait_vm<4>(); bar(); }
#pragma unroll 1
      for (int s = 0; s < 5; ++s) step(G_, IC<0>{}, s);
      step(G_, IC<1>{}, 5);
      step(G_, IC<2>{}, 6);
      step(G_, IC<3>{}, 7);
      if constexpr (G == 0) bar();
      if constexpr (G <= 1) bar();
    };
    bar(); STAMP(5);
    if (grp == 0) run(IC<0>{});
    else if (grp == 1) run(IC<1>{});
    else run(IC<2>{});
  }
  STAMP(13);

  const unsigned lane = lane_id();
  const unsigned hg = hd >> 3, hl = hd & 7, rh = hl & 1, cq = hl >> 1;
  const unsigned soff2 = (4 * cq + 2 * (b & 3) + 8 * rh) & 15;
  const unsigned long long tag = epoch + 1;
  const unsigned wg = b * NH + hd;
  const __amdgpu_buffer_rsrc_t rwo = __builtin_amdgcn_make_buffer_rsrc((void*)wo, (short)0, NH * DH * DM * 2, 0x00020000);
  auto wo_voff = [&](int i) {
    const unsigned n = 64 * i + lane, p = n / 18, ch = n - 18 * p;
    const unsigned k = (p & 19) | ((p & 4) << 1) | ((p & 8) >> 1);
    return k * 1152 + ch * 16;
  };
  auto wo_issue = [&](const unsigned (&vo)[9], unsigned i) {
    const unsigned sg = (i + soff2) & 15, kh = w & 1;
    const unsigned so = ((8 * hg + 4 * kh + (sg >> 2)) * 128 + (sg & 3) * 32) * 1152u + cq * 288u;
    const unsigned dst = ldsb + p2_wslot(i) + kh * 9216u;
#pragma unroll
    for (int p = 0; p < 9; ++p) dma16<POL_WO>(rwo, vo[p], so, dst + p * 1024u);
  };
  unsigned wvo[9];
  if (w == 8 || w == 9) {
#pragma unroll
    for (int p = 0; p < 9; ++p) wvo[p] = wo_voff(p);
  }

  {
    const unsigned r = lane & 31, h = lane >> 5, t = w >> 1, j = w & 1;
    const float l = pl32_sum(l0 + l1);
    const unsigned xw = w * 8192 + lane * 16;
#pragma unroll
    for (int dd = 0; dd < 2; ++dd)
#pragma unroll
      for (int g = 0; g < 4; ++g) {
        f32x4 v = {oacc[2 + dd][4 * g], oacc[2 + dd][4 * g + 1], oacc[2 + dd][4 * g + 2], oacc[2 + dd][4 * g + 3]};
        *(lds_f32x4*)(lds + xw + (dd * 4 + g) * 1024) = v;
      }
    if (h == 0) { f32x2 ml = {m, l}; *(lds_f32x2*)(lds + ML_OFF + w * 256 + r * 8) = ml; }
    if (w == 8 || w == 9) wo_issue(wvo, 0);
    bar();
    f32x2 o = *(lds_f32x2*)(lds + ML_OFF + (w ^ 1) * 256 + r * 8);
    const float mn = fmaxf(m, o[0]);
    const float f = __builtin_amdgcn_exp2f(m - mn), fo = __builtin_amdgcn_exp2f(o[0] - mn);
    const float inv = 1.0f / (l * f + o[1] * fo);
    const float sa = f * inv, sb = fo * inv;
    const unsigned xr = (w ^ 1) * 8192 + lane * 16;
#pragma unroll
    for (int dd = 0; dd < 2; ++dd)
#pragma unroll
      for (int g = 0; g < 4; ++g) {
        f32x4 v = *(lds_f32x4*)(lds + xr + (dd * 4 + g) * 1024);
#pragma unroll
        for (int e = 0; e < 4; ++e) oacc[dd][4 * g + e] = oacc[dd][4 * g + e] * sa + v[e] * sb;
      }
    const __amdgpu_buffer_rsrc_t ro = __builtin_amdgcn_make_buffer_rsrc((void*)(ws + WS_O), (short)0, O_BYTES, 0x00020000);
    const unsigned so = ((wg * 6 + t) * 8 + 4 * j) * 1024u;
#pragma unroll
    for (int i = 0; i < 4; ++i)
      __builtin_amdgcn_raw_buffer_store_b128(__builtin_bit_cast(u32x4, pack8(oacc[i >> 1], i & 1)), ro, so + lane * 16 + i * 1024, 0, O_ST_AUX);
    if (w == 8 || w == 9) { wo_issue(wvo, 1); wait_vm<9>(); } else wait_vm<0>();
    bar();
    if (tid == 0) __hip_atomic_store(flags + (size_t)wg * 16, tag, __ATOMIC_RELAXED, __HIP_MEMORY_SCOPE_AGENT);
  }
  STAMP(14);

  f32x4 acc[3][5];
#pragma unroll
  for (int a = 0; a < 3; ++a)
#pragma unroll
    for (int n = 0; n < 5; ++n) acc[a][n] = f32x4{0.f, 0.f, 0.f, 0.f};
  const unsigned nh = (w >> 2) & 1, kh = w & 1, qh = (w >> 1) & 1;
  if (w >= 10) {
    poll_flags(flags, b * NH + 8 * hg, 1, 8, 64, tag, lane);
    STAMPW(10, 20);
    const __amdgpu_buffer_rsrc_t ro = __builtin_amdgcn_make_buffer_rsrc((void*)(ws + WS_O), (short)0, O_BYTES, 0x00020000);
    auto o_issue = [&](unsigned i) {
      const unsigned sg = (i + soff2) & 15;
      const unsigned head = 8 * hg + 4 * kh + (sg >> 2);
      const unsigned so = (((b * NH + head) * 6 + 3 * rh) * 8 + 2 * (sg & 3)) * 1024u;
      const unsigned dst = ldsb + p2_oslot(i) + kh * 6144u;
#pragma unroll
      for (int q = 0; q < 3; ++q) { dma16<POL_O>(ro, lane * 16, so + q * 8192u, dst + q * 2048u); dma16<POL_O>(ro, lane * 16, so + q * 8192u + 1024u, dst + q * 2048u + 1024u); }
    };
    o_issue(0); o_issue(1);
    STAMPW(10, 22);
#pragma unroll 1
    for (unsigned i = 0; i < 16; ++i) {
      if (i <= 14) wait_vm<6>(); else wait_vm<0>();
      if (i == 0) STAMPW(10, 23);
      bar();
      if (i + 2 < 16) o_issue(i + 2);
    }
  } else if (w >= 8) {
    wo_issue(wvo, 2);
    STAMPW(8, 24);
#pragma unroll 1
    for (unsigned i = 0; i < 16; ++i) {
      if (i <= 13) wait_vm<18>(); else if (i == 14) wait_vm<9>(); else wait_vm<0>();
      if (i == 0) STAMPW(8, 25);
      bar();
      if (i + 3 < 16) wo_issue(wvo, i + 3);
    }
  } else {
    const unsigned c = lane & 15, g = lane >> 4;
    unsigned ba[3];
#pragma unroll
    for (int a = 0; a < 3; ++a) {
      const unsigned qi = 3 * qh + a;
      ba[a] = kh * 6144 + ((qi >> 1) * 2 + (g >> 1)) * 1024 + (16 * (qi & 1) + c + 32 * (g & 1)) * 16;
    }
    const unsigned aa = kh * 9216 + ((g >> 1) * 16 + (g & 1) * 4 + (c >> 2)) * 288 + 5 * nh * 32 + (c & 3) * 8;
    bf16x8 bX[3], aX[5], bY[3], aY[5];
    auto rd = [&](auto NT_, bf16x8 (&bb)[3], bf16x8 (&av)[5], unsigned i) {
      constexpr int NT = decltype(NT_)::value;
      unsigned ob = p2_oslot(i), wb = aa + p2_wslot(i);
      asm volatile("" : "+v"(wb));
#pragma unroll
      for (int a = 0; a < 3; ++a) bb[a] = lds_rd128(lds, ba[a] + ob);
#pragma unroll
      for (int n = 0; n < NT; ++n) av[n] = lds_tr2(lds, wb + n * 32, wb + n * 32 + 2304);
    };
    auto mm = [&](auto NT_, bf16x8 (&bb)[3], bf16x8 (&av)[5]) {
      constexpr int NT = decltype(NT_)::value;
#pragma unroll
      for (int n = 0; n < NT; ++n)
#pragma unroll
        for (int a = 0; a < 3; ++a) acc[a][n] = MFMA16(av[n], bb[a], acc[a][n]);
    };
    unsigned long long t0 = 0;
    auto mix = [&](auto NT_) {
      constexpr int NT = decltype(NT_)::value;
#pragma unroll
      for (int k = 0; k < 3 + 2 * NT - (NT == 4 ? 0 : 0); ++k) {
        __builtin_amdgcn_sched_group_barrier(0x008, 1, 0);
        __builtin_amdgcn_sched_group_barrier(0x100, 1, 0);
      }
      __builtin_amdgcn_sched_group_barrier(0x008, 3 * NT, 0);
    };
    auto gemm = [&](auto NT_) {
      bar();
      STAMP(15);
      rd(NT_, bX, aX, 0);
#pragma unroll 1
      for (unsigned i = 1; i < 15; i += 2) {
        bar(); rd(NT_, bY, aY, i); mm(NT_, bX, aX); mix(NT_);
        bar(); rd(NT_, bX, aX, i + 1); mm(NT_, bY, aY); mix(NT_);
      }
      bar(); rd(NT_, bY, aY, 15); mm(NT_, bX, aX); mix(NT_);
      SB(); mm(NT_, bY, aY);
    };
    if (nh == 0) gemm(IC<5>{}); else gemm(IC<4>{});
    WREC(44, TNOW() - t0);
  }
  STAMP(16);
  bar();
  if (w < 8) {
    const unsigned c = lane & 15, g = lane >> 4;
    const unsigned base = kh * IMG_BYTES + (48 * qh + c) * IMG_STRIDE + (80 * nh + 4 * g) * 4;
#pragma unroll
    for (int a = 0; a < 3; ++a)
#pragma unroll
      for (int n = 0; n < 5; ++n)
        if (n < 4 || nh == 0) *(lds_f32x4*)(lds + base + a * 16 * IMG_STRIDE + n * 64) = acc[a][n];
  }
  bar();
  {
    const __amdgpu_buffer_rsrc_t rp = __builtin_amdgcn_make_buffer_rsrc((void*)(ws + WS_PART), (short)0, PART_BYTES, 0x00020000);
    const unsigned pbase = ((b * 8 + hl) * 4 + hg) * 4 * PART_BLK;
#pragma unroll 1
    for (unsigned n = tid; n < 72 * 36; n += 768) {
      const unsigned rr = n / 36, ch = n - rr * 36;
      unsigned dst = rr / 24; const unsigned row = rr - dst * 24;
      dst += dst >= hg ? 1 : 0;
      const unsigned a0 = (24 * dst + row) * IMG_STRIDE + ch * 16;
      const f32x4 v = *(lds_f32x4*)(lds + a0) + *(lds_f32x4*)(lds + IMG_BYTES + a0);
      __builtin_amdgcn_raw_buffer_store_b128(__builtin_bit_cast(u32x4, v), rp, dst * PART_BLK + row * 576 + ch * 16 + pbase, 0, 16);
    }
    wait_vm<0>();
    bar();
    if (tid == 0) __hip_atomic_store(flags + (size_t)(256 + wg) * 16, tag, __ATOMIC_RELAXED, __HIP_MEMORY_SCOPE_AGENT);
    STAMP(17);
    if (w == 0) poll_flags(flags, 256 + b * NH + hl, 8, 4, hg, tag, lane);
    bar();
    STAMP(18);
#pragma unroll 1
    for (unsigned n = tid; n < 24 * 36; n += 768) {
      const unsigned row = n / 36, ch = n - row * 36;
      const unsigned a0 = (24 * hg + row) * IMG_STRIDE + ch * 16;
      f32x4 v = *(lds_f32x4*)(lds + a0) + *(lds_f32x4*)(lds + IMG_BYTES + a0);
      u32x4 u[3];
#pragma unroll
      for (int k = 0; k < 3; ++k) {
        const unsigned src = k + (k >= (int)hg ? 1 : 0);
        u[k] = __builtin_amdgcn_raw_buffer_load_b128(rp, (((b * 8 + hl) * 4 + src) * 4 + hg) * PART_BLK + row * 576 + ch * 16, 0, 16);
      }
#pragma unroll
      for (int k = 0; k < 3; ++k) v += __builtin_bit_cast(f32x4, u[k]);
      u32x2 o = {cvtpk(v[0], v[1]), cvtpk(v[2], v[3])};
      *(u32x2*)((char*)out + ((size_t)(b * SQ + 96 * rh + 24 * hg + row) * DM + 144 * cq + 4 * ch) * 2) = o;
    }
    if (tid == 0 && hd == 0) __hip_atomic_store(flags + (size_t)(512 + b) * 16, tag, __ATOMIC_RELAXED, __HIP_MEMORY_SCOPE_AGENT);
  }
}

extern "C" void kernel_launch(void* const* d_in, const int* in_sizes, int n_in, void* d_out, int out_size, void* d_ws, size_t ws_size,
                              hipStream_t stream) {
  const __hip_bfloat16* x = (const __hip_bfloat16*)d_in[0];
  const __hip_bfloat16* kc = (const __hip_bfloat16*)d_in[1];
  const __hip_bfloat16* vc = (const __hip_bfloat16*)d_in[2];
  const __hip_bfloat16* wq = (const __hip_bfloat16*)d_in[3];
  const __hip_bfloat16* wo = (const __hip_bfloat16*)d_in[4];
  unsigned long long* diag = (unsigned long long*)((char*)d_ws + (64u << 20));
  mha_kernel<<<256, 768, 0, stream>>>(x, kc, vc, wq, wo, (__hip_bfloat16*)d_out, (char*)d_ws, diag);
}
```

```cpp
#include <hip/hip_runtime.h>
#include <hip/hip_bf16.h>
#include <type_traits>

typedef __bf16 bf16x8 __attribute__((ext_vector_type(8)));
typedef __bf16 bf16x4 __attribute__((ext_vector_type(4)));
typedef __bf16 bf16x2 __attribute__((ext_vector_type(2)));
typedef float f32x16 __attribute__((ext_vector_type(16)));
typedef float f32x4 __attribute__((ext_vector_type(4)));
typedef float f32x2 __attribute__((ext_vector_type(2)));
typedef unsigned u32x4 __attribute__((ext_vector_type(4)));
typedef unsigned u32x2 __attribute__((ext_vector_type(2)));
typedef __attribute__((address_space(3))) char lds_char;
typedef __attribute__((address_space(3))) bf16x4 lds_bf16x4;
typedef __attribute__((address_space(3))) bf16x8 lds_bf16x8;
typedef __attribute__((address_space(3))) f32x4 lds_f32x4;
typedef __attribute__((address_space(3))) f32x2 lds_f32x2;
typedef __attribute__((address_space(1))) unsigned long long gu64;

#define DI __device__ __forceinline__
#define MFMA32(a, b, c) __builtin_amdgcn_mfma_f32_32x32x16_bf16((a), (b), (c), 0, 0, 0)
#define MFMA16(a, b, c) __builtin_amdgcn_mfma_f32_16x16x32_bf16((a), (b), (c), 0, 0, 0)
#define SB() __builtin_amdgcn_sched_barrier(0)
template <int V> using IC = std::integral_constant<int, V>;

#define STAMPW(wv, i) do {} while (0)
#define STAMP(i) do {} while (0)
#define WREC(i, v) do {} while (0)
#define TNOW() 0ull

constexpr int NB = 8, SQ = 192, DM = 576, NH = 32, SK = 512, DH = 128;
constexpr float QSCALE = 0.08838834764831845f * 1.4426950408889634f;
constexpr float THR = 8.0f;

constexpr unsigned CODE_BYTES = 24 * 1024;
constexpr size_t WS_O = 0;
constexpr size_t WS_PART = 16u << 20;
constexpr size_t WS_FLAG = 32u << 20;
constexpr unsigned PART_BLK = 24 * 144 * 4;
constexpr unsigned O_BYTES = NB * NH * 6 * 8 * 1024, PART_BYTES = 8 * 8 * 4 * 4 * PART_BLK;

constexpr unsigned QS_BYTES = 40960, QS_W = 24576;
constexpr unsigned SPARE = 155648, LDS_TOTAL = 163840, ML_OFF = 98304;
DI constexpr unsigned kv_slot(int s) { return (unsigned)((s + 3) & 3) * QS_BYTES; }
DI unsigned qblk(unsigned blk) { return blk < 40 ? 2 * QS_BYTES + blk * 1024 : SPARE + (blk - 40) * 1024; }
constexpr unsigned P2_OSLOT = 12288, P2_WSLOT = 18432, P2_WBASE = 53248, IMG_STRIDE = 592, IMG_BYTES = 96 * IMG_STRIDE;
DI unsigned p2_oslot(unsigned i) { return (i & 3) * P2_OSLOT; }
DI unsigned p2_wslot(unsigned i) { return P2_WBASE + ((i + 3) % 6) * P2_WSLOT; }

DI unsigned rfl(unsigned x) { return __builtin_amdgcn_readfirstlane(x); }
DI unsigned cvtpk(float lo, float hi) { f32x2 v = {lo, hi}; bf16x2 b = __builtin_convertvector(v, bf16x2); return __builtin_bit_cast(unsigned, b); }
DI bf16x8 pack8(const f32x16& x, int s) {
  u32x4 p = {cvtpk(x[8 * s], x[8 * s + 1]), cvtpk(x[8 * s + 2], x[8 * s + 3]), cvtpk(x[8 * s + 4], x[8 * s + 5]), cvtpk(x[8 * s + 6], x[8 * s + 7])};
  return __builtin_bit_cast(bf16x8, p);
}
DI bf16x8 lds_rd128(lds_char* base, unsigned off) { return *(lds_bf16x8*)(base + off); }
DI bf16x8 lds_tr2(lds_char* base, unsigned off0, unsigned off1) {
  bf16x4 lo = __builtin_amdgcn_ds_read_tr16_b64_v4bf16((lds_bf16x4*)(base + off0));
  bf16x4 hi = __builtin_amdgcn_ds_read_tr16_b64_v4bf16((lds_bf16x4*)(base + off1));
  return __builtin_shufflevector(lo, hi, 0, 1, 2, 3, 4, 5, 6, 7);
}
template <int POL> DI void dma16(__amdgpu_buffer_rsrc_t r, unsigned voff, unsigned soff, unsigned m0v) {
  if constexpr (POL == 0) asm volatile("s_mov_b32 m0, %3\n\ts_nop 0\n\tbuffer_load_dwordx4 %0, %1, %2 offen lds" ::"v"(voff), "s"(r), "s"(soff), "s"(m0v) : "memory");
  else if constexpr (POL == 1) asm volatile("s_mov_b32 m0, %3\n\ts_nop 0\n\tbuffer_load_dwordx4 %0, %1, %2 offen nt lds" ::"v"(voff), "s"(r), "s"(soff), "s"(m0v) : "memory");
  else asm volatile("s_mov_b32 m0, %3\n\ts_nop 0\n\tbuffer_load_dwordx4 %0, %1, %2 offen sc1 lds" ::"v"(voff), "s"(r), "s"(soff), "s"(m0v) : "memory");
}
template <int N> DI void wait_vm() { asm volatile("s_waitcnt vmcnt(%0)" ::"i"(N) : "memory"); }
DI void bar() { SB(); asm volatile("s_waitcnt lgkmcnt(0)\n\ts_barrier" ::: "memory"); SB(); }
DI float pl32_max(float v) {
  auto r = __builtin_amdgcn_permlane32_swap(__float_as_uint(v), __float_as_uint(v), false, false);
  return fmaxf(__uint_as_float(r[0]), __uint_as_float(r[1]));
}
DI float pl32_sum(float v) {
  auto r = __builtin_amdgcn_permlane32_swap(__float_as_uint(v), __float_as_uint(v), false, false);
  return __uint_as_float(r[0]) + __uint_as_float(r[1]);
}
DI unsigned lane_id() { unsigned l; asm volatile("v_mbcnt_lo_u32_b32 %0, -1, 0\n\tv_mbcnt_hi_u32_b32 %0, -1, %0" : "=v"(l)); return l; }
DI void poll_flags(gu64* flags, unsigned idx0, unsigned stride, unsigned n, unsigned skip, unsigned long long tag, unsigned lane) {
  const bool mine = lane < n && lane != skip;
  gu64* p = flags + (size_t)(idx0 + (mine ? lane : 0) * stride) * 16;
  for (unsigned spins = 0; spins < 400000u; ++spins) {
    const unsigned long long v = __hip_atomic_load(p, __ATOMIC_RELAXED, __HIP_MEMORY_SCOPE_AGENT);
    if (__all(!mine || v == tag)) break;
    __builtin_amdgcn_s_sleep(1);
  }
}

#define POL_X 0
#define POL_WQ 0
#define O_ST_AUX 16
#define POL_O 2
#define POL_WO 0

__global__ __launch_bounds__(768, 3) void mha_kernel(const __hip_bfloat16* __restrict__ x, const __hip_bfloat16* __restrict__ kc,
                                                     const __hip_bfloat16* __restrict__ vc, const __hip_bfloat16* __restrict__ wq,
                                                     const __hip_bfloat16* __restrict__ wo, __hip_bfloat16* __restrict__ out,
                                                     char* __restrict__ ws, unsigned long long* diag) {
  STAMP(0);
  __shared__ __attribute__((aligned(1024))) char smem[LDS_TOTAL];
  lds_char* lds = (lds_char*)smem;
  const unsigned ldsb = (unsigned)(size_t)smem;
  const unsigned tid = threadIdx.x, w = rfl(tid >> 6);
  const unsigned xcd = blockIdx.x & 7, jj = blockIdx.x >> 3;
  const unsigned b = 4 * (xcd & 1) + (jj & 3), hd = 8 * (xcd >> 1) + (jj >> 2);
  gu64* flags = (gu64*)(ws + WS_FLAG);
  unsigned long long epoch;
  auto read_epoch = [&]() {
    asm volatile("s_load_dwordx2 %0, %1, 0x0\n\ts_waitcnt lgkmcnt(0)" : "=s"(epoch) : "s"((const char*)(ws + WS_FLAG) + (512 + b) * 128) : "memory");
  };

  bf16x8 qf[8];
  f32x16 oacc[4];
  float m, l0, l1;
  {
    const unsigned lane = tid & 63, r = lane & 31, h = lane >> 5;
    const unsigned t = w >> 1, j = w & 1;
    const unsigned g1 = (lane >> 4) & 1, qq = (lane & 15) >> 2, pp = lane & 3, sp = ((pp & 1) << 1) | (pp >> 1);
    const __amdgpu_buffer_rsrc_t rk = __builtin_amdgcn_make_buffer_rsrc((void*)kc, (short)0, NB * NH * SK * DH * 2, 0x00020000);
    const __amdgpu_buffer_rsrc_t rv = __builtin_amdgcn_make_buffer_rsrc((void*)vc, (short)0, NB * NH * SK * DH * 2, 0x00020000);
    const unsigned skv = (b * NH + hd) * (SK * DH * 2);
    const unsigned vk = (lane >> 4) * 256 + ((lane & 15) ^ (lane >> 4)) * 16;
    const unsigned vv = (lane >> 4) * 256 + ((lane & 15) ^ ((lane >> 4) << 2)) * 16;

    const unsigned koff = ((jj >> 2) + 2 * (jj & 3)) % 9;
    if (w >= 4) {
      const unsigned lw = w - 4;
      const __amdgpu_buffer_rsrc_t rx = __builtin_amdgcn_make_buffer_rsrc((void*)x, (short)0, NB * SQ * DM * 2, 0x00020000);
      const __amdgpu_buffer_rsrc_t rw = __builtin_amdgcn_make_buffer_rsrc((void*)wq, (short)0, DM * NH * DH * 2, 0x00020000);
      const unsigned vx = (lane >> 3) * 1152 + (((lane & 7) ^ (lane >> 4) ^ ((lw & 1) << 2)) * 16);
      const unsigned sx = (b * SQ + 8 * lw) * 1152u;
      const unsigned vw = (lane >> 4) * 8192 + (((lane & 15) ^ ((lane >> 4) << 2)) * 16);
      const unsigned sw = 4 * lw * 8192u + hd * 256u;
      auto issue_q = [&](int i) {
        unsigned st = koff + i; st = st >= 9 ? st - 9 : st;
        const unsigned slot = ldsb + (unsigned)(i % 3) * QS_BYTES;
#pragma unroll
        for (int p = 0; p < 3; ++p) dma16<POL_X>(rx, vx, sx + (unsigned)(64 * p) * 1152u + st * 128u, slot + (lw + 8 * p) * 1024u);
#pragma unroll
        for (int p = 0; p < 2; ++p) dma16<POL_WQ>(rw, vw, sw + (st * 64 + 32 * p) * 8192u, slot + QS_W + (lw + 8 * p) * 1024u);
      };
      auto kv4 = [&](int s) {
        const unsigned dst = ldsb + kv_slot(s) + lw * 4096u;
        const unsigned so = skv + ((lw >> 1) & 1) * 65536u + (lw & 1) * 4096u + (unsigned)s * 8192u;
#pragma unroll
        for (int p = 0; p < 4; ++p) {
          if (lw < 4) dma16<1>(rk, vk ^ (p << 6), so + p * 1024u, dst + p * 1024u);
          else dma16<1>(rv, vv, so + p * 1024u, dst + p * 1024u);
        }
      };
      issue_q(0); issue_q(1);
      wait_vm<5>(); bar(); issue_q(2);
      wait_vm<5>(); bar(); issue_q(3);
      wait_vm<5>(); bar(); issue_q(4); kv4(0);
      wait_vm<9>(); bar(); issue_q(5);
      wait_vm<9>(); bar(); issue_q(6);
      wait_vm<5>(); bar(); issue_q(7);
      wait_vm<5>(); bar(); issue_q(8);
      wait_vm<5>(); bar(); kv4(1);
      wait_vm<4>(); bar(); kv4(2);
      read_epoch();
      bar();
      bar();
    } else {
      read_epoch();
      if (w < 2) {
        unsigned long long pc;
        asm volatile("s_getpc_b64 %0" : "=s"(pc));
        const unsigned off = (jj * 2 + w) * 1024u;
        if (off + 1024u <= CODE_BYTES) {
          u32x4 t;
          const char* p = (const char*)(pc & ~127ull) + off + lane * 16;
          asm volatile("global_load_dwordx4 %0, %1, off\n\ts_waitcnt vmcnt(0)" : "=v"(t) : "v"(p) : "memory");
        }
      }
      const unsigned qh = w >> 1, dh = w & 1;
      const unsigned xa0 = (96 * qh + r) * 128 + ((h ^ ((r >> 1) & 7)) << 4);
      const unsigned wa0 = QS_W + (8 * h + qq) * 256 + ((4 * ((2 * dh) ^ qq) + 2 * g1 + (sp >> 1)) << 4) + (sp & 1) * 8;
      f32x16 qacc[3][2];
#pragma unroll
      for (int a = 0; a < 3; ++a)
#pragma unroll
        for (int i = 0; i < 16; ++i) { qacc[a][0][i] = 0.f; qacc[a][1][i] = 0.f; }
      bf16x8 bA[3], aA[2], bB[3], aB[2];
      unsigned xb, wb;
      auto rd = [&](bf16x8 (&bb)[3], bf16x8 (&aa)[2], int s) {
        const unsigned xs = xb ^ (s << 5), wb1 = wb ^ 64;
        aa[0] = lds_tr2(lds, wb + s * 4096, wb + s * 4096 + 1024);
        bb[0] = lds_rd128(lds, xs); bb[1] = lds_rd128(lds, xs + 4096); bb[2] = lds_rd128(lds, xs + 8192);
        aa[1] = lds_tr2(lds, wb1 + s * 4096, wb1 + s * 4096 + 1024);
      };
      auto mm = [&](bf16x8 (&bb)[3], bf16x8 (&aa)[2]) {
        qacc[0][0] = MFMA32(aa[0], bb[0], qacc[0][0]);
        qacc[1][0] = MFMA32(aa[0], bb[1], qacc[1][0]);
        qacc[2][0] = MFMA32(aa[0], bb[2], qacc[2][0]);
        qacc[0][1] = MFMA32(aa[1], bb[0], qacc[0][1]);
        qacc[1][1] = MFMA32(aa[1], bb[1], qacc[1][1]);
        qacc[2][1] = MFMA32(aa[1], bb[2], qacc[2][1]);
      };
      bar();
      STAMP(1);
      xb = xa0; wb = wa0;
      asm volatile("" : "+v"(xb), "+v"(wb));
      rd(bA, aA, 0);
#pragma unroll 1
      for (int st = 0; st < 8; ++st) {
        SB(); rd(bB, aB, 1); SB(); mm(bA, aA);
        SB(); rd(bA, aA, 2); SB(); mm(bB, aB);
        SB(); rd(bB, aB, 3); SB(); mm(bA, aA);
        bar();
        const unsigned so = (unsigned)((st + 1) % 3) * QS_BYTES;
        xb = xa0 + so; wb = wa0 + so;
        asm volatile("" : "+v"(xb), "+v"(wb));
        rd(bA, aA, 0);
        SB(); mm(bB, aB);
      }
      SB(); rd(bB, aB, 1); SB(); mm(bA, aA);
      SB(); rd(bA, aA, 2); SB(); mm(bB, aB);
      SB(); rd(bB, aB, 3); SB(); mm(bA, aA);
      SB(); mm(bB, aB);
      bar();
      STAMP(4);
#pragma unroll
      for (int a = 0; a < 3; ++a)
#pragma unroll
        for (int d = 0; d < 2; ++d) {
#pragma unroll
          for (int i = 0; i < 16; ++i) qacc[a][d][i] *= QSCALE;
#pragma unroll
          for (int s2 = 0; s2 < 2; ++s2) {
            const unsigned blk = (3 * qh + a) * 8 + 4 * dh + 2 * d + s2;
            *(lds_bf16x8*)(lds + qblk(blk) + lane * 16) = pack8(qacc[a][d], s2);
          }
        }
      bar();
    }
#pragma unroll
    for (int i = 0; i < 8; ++i) qf[i] = lds_rd128(lds, qblk(t * 8 + i) + lane * 16);

    const unsigned grp = w >> 2;
#pragma unroll
    for (int d = 0; d < 4; ++d)
#pragma unroll
      for (int i = 0; i < 16; ++i) oacc[d][i] = 0.f;
    m = -__builtin_inff(); l0 = 0.f; l1 = 0.f;
    const unsigned ka0 = j * 8192 + r * 256 + ((h ^ (r & 15)) << 4);
    const unsigned va0 = (16384 + j * 8192 + (4 * h + qq) * 256 + ((4 * qq + 2 * g1 + (sp >> 1)) << 4) + (sp & 1) * 8) ^ (j << 7);
    const unsigned vkx = vk ^ ((w & 3) << 6);
    const unsigned sp0 = skv + ((w >> 3) & 1) * 65536u + (w & 7) * 1024u;
    const unsigned sp1 = skv + (((w + 12) >> 3) & 1) * 65536u + ((w + 12) & 7) * 1024u;
    const unsigned sp2 = skv + (((w + 24) >> 3) & 1) * 65536u + ((w + 24) & 7) * 1024u;

    bf16x8 kf[4];
    auto step = [&](auto G_, int s) {
      constexpr int G = decltype(G_)::value;
      constexpr int NP = G == 2 ? 2 : 3;
      unsigned vb = va0 + kv_slot(s), kc0 = ka0 + kv_slot(s), kb = ka0 + kv_slot(s + 1);
      asm volatile("" : "+v"(kb), "+v"(vb), "+v"(kc0));
      __builtin_amdgcn_s_setprio(1);
      f32x16 sc;
      bf16x8 vf[4][2];
      {
        bf16x8 kg[4];
#pragma unroll
        for (int ks = 0; ks < 4; ++ks) kg[ks] = lds_rd128(lds, kc0 ^ ((ks + 4) << 5));
        SB();
#pragma unroll
        for (int i = 0; i < 16; ++i) sc[i] = 0.f;
#pragma unroll
        for (int ks = 0; ks < 4; ++ks) sc = MFMA32(kf[ks], qf[ks], sc);
        SB();
#pragma unroll
        for (int d = 0; d < 4; ++d) { const unsigned va = vb ^ (d << 6); vf[d][0] = lds_tr2(lds, va, va + 2048); }
        SB();
#pragma unroll
        for (int ks = 0; ks < 4; ++ks) sc = MFMA32(kg[ks], qf[ks + 4], sc);
        SB();
#pragma unroll
        for (int d = 0; d < 4; ++d) { const unsigned va = vb ^ (d << 6); vf[d][1] = lds_tr2(lds, va + 4096, va + 4096 + 2048); }
        SB();
      }
      __builtin_amdgcn_s_setprio(0);
      if constexpr (G == 1) { if (s <= 5) wait_vm<NP>(); else wait_vm<0>(); }
      bar();
      {
        const float t0 = fmaxf(fmaxf(sc[0], sc[1]), sc[2]), t1 = fmaxf(fmaxf(sc[3], sc[4]), sc[5]), t2 = fmaxf(fmaxf(sc[6], sc[7]), sc[8]);
        const float t3 = fmaxf(fmaxf(sc[9], sc[10]), sc[11]), t4 = fmaxf(fmaxf(sc[12], sc[13]), sc[14]);
        float tm = fmaxf(fmaxf(fmaxf(t0, t1), t2), fmaxf(fmaxf(t3, t4), sc[15]));
        tm = pl32_max(tm);
        if (__any(tm > m + THR)) {
          const float mn = fmaxf(m, tm);
          const float al = __builtin_amdgcn_exp2f(m - mn);
          l0 *= al; l1 *= al; m = mn;
#pragma unroll
          for (int d = 0; d < 4; ++d)
#pragma unroll
            for (int i = 0; i < 16; ++i) oacc[d][i] *= al;
        }
      }
#pragma unroll
      for (int i = 0; i < 16; ++i) sc[i] -= m;
      SB();
#pragma unroll
      for (int i = 0; i < 16; ++i) sc[i] = __builtin_amdgcn_exp2f(sc[i]);
      bf16x8 p0 = pack8(sc, 0), p1 = pack8(sc, 1);
#pragma unroll
      for (int i = 0; i < 16; i += 2) { l0 += sc[i]; l1 += sc[i + 1]; }
      asm volatile("" : "+v"(p0), "+v"(p1), "+v"(l0), "+v"(l1));
      if constexpr (G == 0) { if (s <= 5) wait_vm<NP>(); else wait_vm<0>(); }
      bar();
      __builtin_amdgcn_s_setprio(1);
#pragma unroll
      for (int d = 0; d < 4; ++d) oacc[d] = MFMA32(vf[d][0], p0, oacc[d]);
      SB();
#pragma unroll
      for (int ks = 0; ks < 4; ++ks) kf[ks] = lds_rd128(lds, kb ^ (ks << 5));
      SB();
#pragma unroll
      for (int d = 0; d < 4; ++d) oacc[d] = MFMA32(vf[d][1], p1, oacc[d]);
      __builtin_amdgcn_s_setprio(0);
      SB();
      if (s <= 4) {
        const unsigned dst = ldsb + kv_slot(s + 3) + w * 1024u, sofs = (unsigned)(s + 3) * 8192u;
        dma16<1>(rk, vkx, sp0 + sofs, dst);
        if constexpr (G == 0) dma16<1>(rk, vkx, sp1 + sofs, dst + 12288u); else dma16<1>(rv, vv, sp1 + sofs, dst + 12288u);
        if constexpr (G != 2) dma16<1>(rv, vv, sp2 + sofs, dst + 24576u);
      }
      if constexpr (G == 2) { if (s <= 4) wait_vm<NP>(); else wait_vm<0>(); }
      bar();
    };
    auto run = [&](auto G_) {
      constexpr int G = decltype(G_)::value;
      {
        unsigned kb0 = ka0 + kv_slot(0);
        asm volatile("" : "+v"(kb0));
#pragma unroll
        for (int ks = 0; ks < 4; ++ks) kf[ks] = lds_rd128(lds, kb0 ^ (ks << 5));
      }
      if constexpr (G >= 1) bar();
      if constexpr (G == 2) { wait_vm<4>(); bar(); }
#pragma unroll 1
      for (int s = 0; s < 8; ++s) step(G_, s);
      if constexpr (G == 0) bar();
      if constexpr (G <= 1) bar();
    };
    bar(); STAMP(5);
    if (grp == 0) run(IC<0>{});
    else if (grp == 1) run(IC<1>{});
    else run(IC<2>{});
  }
  STAMP(13);

  const unsigned lane = lane_id();
  const unsigned hg = hd >> 3, hl = hd & 7, rh = hl & 1, cq = hl >> 1;
  const unsigned soff2 = (4 * cq + 2 * (b & 3) + 8 * rh) & 15;
  const unsigned long long tag = epoch + 1;
  const unsigned wg = b * NH + hd;
  const __amdgpu_buffer_rsrc_t rwo = __builtin_amdgcn_make_buffer_rsrc((void*)wo, (short)0, NH * DH * DM * 2, 0x00020000);
  auto wo_voff = [&](int i) {
    const unsigned n = 64 * i + lane, p = n / 18, ch = n - 18 * p;
    const unsigned k = (p & 19) | ((p & 4) << 1) | ((p & 8) >> 1);
    return k * 1152 + ch * 16;
  };
  auto wo_issue = [&](const unsigned (&vo)[9], unsigned i) {
    const unsigned sg = (i + soff2) & 15, kh = w & 1;
    const unsigned so = ((8 * hg + 4 * kh + (sg >> 2)) * 128 + (sg & 3) * 32) * 1152u + cq * 288u;
    const unsigned dst = ldsb + p2_wslot(i) + kh * 9216u;
#pragma unroll
    for (int p = 0; p < 9; ++p) dma16<POL_WO>(rwo, vo[p], so, dst + p * 1024u);
  };
  unsigned wvo[9];
  if (w == 8 || w == 9) {
#pragma unroll
    for (int p = 0; p < 9; ++p) wvo[p] = wo_voff(p);
  }

  {
    const unsigned r = lane & 31, h = lane >> 5, t = w >> 1, j = w & 1;
    const float l = pl32_sum(l0 + l1);
    const unsigned xw = w * 8192 + lane * 16;
#pragma unroll
    for (int dd = 0; dd < 2; ++dd)
#pragma unroll
      for (int g = 0; g < 4; ++g) {
        f32x4 v = {oacc[2 + dd][4 * g], oacc[2 + dd][4 * g + 1], oacc[2 + dd][4 * g + 2], oacc[2 + dd][4 * g + 3]};
        *(lds_f32x4*)(lds + xw + (dd * 4 + g) * 1024) = v;
      }
    if (h == 0) { f32x2 ml = {m, l}; *(lds_f32x2*)(lds + ML_OFF + w * 256 + r * 8) = ml; }
    if (w == 8 || w == 9) wo_issue(wvo, 0);
    bar();
    f32x2 o = *(lds_f32x2*)(lds + ML_OFF + (w ^ 1) * 256 + r * 8);
    const float mn = fmaxf(m, o[0]);
    const float f = __builtin_amdgcn_exp2f(m - mn), fo = __builtin_amdgcn_exp2f(o[0] - mn);
    const float inv = 1.0f / (l * f + o[1] * fo);
    const float sa = f * inv, sb = fo * inv;
    const unsigned xr = (w ^ 1) * 8192 + lane * 16;
#pragma unroll
    for (int dd = 0; dd < 2; ++dd)
#pragma unroll
      for (int g = 0; g < 4; ++g) {
        f32x4 v = *(lds_f32x4*)(lds + xr + (dd * 4 + g) * 1024);
#pragma unroll
        for (int e = 0; e < 4; ++e) oacc[dd][4 * g + e] = oacc[dd][4 * g + e] * sa + v[e] * sb;
      }
    const __amdgpu_buffer_rsrc_t ro = __builtin_amdgcn_make_buffer_rsrc((void*)(ws + WS_O), (short)0, O_BYTES, 0x00020000);
    const unsigned so = ((wg * 6 + t) * 8 + 4 * j) * 1024u;
#pragma unroll
    for (int i = 0; i < 4; ++i)
      __builtin_amdgcn_raw_buffer_store_b128(__builtin_bit_cast(u32x4, pack8(oacc[i >> 1], i & 1)), ro, so + lane * 16 + i * 1024, 0, O_ST_AUX);
    if (w == 8 || w == 9) { wo_issue(wvo, 1); wait_vm<9>(); } else wait_vm<0>();
    bar();
    if (tid == 0) __hip_atomic_store(flags + (size_t)wg * 16, tag, __ATOMIC_RELAXED, __HIP_MEMORY_SCOPE_AGENT);
  }
  STAMP(14);

  f32x4 acc[3][5];
#pragma unroll
  for (int a = 0; a < 3; ++a)
#pragma unroll
    for (int n = 0; n < 5; ++n) acc[a][n] = f32x4{0.f, 0.f, 0.f, 0.f};
  const unsigned nh = (w >> 2) & 1, kh = w & 1, qh = (w >> 1) & 1;
  if (w >= 10) {
    poll_flags(flags, b * NH + 8 * hg, 1, 8, 64, tag, lane);
    STAMPW(10, 20);
    const __amdgpu_buffer_rsrc_t ro = __builtin_amdgcn_make_buffer_rsrc((void*)(ws + WS_O), (short)0, O_BYTES, 0x00020000);
    auto o_issue = [&](unsigned i) {
      const unsigned sg = (i + soff2) & 15;
      const unsigned head = 8 * hg + 4 * kh + (sg >> 2);
      const unsigned so = (((b * NH + head) * 6 + 3 * rh) * 8 + 2 * (sg & 3)) * 1024u;
      const unsigned dst = ldsb + p2_oslot(i) + kh * 6144u;
#pragma unroll
      for (int q = 0; q < 3; ++q) { dma16<POL_O>(ro, lane * 16, so + q * 8192u, dst + q * 2048u); dma16<POL_O>(ro, lane * 16, so + q * 8192u + 1024u, dst + q * 2048u + 1024u); }
    };
    o_issue(0); o_issue(1);
    STAMPW(10, 22);
#pragma unroll 1
    for (unsigned i = 0; i < 16; ++i) {
      if (i <= 14) wait_vm<6>(); else wait_vm<0>();
      if (i == 0) STAMPW(10, 23);
      bar();
      if (i + 2 < 16) o_issue(i + 2);
    }
  } else if (w >= 8) {
    wo_issue(wvo, 2);
    STAMPW(8, 24);
#pragma unroll 1
    for (unsigned i = 0; i < 16; ++i) {
      if (i <= 13) wait_vm<18>(); else if (i == 14) wait_vm<9>(); else wait_vm<0>();
      if (i == 0) STAMPW(8, 25);
      bar();
      if (i + 3 < 16) wo_issue(wvo, i + 3);
    }
  } else {
    const unsigned c = lane & 15, g = lane >> 4;
    unsigned ba[3];
#pragma unroll
    for (int a = 0; a < 3; ++a) {
      const unsigned qi = 3 * qh + a;
      ba[a] = kh * 6144 + ((qi >> 1) * 2 + (g >> 1)) * 1024 + (16 * (qi & 1) + c + 32 * (g & 1)) * 16;
    }
    const unsigned aa = kh * 9216 + ((g >> 1) * 16 + (g & 1) * 4 + (c >> 2)) * 288 + 5 * nh * 32 + (c & 3) * 8;
    bf16x8 bX[3], aX[5], bY[3], aY[5];
    auto rd = [&](auto NT_, bf16x8 (&bb)[3], bf16x8 (&av)[5], unsigned i) {
      constexpr int NT = decltype(NT_)::value;
      unsigned ob = p2_oslot(i), wb = aa + p2_wslot(i);
      asm volatile("" : "+v"(wb));
#pragma unroll
      for (int a = 0; a < 3; ++a) bb[a] = lds_rd128(lds, ba[a] + ob);
#pragma unroll
      for (int n = 0; n < NT; ++n) av[n] = lds_tr2(lds, wb + n * 32, wb + n * 32 + 2304);
    };
    auto mm = [&](auto NT_, bf16x8 (&bb)[3], bf16x8 (&av)[5]) {
      constexpr int NT = decltype(NT_)::value;
#pragma unroll
      for (int n = 0; n < NT; ++n)
#pragma unroll
        for (int a = 0; a < 3; ++a) acc[a][n] = MFMA16(av[n], bb[a], acc[a][n]);
    };
    unsigned long long t0 = 0;
    auto mix = [&](auto NT_) {
      constexpr int NT = decltype(NT_)::value;
#pragma unroll
      for (int k = 0; k < 3 + 2 * NT - (NT == 4 ? 0 : 0); ++k) {
        __builtin_amdgcn_sched_group_barrier(0x008, 1, 0);
        __builtin_amdgcn_sched_group_barrier(0x100, 1, 0);
      }
      __builtin_amdgcn_sched_group_barrier(0x008, 3 * NT, 0);
    };
    auto gemm = [&](auto NT_) {
      bar();
      STAMP(15);
      rd(NT_, bX, aX, 0);
#pragma unroll 1
      for (unsigned i = 1; i < 15; i += 2) {
        bar(); rd(NT_, bY, aY, i); mm(NT_, bX, aX); mix(NT_);
        bar(); rd(NT_, bX, aX, i + 1); mm(NT_, bY, aY); mix(NT_);
      }
      bar(); rd(NT_, bY, aY, 15); mm(NT_, bX, aX); mix(NT_);
      SB(); mm(NT_, bY, aY);
    };
    if (nh == 0) gemm(IC<5>{}); else gemm(IC<4>{});
    WREC(44, TNOW() - t0);
  }
  STAMP(16);
  bar();
  if (w < 8) {
    const unsigned c = lane & 15, g = lane >> 4;
    const unsigned base = kh * IMG_BYTES + (48 * qh + c) * IMG_STRIDE + (80 * nh + 4 * g) * 4;
#pragma unroll
    for (int a = 0; a < 3; ++a)
#pragma unroll
      for (int n = 0; n < 5; ++n)
        if (n < 4 || nh == 0) *(lds_f32x4*)(lds + base + a * 16 * IMG_STRIDE + n * 64) = acc[a][n];
  }
  bar();
  {
    const __amdgpu_buffer_rsrc_t rp = __builtin_amdgcn_make_buffer_rsrc((void*)(ws + WS_PART), (short)0, PART_BYTES, 0x00020000);
    const unsigned pbase = ((b * 8 + hl) * 4 + hg) * 4 * PART_BLK;
#pragma unroll 1
    for (unsigned n = tid; n < 72 * 36; n += 768) {
      const unsigned rr = n / 36, ch = n - rr * 36;
      unsigned dst = rr / 24; const unsigned row = rr - dst * 24;
      dst += dst >= hg ? 1 : 0;
      const unsigned a0 = (24 * dst + row) * IMG_STRIDE + ch * 16;
      const f32x4 v = *(lds_f32x4*)(lds + a0) + *(lds_f32x4*)(lds + IMG_BYTES + a0);
      __builtin_amdgcn_raw_buffer_store_b128(__builtin_bit_cast(u32x4, v), rp, dst * PART_BLK + row * 576 + ch * 16 + pbase, 0, 16);
    }
    wait_vm<0>();
    bar();
    if (tid == 0) __hip_atomic_store(flags + (size_t)(256 + wg) * 16, tag, __ATOMIC_RELAXED, __HIP_MEMORY_SCOPE_AGENT);
    STAMP(17);
    if (w == 0) poll_flags(flags, 256 + b * NH + hl, 8, 4, hg, tag, lane);
    bar();
    STAMP(18);
#pragma unroll 1
    for (unsigned n = tid; n < 24 * 36; n += 768) {
      const unsigned row = n / 36, ch = n - row * 36;
      const unsigned a0 = (24 * hg + row) * IMG_STRIDE + ch * 16;
      f32x4 v = *(lds_f32x4*)(lds + a0) + *(lds_f32x4*)(lds + IMG_BYTES + a0);
      u32x4 u[3];
#pragma unroll
      for (int k = 0; k < 3; ++k) {
        const unsigned src = k + (k >= (int)hg ? 1 : 0);
        u[k] = __builtin_amdgcn_raw_buffer_load_b128(rp, (((b * 8 + hl) * 4 + src) * 4 + hg) * PART_BLK + row * 576 + ch * 16, 0, 16);
      }
#pragma unroll
      for (int k = 0; k < 3; ++k) v += __builtin_bit_cast(f32x4, u[k]);
      u32x2 o = {cvtpk(v[0], v[1]), cvtpk(v[2], v[3])};
      *(u32x2*)((char*)out + ((size_t)(b * SQ + 96 * rh + 24 * hg + row) * DM + 144 * cq + 4 * ch) * 2) = o;
    }
    if (tid == 0 && hd == 0) __hip_atomic_store(flags + (size_t)(512 + b) * 16, tag, __ATOMIC_RELAXED, __HIP_MEMORY_SCOPE_AGENT);
  }
}

extern "C" void kernel_launch(void* const* d_in, const int* in_sizes, int n_in, void* d_out, int out_size, void* d_ws, size_t ws_size,
                              hipStream_t stream) {
  const __hip_bfloat16* x = (const __hip_bfloat16*)d_in[0];
  const __hip_bfloat16* kc = (const __hip_bfloat16*)d_in[1];
  const __hip_bfloat16* vc = (const __hip_bfloat16*)d_in[2];
  const __hip_bfloat16* wq = (const __hip_bfloat16*)d_in[3];
  const __hip_bfloat16* wo = (const __hip_bfloat16*)d_in[4];
  unsigned long long* diag = (unsigned long long*)((char*)d_ws + (64u << 20));
  mha_kernel<<<256, 768, 0, stream>>>(x, kc, vc, wq, wo, (__hip_bfloat16*)d_out, (char*)d_ws, diag);
}
```

```cpp
#define SELFTIME 2
#include <hip/hip_runtime.h>
#include <hip/hip_bf16.h>
#include <type_traits>

typedef __bf16 bf16x8 __attribute__((ext_vector_type(8)));
typedef __bf16 bf16x4 __attribute__((ext_vector_type(4)));
typedef __bf16 bf16x2 __attribute__((ext_vector_type(2)));
typedef float f32x16 __attribute__((ext_vector_type(16)));
typedef float f32x4 __attribute__((ext_vector_type(4)));
typedef float f32x2 __attribute__((ext_vector_type(2)));
typedef unsigned u32x4 __attribute__((ext_vector_type(4)));
typedef unsigned u32x2 __attribute__((ext_vector_type(2)));
typedef __attribute__((address_space(3))) char lds_char;
typedef __attribute__((address_space(3))) bf16x4 lds_bf16x4;
typedef __attribute__((address_space(3))) bf16x8 lds_bf16x8;
typedef __attribute__((address_space(3))) f32x4 lds_f32x4;
typedef __attribute__((address_space(3))) f32x2 lds_f32x2;
typedef __attribute__((address_space(1))) unsigned long long gu64;

#define DI __device__ __forceinline__
#define MFMA32(a, b, c) __builtin_amdgcn_mfma_f32_32x32x16_bf16((a), (b), (c), 0, 0, 0)
#define MFMA16(a, b, c) __builtin_amdgcn_mfma_f32_16x16x32_bf16((a), (b), (c), 0, 0, 0)
#define SB() __builtin_amdgcn_sched_barrier(0)
template <int V> using IC = std::integral_constant<int, V>;

#define STAMPW(wv, i) do {} while (0)
#define STAMP(i) do {} while (0)
#define WREC(i, v) do {} while (0)
#define TNOW() 0ull

constexpr int NB = 8, SQ = 192, DM = 576, NH = 32, SK = 512, DH = 128;
constexpr float QSCALE = 0.08838834764831845f * 1.4426950408889634f;
constexpr float THR = 8.0f;

constexpr unsigned CODE_BYTES = 24 * 1024;
constexpr size_t WS_O = 0;
constexpr size_t WS_PART = 16u << 20;
constexpr size_t WS_FLAG = 32u << 20;
constexpr unsigned PART_BLK = 24 * 144 * 4;
constexpr unsigned O_BYTES = NB * NH * 6 * 8 * 1024, PART_BYTES = 8 * 8 * 4 * 4 * PART_BLK;

constexpr unsigned QS_BYTES = 40960, QS_W = 24576;
constexpr unsigned SPARE = 155648, LDS_TOTAL = 163840, ML_OFF = 98304;
DI constexpr unsigned kv_slot(int s) { return (unsigned)((s + 3) & 3) * QS_BYTES; }
DI unsigned qblk(unsigned blk) { return blk < 40 ? 2 * QS_BYTES + blk * 1024 : SPARE + (blk - 40) * 1024; }
constexpr unsigned P2_OSLOT = 12288, P2_WSLOT = 18432, P2_WBASE = 53248, IMG_STRIDE = 592, IMG_BYTES = 96 * IMG_STRIDE;
DI unsigned p2_oslot(unsigned i) { return (i & 3) * P2_OSLOT; }
DI unsigned p2_wslot(unsigned i) { return P2_WBASE + ((i + 3) % 6) * P2_WSLOT; }

DI unsigned rfl(unsigned x) { return __builtin_amdgcn_readfirstlane(x); }
DI unsigned cvtpk(float lo, float hi) { f32x2 v = {lo, hi}; bf16x2 b = __builtin_convertvector(v, bf16x2); return __builtin_bit_cast(unsigned, b); }
DI bf16x8 pack8(const f32x16& x, int s) {
  u32x4 p = {cvtpk(x[8 * s], x[8 * s + 1]), cvtpk(x[8 * s + 2], x[8 * s + 3]), cvtpk(x[8 * s + 4], x[8 * s + 5]), cvtpk(x[8 * s + 6], x[8 * s + 7])};
  return __builtin_bit_cast(bf16x8, p);
}
DI bf16x8 lds_rd128(lds_char* base, unsigned off) { return *(lds_bf16x8*)(base + off); }
DI bf16x8 lds_tr2(lds_char* base, unsigned off0, unsigned off1) {
  bf16x4 lo = __builtin_amdgcn_ds_read_tr16_b64_v4bf16((lds_bf16x4*)(base + off0));
  bf16x4 hi = __builtin_amdgcn_ds_read_tr16_b64_v4bf16((lds_bf16x4*)(base + off1));
  return __builtin_shufflevector(lo, hi, 0, 1, 2, 3, 4, 5, 6, 7);
}
template <int POL> DI void dma16(__amdgpu_buffer_rsrc_t r, unsigned voff, unsigned soff, unsigned m0v) {
  if constexpr (POL == 0) asm volatile("s_mov_b32 m0, %3\n\ts_nop 0\n\tbuffer_load_dwordx4 %0, %1, %2 offen lds" ::"v"(voff), "s"(r), "s"(soff), "s"(m0v) : "memory");
  else if constexpr (POL == 1) asm volatile("s_mov_b32 m0, %3\n\ts_nop 0\n\tbuffer_load_dwordx4 %0, %1, %2 offen nt lds" ::"v"(voff), "s"(r), "s"(soff), "s"(m0v) : "memory");
  else asm volatile("s_mov_b32 m0, %3\n\ts_nop 0\n\tbuffer_load_dwordx4 %0, %1, %2 offen sc1 lds" ::"v"(voff), "s"(r), "s"(soff), "s"(m0v) : "memory");
}
template <int N> DI void wait_vm() { asm volatile("s_waitcnt vmcnt(%0)" ::"i"(N) : "memory"); }
DI void bar() { SB(); asm volatile("s_waitcnt lgkmcnt(0)\n\ts_barrier" ::: "memory"); SB(); }
DI float pl32_max(float v) {
  auto r = __builtin_amdgcn_permlane32_swap(__float_as_uint(v), __float_as_uint(v), false, false);
  return fmaxf(__uint_as_float(r[0]), __uint_as_float(r[1]));
}
DI float pl32_sum(float v) {
  auto r = __builtin_amdgcn_permlane32_swap(__float_as_uint(v), __float_as_uint(v), false, false);
  return __uint_as_float(r[0]) + __uint_as_float(r[1]);
}
DI unsigned lane_id() { unsigned l; asm volatile("v_mbcnt_lo_u32_b32 %0, -1, 0\n\tv_mbcnt_hi_u32_b32 %0, -1, %0" : "=v"(l)); return l; }
DI void poll_flags(gu64* flags, unsigned idx0, unsigned stride, unsigned n, unsigned skip, unsigned long long tag, unsigned lane) {
  const bool mine = lane < n && lane != skip;
  gu64* p = flags + (size_t)(idx0 + (mine ? lane : 0) * stride) * 16;
  for (unsigned spins = 0; spins < 400000u; ++spins) {
    const unsigned long long v = __hip_atomic_load(p, __ATOMIC_RELAXED, __HIP_MEMORY_SCOPE_AGENT);
    if (__all(!mine || v == tag)) break;
    __builtin_amdgcn_s_sleep(1);
  }
}

#define POL_X 0
#define POL_WQ 0
#define O_ST_AUX 16
#define POL_O 2
#define POL_WO 0

__global__ __launch_bounds__(768, 3) void mha_kernel(const __hip_bfloat16* __restrict__ x, const __hip_bfloat16* __restrict__ kc,
                                                     const __hip_bfloat16* __restrict__ vc, const __hip_bfloat16* __restrict__ wq,
                                                     const __hip_bfloat16* __restrict__ wo, __hip_bfloat16* __restrict__ out,
                                                     char* __restrict__ ws, unsigned long long* diag) {
  STAMP(0);
  const unsigned long long st_r0 = __builtin_amdgcn_s_memrealtime(), st_c0 = __builtin_amdgcn_s_memtime();
  __shared__ __attribute__((aligned(1024))) char smem[LDS_TOTAL];
  lds_char* lds = (lds_char*)smem;
  const unsigned ldsb = (unsigned)(size_t)smem;
  const unsigned tid = threadIdx.x, w = rfl(tid >> 6);
  const unsigned xcd = blockIdx.x & 7, jj = blockIdx.x >> 3;
  const unsigned b = 4 * (xcd & 1) + (jj & 3), hd = 8 * (xcd >> 1) + (jj >> 2);
  gu64* flags = (gu64*)(ws + WS_FLAG);
  unsigned long long epoch;
  auto read_epoch = [&]() {
    asm volatile("s_load_dwordx2 %0, %1, 0x0\n\ts_waitcnt lgkmcnt(0)" : "=s"(epoch) : "s"((const char*)(ws + WS_FLAG) + (512 + b) * 128) : "memory");
  };

  bf16x8 qf[8];
  f32x16 oacc[4];
  float m, l0, l1;
  {
    const unsigned lane = tid & 63, r = lane & 31, h = lane >> 5;
    const unsigned t = w >> 1, j = w & 1;
    const unsigned g1 = (lane >> 4) & 1, qq = (lane & 15) >> 2, pp = lane & 3, sp = ((pp & 1) << 1) | (pp >> 1);
    const __amdgpu_buffer_rsrc_t rk = __builtin_amdgcn_make_buffer_rsrc((void*)kc, (short)0, NB * NH * SK * DH * 2, 0x00020000);
    const __amdgpu_buffer_rsrc_t rv = __builtin_amdgcn_make_buffer_rsrc((void*)vc, (short)0, NB * NH * SK * DH * 2, 0x00020000);
    const unsigned skv = (b * NH + hd) * (SK * DH * 2);
    const unsigned vk = (lane >> 4) * 256 + ((lane & 15) ^ (lane >> 4)) * 16;
    const unsigned vv = (lane >> 4) * 256 + ((lane & 15) ^ ((lane >> 4) << 2)) * 16;

    const unsigned koff = ((jj >> 2) + 2 * (jj & 3)) % 9;
    if (w >= 4) {
      const unsigned lw = w - 4;
      const __amdgpu_buffer_rsrc_t rx = __builtin_amdgcn_make_buffer_rsrc((void*)x, (short)0, NB * SQ * DM * 2, 0x00020000);
      const __amdgpu_buffer_rsrc_t rw = __builtin_amdgcn_make_buffer_rsrc((void*)wq, (short)0, DM * NH * DH * 2, 0x00020000);
      const unsigned vx = (lane >> 3) * 1152 + (((lane & 7) ^ (lane >> 4) ^ ((lw & 1) << 2)) * 16);
      const unsigned sx = (b * SQ + 8 * lw) * 1152u;
      const unsigned vw = (lane >> 4) * 8192 + (((lane & 15) ^ ((lane >> 4) << 2)) * 16);
      const unsigned sw = 4 * lw * 8192u + hd * 256u;
      auto issue_q = [&](int i) {
        unsigned st = koff + i; st = st >= 9 ? st - 9 : st;
        const unsigned slot = ldsb + (unsigned)(i % 3) * QS_BYTES;
#pragma unroll
        for (int p = 0; p < 3; ++p) dma16<POL_X>(rx, vx, sx + (unsigned)(64 * p) * 1152u + st * 128u, slot + (lw + 8 * p) * 1024u);
#pragma unroll
        for (int p = 0; p < 2; ++p) dma16<POL_WQ>(rw, vw, sw + (st * 64 + 32 * p) * 8192u, slot + QS_W + (lw + 8 * p) * 1024u);
      };
      auto kv4 = [&](int s) {
        const unsigned dst = ldsb + kv_slot(s) + lw * 4096u;
        const unsigned so = skv + ((lw >> 1) & 1) * 65536u + (lw & 1) * 4096u + (unsigned)s * 8192u;
#pragma unroll
        for (int p = 0; p < 4; ++p) {
          if (lw < 4) dma16<1>(rk, vk ^ (p << 6), so + p * 1024u, dst + p * 1024u);
          else dma16<1>(rv, vv, so + p * 1024u, dst + p * 1024u);
        }
      };
      issue_q(0); issue_q(1);
      wait_vm<5>(); bar(); issue_q(2);
      wait_vm<5>(); bar(); issue_q(3);
      wait_vm<5>(); bar(); issue_q(4); kv4(0);
      wait_vm<9>(); bar(); issue_q(5);
      wait_vm<9>(); bar(); issue_q(6);
      wait_vm<5>(); bar(); issue_q(7);
      wait_vm<5>(); bar(); issue_q(8);
      wait_vm<5>(); bar(); kv4(1);
      wait_vm<4>(); bar(); kv4(2);
      read_epoch();
      bar();
      bar();
    } else {
      read_epoch();
      if (w < 2) {
        unsigned long long pc;
        asm volatile("s_getpc_b64 %0" : "=s"(pc));
        const unsigned off = (jj * 2 + w) * 1024u;
        if (off + 1024u <= CODE_BYTES) {
          u32x4 t;
          const char* p = (const char*)(pc & ~127ull) + off + lane * 16;
          asm volatile("global_load_dwordx4 %0, %1, off\n\ts_waitcnt vmcnt(0)" : "=v"(t) : "v"(p) : "memory");
        }
      }
      const unsigned qh = w >> 1, dh = w & 1;
      const unsigned xa0 = (96 * qh + r) * 128 + ((h ^ ((r >> 1) & 7)) << 4);
      const unsigned wa0 = QS_W + (8 * h + qq) * 256 + ((4 * ((2 * dh) ^ qq) + 2 * g1 + (sp >> 1)) << 4) + (sp & 1) * 8;
      f32x16 qacc[3][2];
#pragma unroll
      for (int a = 0; a < 3; ++a)
#pragma unroll
        for (int i = 0; i < 16; ++i) { qacc[a][0][i] = 0.f; qacc[a][1][i] = 0.f; }
      bf16x8 bA[3], aA[2], bB[3], aB[2];
      unsigned xb, wb;
      auto rd = [&](bf16x8 (&bb)[3], bf16x8 (&aa)[2], int s) {
        const unsigned xs = xb ^ (s << 5), wb1 = wb ^ 64;
        aa[0] = lds_tr2(lds, wb + s * 4096, wb + s * 4096 + 1024);
        bb[0] = lds_rd128(lds, xs); bb[1] = lds_rd128(lds, xs + 4096); bb[2] = lds_rd128(lds, xs + 8192);
        aa[1] = lds_tr2(lds, wb1 + s * 4096, wb1 + s * 4096 + 1024);
      };
      auto mm = [&](bf16x8 (&bb)[3], bf16x8 (&aa)[2]) {
        qacc[0][0] = MFMA32(aa[0], bb[0], qacc[0][0]);
        qacc[1][0] = MFMA32(aa[0], bb[1], qacc[1][0]);
        qacc[2][0] = MFMA32(aa[0], bb[2], qacc[2][0]);
        qacc[0][1] = MFMA32(aa[1], bb[0], qacc[0][1]);
        qacc[1][1] = MFMA32(aa[1], bb[1], qacc[1][1]);
        qacc[2][1] = MFMA32(aa[1], bb[2], qacc[2][1]);
      };
      bar();
      STAMP(1);
      xb = xa0; wb = wa0;
      asm volatile("" : "+v"(xb), "+v"(wb));
      rd(bA, aA, 0);
#pragma unroll 1
      for (int st = 0; st < 8; ++st) {
        SB(); rd(bB, aB, 1); SB(); mm(bA, aA);
        SB(); rd(bA, aA, 2); SB(); mm(bB, aB);
        SB(); rd(bB, aB, 3); SB(); mm(bA, aA);
        bar();
        const unsigned so = (unsigned)((st + 1) % 3) * QS_BYTES;
        xb = xa0 + so; wb = wa0 + so;
        asm volatile("" : "+v"(xb), "+v"(wb));
        rd(bA, aA, 0);
        SB(); mm(bB, aB);
      }
      SB(); rd(bB, aB, 1); SB(); mm(bA, aA);
      SB(); rd(bA, aA, 2); SB(); mm(bB, aB);
      SB(); rd(bB, aB, 3); SB(); mm(bA, aA);
      SB(); mm(bB, aB);
      bar();
      STAMP(4);
#pragma unroll
      for (int a = 0; a < 3; ++a)
#pragma unroll
        for (int d = 0; d < 2; ++d) {
#pragma unroll
          for (int i = 0; i < 16; ++i) qacc[a][d][i] *= QSCALE;
#pragma unroll
          for (int s2 = 0; s2 < 2; ++s2) {
            const unsigned blk = (3 * qh + a) * 8 + 4 * dh + 2 * d + s2;
            *(lds_bf16x8*)(lds + qblk(blk) + lane * 16) = pack8(qacc[a][d], s2);
          }
        }
      bar();
    }
#pragma unroll
    for (int i = 0; i < 8; ++i) qf[i] = lds_rd128(lds, qblk(t * 8 + i) + lane * 16);

    const unsigned grp = w >> 2;
#pragma unroll
    for (int d = 0; d < 4; ++d)
#pragma unroll
      for (int i = 0; i < 16; ++i) oacc[d][i] = 0.f;
    m = -__builtin_inff(); l0 = 0.f; l1 = 0.f;
    const unsigned ka0 = j * 8192 + r * 256 + ((h ^ (r & 15)) << 4);
    const unsigned va0 = (16384 + j * 8192 + (4 * h + qq) * 256 + ((4 * qq + 2 * g1 + (sp >> 1)) << 4) + (sp & 1) * 8) ^ (j << 7);
    const unsigned vkx = vk ^ ((w & 3) << 6);
    const unsigned sp0 = skv + ((w >> 3) & 1) * 65536u + (w & 7) * 1024u;
    const unsigned sp1 = skv + (((w + 12) >> 3) & 1) * 65536u + ((w + 12) & 7) * 1024u;
    const unsigned sp2 = skv + (((w + 24) >> 3) & 1) * 65536u + ((w + 24) & 7) * 1024u;

    bf16x8 kf[4];
    auto step = [&](auto G_, int s) {
      constexpr int G = decltype(G_)::value;
      constexpr int NP = G == 2 ? 2 : 3;
      unsigned vb = va0 + kv_slot(s), kc0 = ka0 + kv_slot(s), kb = ka0 + kv_slot(s + 1);
      asm volatile("" : "+v"(kb), "+v"(vb), "+v"(kc0));
      __builtin_amdgcn_s_setprio(1);
      f32x16 sc;
      bf16x8 vf[4][2];
      {
        bf16x8 kg[4];
#pragma unroll
        for (int ks = 0; ks < 4; ++ks) kg[ks] = lds_rd128(lds, kc0 ^ ((ks + 4) << 5));
        SB();
#pragma unroll
        for (int i = 0; i < 16; ++i) sc[i] = 0.f;
#pragma unroll
        for (int ks = 0; ks < 4; ++ks) sc = MFMA32(kf[ks], qf[ks], sc);
        SB();
#pragma unroll
        for (int d = 0; d < 4; ++d) { const unsigned va = vb ^ (d << 6); vf[d][0] = lds_tr2(lds, va, va + 2048); }
        SB();
#pragma unroll
        for (int ks = 0; ks < 4; ++ks) sc = MFMA32(kg[ks], qf[ks + 4], sc);
        SB();
#pragma unroll
        for (int d = 0; d < 4; ++d) { const unsigned va = vb ^ (d << 6); vf[d][1] = lds_tr2(lds, va + 4096, va + 4096 + 2048); }
        SB();
      }
      __builtin_amdgcn_s_setprio(0);
      if constexpr (G == 1) { if (s <= 5) wait_vm<NP>(); else wait_vm<0>(); }
      bar();
      {
        const float t0 = fmaxf(fmaxf(sc[0], sc[1]), sc[2]), t1 = fmaxf(fmaxf(sc[3], sc[4]), sc[5]), t2 = fmaxf(fmaxf(sc[6], sc[7]), sc[8]);
        const float t3 = fmaxf(fmaxf(sc[9], sc[10]), sc[11]), t4 = fmaxf(fmaxf(sc[12], sc[13]), sc[14]);
        float tm = fmaxf(fmaxf(fmaxf(t0, t1), t2), fmaxf(fmaxf(t3, t4), sc[15]));
        tm = pl32_max(tm);
        if (__any(tm > m + THR)) {
          const float mn = fmaxf(m, tm);
          const float al = __builtin_amdgcn_exp2f(m - mn);
          l0 *= al; l1 *= al; m = mn;
#pragma unroll
          for (int d = 0; d < 4; ++d)
#pragma unroll
            for (int i = 0; i < 16; ++i) oacc[d][i] *= al;
        }
      }
#pragma unroll
      for (int i = 0; i < 16; ++i) sc[i] -= m;
      SB();
#pragma unroll
      for (int i = 0; i < 16; ++i) sc[i] = __builtin_amdgcn_exp2f(sc[i]);
      bf16x8 p0 = pack8(sc, 0), p1 = pack8(sc, 1);
#pragma unroll
      for (int i = 0; i < 16; i += 2) { l0 += sc[i]; l1 += sc[i + 1]; }
      asm volatile("" : "+v"(p0), "+v"(p1), "+v"(l0), "+v"(l1));
      if constexpr (G == 0) { if (s <= 5) wait_vm<NP>(); else wait_vm<0>(); }
      bar();
      __builtin_amdgcn_s_setprio(1);
#pragma unroll
      for (int d = 0; d < 4; ++d) oacc[d] = MFMA32(vf[d][0], p0, oacc[d]);
      SB();
#pragma unroll
      for (int ks = 0; ks < 4; ++ks) kf[ks] = lds_rd128(lds, kb ^ (ks << 5));
      SB();
#pragma unroll
      for (int d = 0; d < 4; ++d) oacc[d] = MFMA32(vf[d][1], p1, oacc[d]);
      __builtin_amdgcn_s_setprio(0);
      SB();
      if (s <= 4) {
        const unsigned dst = ldsb + kv_slot(s + 3) + w * 1024u, sofs = (unsigned)(s + 3) * 8192u;
        dma16<1>(rk, vkx, sp0 + sofs, dst);
        if constexpr (G == 0) dma16<1>(rk, vkx, sp1 + sofs, dst + 12288u); else dma16<1>(rv, vv, sp1 + sofs, dst + 12288u);
        if constexpr (G != 2) dma16<1>(rv, vv, sp2 + sofs, dst + 24576u);
      }
      if constexpr (G == 2) { if (s <= 4) wait_vm<NP>(); else wait_vm<0>(); }
      bar();
    };
    auto run = [&](auto G_) {
      constexpr int G = decltype(G_)::value;
      {
        unsigned kb0 = ka0 + kv_slot(0);
        asm volatile("" : "+v"(kb0));
#pragma unroll
        for (int ks = 0; ks < 4; ++ks) kf[ks] = lds_rd128(lds, kb0 ^ (ks << 5));
      }
      if constexpr (G >= 1) bar();
      if constexpr (G == 2) { wait_vm<4>(); bar(); }
#pragma unroll 1
      for (int s = 0; s < 8; ++s) step(G_, s);
      if constexpr (G == 0) bar();
      if constexpr (G <= 1) bar();
    };
    bar(); STAMP(5);
    if (grp == 0) run(IC<0>{});
    else if (grp == 1) run(IC<1>{});
    else run(IC<2>{});
  }
  STAMP(13);

  const unsigned lane = lane_id();
  const unsigned hg = hd >> 3, hl = hd & 7, rh = hl & 1, cq = hl >> 1;
  const unsigned soff2 = (4 * cq + 2 * (b & 3) + 8 * rh) & 15;
  const unsigned long long tag = epoch + 1;
  const unsigned wg = b * NH + hd;
  const __amdgpu_buffer_rsrc_t rwo = __builtin_amdgcn_make_buffer_rsrc((void*)wo, (short)0, NH * DH * DM * 2, 0x00020000);
  auto wo_voff = [&](int i) {
    const unsigned n = 64 * i + lane, p = n / 18, ch = n - 18 * p;
    const unsigned k = (p & 19) | ((p & 4) << 1) | ((p & 8) >> 1);
    return k * 1152 + ch * 16;
  };
  auto wo_issue = [&](const unsigned (&vo)[9], unsigned i) {
    const unsigned sg = (i + soff2) & 15, kh = w & 1;
    const unsigned so = ((8 * hg + 4 * kh + (sg >> 2)) * 128 + (sg & 3) * 32) * 1152u + cq * 288u;
    const unsigned dst = ldsb + p2_wslot(i) + kh * 9216u;
#pragma unroll
    for (int p = 0; p < 9; ++p) dma16<POL_WO>(rwo, vo[p], so, dst + p * 1024u);
  };
  unsigned wvo[9];
  if (w == 8 || w == 9) {
#pragma unroll
    for (int p = 0; p < 9; ++p) wvo[p] = wo_voff(p);
  }

  {
    const unsigned r = lane & 31, h = lane >> 5, t = w >> 1, j = w & 1;
    const float l = pl32_sum(l0 + l1);
    const unsigned xw = w * 8192 + lane * 16;
#pragma unroll
    for (int dd = 0; dd < 2; ++dd)
#pragma unroll
      for (int g = 0; g < 4; ++g) {
        f32x4 v = {oacc[2 + dd][4 * g], oacc[2 + dd][4 * g + 1], oacc[2 + dd][4 * g + 2], oacc[2 + dd][4 * g + 3]};
        *(lds_f32x4*)(lds + xw + (dd * 4 + g) * 1024) = v;
      }
    if (h == 0) { f32x2 ml = {m, l}; *(lds_f32x2*)(lds + ML_OFF + w * 256 + r * 8) = ml; }
    if (w == 8 || w == 9) wo_issue(wvo, 0);
    bar();
    f32x2 o = *(lds_f32x2*)(lds + ML_OFF + (w ^ 1) * 256 + r * 8);
    const float mn = fmaxf(m, o[0]);
    const float f = __builtin_amdgcn_exp2f(m - mn), fo = __builtin_amdgcn_exp2f(o[0] - mn);
    const float inv = 1.0f / (l * f + o[1] * fo);
    const float sa = f * inv, sb = fo * inv;
    const unsigned xr = (w ^ 1) * 8192 + lane * 16;
#pragma unroll
    for (int dd = 0; dd < 2; ++dd)
#pragma unroll
      for (int g = 0; g < 4; ++g) {
        f32x4 v = *(lds_f32x4*)(lds + xr + (dd * 4 + g) * 1024);
#pragma unroll
        for (int e = 0; e < 4; ++e) oacc[dd][4 * g + e] = oacc[dd][4 * g + e] * sa + v[e] * sb;
      }
    const __amdgpu_buffer_rsrc_t ro = __builtin_amdgcn_make_buffer_rsrc((void*)(ws + WS_O), (short)0, O_BYTES, 0x00020000);
    const unsigned so = ((wg * 6 + t) * 8 + 4 * j) * 1024u;
#pragma unroll
    for (int i = 0; i < 4; ++i)
      __builtin_amdgcn_raw_buffer_store_b128(__builtin_bit_cast(u32x4, pack8(oacc[i >> 1], i & 1)), ro, so + lane * 16 + i * 1024, 0, O_ST_AUX);
    if (w == 8 || w == 9) { wo_issue(wvo, 1); wait_vm<9>(); } else wait_vm<0>();
    bar();
    if (tid == 0) __hip_atomic_store(flags + (size_t)wg * 16, tag, __ATOMIC_RELAXED, __HIP_MEMORY_SCOPE_AGENT);
  }
  STAMP(14);

  f32x4 acc[3][5];
#pragma unroll
  for (int a = 0; a < 3; ++a)
#pragma unroll
    for (int n = 0; n < 5; ++n) acc[a][n] = f32x4{0.f, 0.f, 0.f, 0.f};
  const unsigned nh = (w >> 2) & 1, kh = w & 1, qh = (w >> 1) & 1;
  if (w >= 10) {
    poll_flags(flags, b * NH + 8 * hg, 1, 8, 64, tag, lane);
    STAMPW(10, 20);
    const __amdgpu_buffer_rsrc_t ro = __builtin_amdgcn_make_buffer_rsrc((void*)(ws + WS_O), (short)0, O_BYTES, 0x00020000);
    auto o_issue = [&](unsigned i) {
      const unsigned sg = (i + soff2) & 15;
      const unsigned head = 8 * hg + 4 * kh + (sg >> 2);
      const unsigned so = (((b * NH + head) * 6 + 3 * rh) * 8 + 2 * (sg & 3)) * 1024u;
      const unsigned dst = ldsb + p2_oslot(i) + kh * 6144u;
#pragma unroll
      for (int q = 0; q < 3; ++q) { dma16<POL_O>(ro, lane * 16, so + q * 8192u, dst + q * 2048u); dma16<POL_O>(ro, lane * 16, so + q * 8192u + 1024u, dst + q * 2048u + 1024u); }
    };
    o_issue(0); o_issue(1);
    STAMPW(10, 22);
#pragma unroll 1
    for (unsigned i = 0; i < 16; ++i) {
      if (i <= 14) wait_vm<6>(); else wait_vm<0>();
      if (i == 0) STAMPW(10, 23);
      bar();
      if (i + 2 < 16) o_issue(i + 2);
    }
  } else if (w >= 8) {
    wo_issue(wvo, 2);
    STAMPW(8, 24);
#pragma unroll 1
    for (unsigned i = 0; i < 16; ++i) {
      if (i <= 13) wait_vm<18>(); else if (i == 14) wait_vm<9>(); else wait_vm<0>();
      if (i == 0) STAMPW(8, 25);
      bar();
      if (i + 3 < 16) wo_issue(wvo, i + 3);
    }
  } else {
    const unsigned c = lane & 15, g = lane >> 4;
    unsigned ba[3];
#pragma unroll
    for (int a = 0; a < 3; ++a) {
      const unsigned qi = 3 * qh + a;
      ba[a] = kh * 6144 + ((qi >> 1) * 2 + (g >> 1)) * 1024 + (16 * (qi & 1) + c + 32 * (g & 1)) * 16;
    }
    const unsigned aa = kh * 9216 + ((g >> 1) * 16 + (g & 1) * 4 + (c >> 2)) * 288 + 5 * nh * 32 + (c & 3) * 8;
    bf16x8 bX[3], aX[5], bY[3], aY[5];
    auto rd = [&](auto NT_, bf16x8 (&bb)[3], bf16x8 (&av)[5], unsigned i) {
      constexpr int NT = decltype(NT_)::value;
      unsigned ob = p2_oslot(i), wb = aa + p2_wslot(i);
      asm volatile("" : "+v"(wb));
#pragma unroll
      for (int a = 0; a < 3; ++a) bb[a] = lds_rd128(lds, ba[a] + ob);
#pragma unroll
      for (int n = 0; n < NT; ++n) av[n] = lds_tr2(lds, wb + n * 32, wb + n * 32 + 2304);
    };
    auto mm = [&](auto NT_, bf16x8 (&bb)[3], bf16x8 (&av)[5]) {
      constexpr int NT = decltype(NT_)::value;
#pragma unroll
      for (int n = 0; n < NT; ++n)
#pragma unroll
        for (int a = 0; a < 3; ++a) acc[a][n] = MFMA16(av[n], bb[a], acc[a][n]);
    };
    unsigned long long t0 = 0;
    auto mix = [&](auto NT_) {
      constexpr int NT = decltype(NT_)::value;
#pragma unroll
      for (int k = 0; k < 3 + 2 * NT - (NT == 4 ? 0 : 0); ++k) {
        __builtin_amdgcn_sched_group_barrier(0x008, 1, 0);
        __builtin_amdgcn_sched_group_barrier(0x100, 1, 0);
      }
      __builtin_amdgcn_sched_group_barrier(0x008, 3 * NT, 0);
    };
    auto gemm = [&](auto NT_) {
      bar();
      STAMP(15);
      rd(NT_, bX, aX, 0);
#pragma unroll 1
      for (unsigned i = 1; i < 15; i += 2) {
        bar(); rd(NT_, bY, aY, i); mm(NT_, bX, aX); mix(NT_);
        bar(); rd(NT_, bX, aX, i + 1); mm(NT_, bY, aY); mix(NT_);
      }
      bar(); rd(NT_, bY, aY, 15); mm(NT_, bX, aX); mix(NT_);
      SB(); mm(NT_, bY, aY);
    };
    if (nh == 0) gemm(IC<5>{}); else gemm(IC<4>{});
    WREC(44, TNOW() - t0);
  }
  STAMP(16);
  bar();
  if (w < 8) {
    const unsigned c = lane & 15, g = lane >> 4;
    const unsigned base = kh * IMG_BYTES + (48 * qh + c) * IMG_STRIDE + (80 * nh + 4 * g) * 4;
#pragma unroll
    for (int a = 0; a < 3; ++a)
#pragma unroll
      for (int n = 0; n < 5; ++n)
        if (n < 4 || nh == 0) *(lds_f32x4*)(lds + base + a * 16 * IMG_STRIDE + n * 64) = acc[a][n];
  }
  bar();
  {
    const __amdgpu_buffer_rsrc_t rp = __builtin_amdgcn_make_buffer_rsrc((void*)(ws + WS_PART), (short)0, PART_BYTES, 0x00020000);
    const unsigned pbase = ((b * 8 + hl) * 4 + hg) * 4 * PART_BLK;
#pragma unroll 1
    for (unsigned n = tid; n < 72 * 36; n += 768) {
      const unsigned rr = n / 36, ch = n - rr * 36;
      unsigned dst = rr / 24; const unsigned row = rr - dst * 24;
      dst += dst >= hg ? 1 : 0;
      const unsigned a0 = (24 * dst + row) * IMG_STRIDE + ch * 16;
      const f32x4 v = *(lds_f32x4*)(lds + a0) + *(lds_f32x4*)(lds + IMG_BYTES + a0);
      __builtin_amdgcn_raw_buffer_store_b128(__builtin_bit_cast(u32x4, v), rp, dst * PART_BLK + row * 576 + ch * 16 + pbase, 0, 16);
    }
    wait_vm<0>();
    bar();
    if (tid == 0) __hip_atomic_store(flags + (size_t)(256 + wg) * 16, tag, __ATOMIC_RELAXED, __HIP_MEMORY_SCOPE_AGENT);
    STAMP(17);
    if (w == 0) poll_flags(flags, 256 + b * NH + hl, 8, 4, hg, tag, lane);
    bar();
    STAMP(18);
#pragma unroll 1
    for (unsigned n = tid; n < 24 * 36; n += 768) {
      const unsigned row = n / 36, ch = n - row * 36;
      const unsigned a0 = (24 * hg + row) * IMG_STRIDE + ch * 16;
      f32x4 v = *(lds_f32x4*)(lds + a0) + *(lds_f32x4*)(lds + IMG_BYTES + a0);
      u32x4 u[3];
#pragma unroll
      for (int k = 0; k < 3; ++k) {
        const unsigned src = k + (k >= (int)hg ? 1 : 0);
        u[k] = __builtin_amdgcn_raw_buffer_load_b128(rp, (((b * 8 + hl) * 4 + src) * 4 + hg) * PART_BLK + row * 576 + ch * 16, 0, 16);
      }
#pragma unroll
      for (int k = 0; k < 3; ++k) v += __builtin_bit_cast(f32x4, u[k]);
      u32x2 o = {cvtpk(v[0], v[1]), cvtpk(v[2], v[3])};
      *(u32x2*)((char*)out + ((size_t)(b * SQ + 96 * rh + 24 * hg + row) * DM + 144 * cq + 4 * ch) * 2) = o;
    }
    if (tid == 0 && hd == 0) __hip_atomic_store(flags + (size_t)(512 + b) * 16, tag, __ATOMIC_RELAXED, __HIP_MEMORY_SCOPE_AGENT);
  }
  if (tid == 0) { diag[blockIdx.x * 4] = st_r0; diag[blockIdx.x * 4 + 1] = __builtin_amdgcn_s_memrealtime(); diag[blockIdx.x * 4 + 2] = __builtin_amdgcn_s_memtime() - st_c0; }
}
__global__ void spin_kernel(const unsigned long long* diag) {
  __shared__ unsigned long long mn[256], mx[256], cy[256];
  const unsigned t = threadIdx.x;
  mn[t] = diag[t * 4]; mx[t] = diag[t * 4 + 1]; cy[t] = diag[t * 4 + 2];
  __syncthreads();
  if (t == 0) {
    unsigned long long a = ~0ull, b = 0, c = 0;
    for (int i = 0; i < 256; ++i) { a = mn[i] < a ? mn[i] : a; b = mx[i] > b ? mx[i] : b; }
    c = cy[0];
    const unsigned long long dur = c / 2;
    const unsigned long long t0 = __builtin_amdgcn_s_memrealtime();
    while (__builtin_amdgcn_s_memrealtime() - t0 < dur) __builtin_amdgcn_s_sleep(8);
  }
}

extern "C" void kernel_launch(void* const* d_in, const int* in_sizes, int n_in, void* d_out, int out_size, void* d_ws, size_t ws_size,
                              hipStream_t stream) {
  const __hip_bfloat16* x = (const __hip_bfloat16*)d_in[0];
  const __hip_bfloat16* kc = (const __hip_bfloat16*)d_in[1];
  const __hip_bfloat16* vc = (const __hip_bfloat16*)d_in[2];
  const __hip_bfloat16* wq = (const __hip_bfloat16*)d_in[3];
  const __hip_bfloat16* wo = (const __hip_bfloat16*)d_in[4];
  unsigned long long* diag = (unsigned long long*)((char*)d_ws + (64u << 20));
  mha_kernel<<<256, 768, 0, stream>>>(x, kc, vc, wq, wo, (__hip_bfloat16*)d_out, (char*)d_ws, diag);
  spin_kernel<<<1, 256, 0, stream>>>(diag);
}
```

```cpp
#define OUT_AUX 2
#include <hip/hip_runtime.h>
#include <hip/hip_bf16.h>
#include <type_traits>

typedef __bf16 bf16x8 __attribute__((ext_vector_type(8)));
typedef __bf16 bf16x4 __attribute__((ext_vector_type(4)));
typedef __bf16 bf16x2 __attribute__((ext_vector_type(2)));
typedef float f32x16 __attribute__((ext_vector_type(16)));
typedef float f32x4 __attribute__((ext_vector_type(4)));
typedef float f32x2 __attribute__((ext_vector_type(2)));
typedef unsigned u32x4 __attribute__((ext_vector_type(4)));
typedef unsigned u32x2 __attribute__((ext_vector_type(2)));
typedef __attribute__((address_space(3))) char lds_char;
typedef __attribute__((address_space(3))) bf16x4 lds_bf16x4;
typedef __attribute__((address_space(3))) bf16x8 lds_bf16x8;
typedef __attribute__((address_space(3))) f32x4 lds_f32x4;
typedef __attribute__((address_space(3))) f32x2 lds_f32x2;
typedef __attribute__((address_space(1))) unsigned long long gu64;

#define DI __device__ __forceinline__
#define MFMA32(a, b, c) __builtin_amdgcn_mfma_f32_32x32x16_bf16((a), (b), (c), 0, 0, 0)
#define MFMA16(a, b, c) __builtin_amdgcn_mfma_f32_16x16x32_bf16((a), (b), (c), 0, 0, 0)
#define SB() __builtin_amdgcn_sched_barrier(0)
template <int V> using IC = std::integral_constant<int, V>;
#define SG_MFMA(n) __builtin_amdgcn_sched_group_barrier(0x008, (n), 0)
#define SG_DSRD(n) __builtin_amdgcn_sched_group_barrier(0x100, (n), 0)

#define STAMPW(wv, i) do {} while (0)
#define STAMP(i) do {} while (0)
#define WREC(i, v) do {} while (0)
#define TNOW() 0ull

constexpr int NB = 8, SQ = 192, DM = 576, NH = 32, SK = 512, DH = 128;
constexpr float QSCALE = 0.08838834764831845f * 1.4426950408889634f;
constexpr float THR = 8.0f;

constexpr unsigned CODE_BYTES = 24 * 1024;
constexpr size_t WS_O = 0;
constexpr size_t WS_PART = 16u << 20;
constexpr size_t WS_FLAG = 32u << 20;
constexpr unsigned PART_BLK = 24 * 144 * 4;
constexpr unsigned O_BYTES = NB * NH * 6 * 8 * 1024, PART_BYTES = 8 * 8 * 4 * 4 * PART_BLK;

constexpr unsigned QS_BYTES = 40960, QS_W = 24576;
constexpr unsigned SPARE = 155648, LDS_TOTAL = 163840, ML_OFF = 98304;
DI constexpr unsigned kv_slot(int s) { return (unsigned)((s + 3) & 3) * QS_BYTES; }
DI unsigned qblk(unsigned blk) { return blk < 40 ? 2 * QS_BYTES + blk * 1024 : SPARE + (blk - 40) * 1024; }
constexpr unsigned P2_OSLOT = 12288, P2_WSLOT = 18432, P2_WBASE = 53248, IMG_STRIDE = 592, IMG_BYTES = 96 * IMG_STRIDE;
DI unsigned p2_oslot(unsigned i) { return (i & 3) * P2_OSLOT; }
DI unsigned p2_wslot(unsigned i) { return P2_WBASE + ((i + 3) % 6) * P2_WSLOT; }

DI unsigned rfl(unsigned x) { return __builtin_amdgcn_readfirstlane(x); }
DI unsigned cvtpk(float lo, float hi) { f32x2 v = {lo, hi}; bf16x2 b = __builtin_convertvector(v, bf16x2); return __builtin_bit_cast(unsigned, b); }
DI bf16x8 pack8(const f32x16& x, int s) {
  u32x4 p = {cvtpk(x[8 * s], x[8 * s + 1]), cvtpk(x[8 * s + 2], x[8 * s + 3]), cvtpk(x[8 * s + 4], x[8 * s + 5]), cvtpk(x[8 * s + 6], x[8 * s + 7])};
  return __builtin_bit_cast(bf16x8, p);
}
DI bf16x8 lds_rd128(lds_char* base, unsigned off) { return *(lds_bf16x8*)(base + off); }
DI bf16x8 lds_tr2(lds_char* base, unsigned off0, unsigned off1) {
  bf16x4 lo = __builtin_amdgcn_ds_read_tr16_b64_v4bf16((lds_bf16x4*)(base + off0));
  bf16x4 hi = __builtin_amdgcn_ds_read_tr16_b64_v4bf16((lds_bf16x4*)(base + off1));
  return __builtin_shufflevector(lo, hi, 0, 1, 2, 3, 4, 5, 6, 7);
}
template <int POL> DI void dma16(__amdgpu_buffer_rsrc_t r, unsigned voff, unsigned soff, unsigned m0v) {
  if constexpr (POL == 0) asm volatile("s_mov_b32 m0, %3\n\ts_nop 0\n\tbuffer_load_dwordx4 %0, %1, %2 offen lds" ::"v"(voff), "s"(r), "s"(soff), "s"(m0v) : "memory");
  else if constexpr (POL == 1) asm volatile("s_mov_b32 m0, %3\n\ts_nop 0\n\tbuffer_load_dwordx4 %0, %1, %2 offen nt lds" ::"v"(voff), "s"(r), "s"(soff), "s"(m0v) : "memory");
  else asm volatile("s_mov_b32 m0, %3\n\ts_nop 0\n\tbuffer_load_dwordx4 %0, %1, %2 offen sc1 lds" ::"v"(voff), "s"(r), "s"(soff), "s"(m0v) : "memory");
}
template <int N> DI void wait_vm() { asm volatile("s_waitcnt vmcnt(%0)" ::"i"(N) : "memory"); }
DI void bar() { SB(); asm volatile("s_waitcnt lgkmcnt(0)\n\ts_barrier" ::: "memory"); SB(); }
DI float pl32_max(float v) {
  auto r = __builtin_amdgcn_permlane32_swap(__float_as_uint(v), __float_as_uint(v), false, false);
  return fmaxf(__uint_as_float(r[0]), __uint_as_float(r[1]));
}
DI float pl32_sum(float v) {
  auto r = __builtin_amdgcn_permlane32_swap(__float_as_uint(v), __float_as_uint(v), false, false);
  return __uint_as_float(r[0]) + __uint_as_float(r[1]);
}
DI unsigned lane_id() { unsigned l; asm volatile("v_mbcnt_lo_u32_b32 %0, -1, 0\n\tv_mbcnt_hi_u32_b32 %0, -1, %0" : "=v"(l)); return l; }
DI void poll_flags(gu64* flags, unsigned idx0, unsigned stride, unsigned n, unsigned skip, unsigned long long tag, unsigned lane) {
  const bool mine = lane < n && lane != skip;
  gu64* p = flags + (size_t)(idx0 + (mine ? lane : 0) * stride) * 16;
  for (unsigned spins = 0; spins < 400000u; ++spins) {
    const unsigned long long v = __hip_atomic_load(p, __ATOMIC_RELAXED, __HIP_MEMORY_SCOPE_AGENT);
    if (__all(!mine || v == tag)) break;
    __builtin_amdgcn_s_sleep(1);
  }
}

#define POL_X 0
#define POL_WQ 0
#define O_ST_AUX 16
#define POL_O 2
#define POL_WO 0

__global__ __launch_bounds__(768, 3) void mha_kernel(const __hip_bfloat16* __restrict__ x, const __hip_bfloat16* __restrict__ kc,
                                                     const __hip_bfloat16* __restrict__ vc, const __hip_bfloat16* __restrict__ wq,
                                                     const __hip_bfloat16* __restrict__ wo, __hip_bfloat16* __restrict__ out,
                                                     char* __restrict__ ws, unsigned long long* diag) {
  STAMP(0);
  __shared__ __attribute__((aligned(1024))) char smem[LDS_TOTAL];
  lds_char* lds = (lds_char*)smem;
  const unsigned ldsb = (unsigned)(size_t)smem;
  const unsigned tid = threadIdx.x, w = rfl(tid >> 6);
  const unsigned xcd = blockIdx.x & 7, jj = blockIdx.x >> 3;
  const unsigned b = 4 * (xcd & 1) + (jj & 3), hd = 8 * (xcd >> 1) + (jj >> 2);
  gu64* flags = (gu64*)(ws + WS_FLAG);
  unsigned long long epoch;
  auto read_epoch = [&]() {
    asm volatile("s_load_dwordx2 %0, %1, 0x0\n\ts_waitcnt lgkmcnt(0)" : "=s"(epoch) : "s"((const char*)(ws + WS_FLAG) + (512 + b) * 128) : "memory");
  };

  bf16x8 qf[8];
  f32x16 oacc[4];
  float m, l0, l1;
  {
    const unsigned lane = tid & 63, r = lane & 31, h = lane >> 5;
    const unsigned t = w >> 1, j = w & 1;
    const unsigned g1 = (lane >> 4) & 1, qq = (lane & 15) >> 2, pp = lane & 3, sp = ((pp & 1) << 1) | (pp >> 1);
    const __amdgpu_buffer_rsrc_t rk = __builtin_amdgcn_make_buffer_rsrc((void*)kc, (short)0, NB * NH * SK * DH * 2, 0x00020000);
    const __amdgpu_buffer_rsrc_t rv = __builtin_amdgcn_make_buffer_rsrc((void*)vc, (short)0, NB * NH * SK * DH * 2, 0x00020000);
    const unsigned skv = (b * NH + hd) * (SK * DH * 2);
    const unsigned vk = (lane >> 4) * 256 + ((lane & 15) ^ (lane >> 4)) * 16;
    const unsigned vv = (lane >> 4) * 256 + ((lane & 15) ^ ((lane >> 4) << 2)) * 16;

    const unsigned koff = ((jj >> 2) + 2 * (jj & 3)) % 9;
    if (w >= 4) {
      const unsigned lw = w - 4;
      const __amdgpu_buffer_rsrc_t rx = __builtin_amdgcn_make_buffer_rsrc((void*)x, (short)0, NB * SQ * DM * 2, 0x00020000);
      const __amdgpu_buffer_rsrc_t rw = __builtin_amdgcn_make_buffer_rsrc((void*)wq, (short)0, DM * NH * DH * 2, 0x00020000);
      const unsigned vx = (lane >> 3) * 1152 + (((lane & 7) ^ (lane >> 4) ^ ((lw & 1) << 2)) * 16);
      const unsigned sx = (b * SQ + 8 * lw) * 1152u;
      const unsigned vw = (lane >> 4) * 8192 + (((lane & 15) ^ ((lane >> 4) << 2)) * 16);
      const unsigned sw = 4 * lw * 8192u + hd * 256u;
      auto issue_q = [&](int i) {
        unsigned st = koff + i; st = st >= 9 ? st - 9 : st;
        const unsigned slot = ldsb + (unsigned)(i % 3) * QS_BYTES;
#pragma unroll
        for (int p = 0; p < 3; ++p) dma16<POL_X>(rx, vx, sx + (unsigned)(64 * p) * 1152u + st * 128u, slot + (lw + 8 * p) * 1024u);
#pragma unroll
        for (int p = 0; p < 2; ++p) dma16<POL_WQ>(rw, vw, sw + (st * 64 + 32 * p) * 8192u, slot + QS_W + (lw + 8 * p) * 1024u);
      };
      auto kv4 = [&](int s) {
        const unsigned dst = ldsb + kv_slot(s) + lw * 4096u;
        const unsigned so = skv + ((lw >> 1) & 1) * 65536u + (lw & 1) * 4096u + (unsigned)s * 8192u;
#pragma unroll
        for (int p = 0; p < 4; ++p) {
          if (lw < 4) dma16<1>(rk, vk ^ (p << 6), so + p * 1024u, dst + p * 1024u);
          else dma16<1>(rv, vv, so + p * 1024u, dst + p * 1024u);
        }
      };
      issue_q(0); issue_q(1);
      wait_vm<5>(); bar(); issue_q(2);
      wait_vm<5>(); bar(); issue_q(3);
      wait_vm<5>(); bar(); issue_q(4); kv4(0);
      wait_vm<9>(); bar(); issue_q(5);
      wait_vm<9>(); bar(); issue_q(6);
      wait_vm<5>(); bar(); issue_q(7);
      wait_vm<5>(); bar(); issue_q(8);
      wait_vm<5>(); bar(); kv4(1);
      wait_vm<4>(); bar(); kv4(2);
      read_epoch();
      bar();
      bar();
    } else {
      read_epoch();
      if (w < 2) {
        unsigned long long pc;
        asm volatile("s_getpc_b64 %0" : "=s"(pc));
        const unsigned off = (jj * 2 + w) * 1024u;
        if (off + 1024u <= CODE_BYTES) {
          u32x4 t;
          const char* p = (const char*)(pc & ~127ull) + off + lane * 16;
          asm volatile("global_load_dwordx4 %0, %1, off\n\ts_waitcnt vmcnt(0)" : "=v"(t) : "v"(p) : "memory");
        }
      }
      const unsigned qh = w >> 1, dh = w & 1;
      const unsigned xa0 = (96 * qh + r) * 128 + ((h ^ ((r >> 1) & 7)) << 4);
      const unsigned wa0 = QS_W + (8 * h + qq) * 256 + ((4 * ((2 * dh) ^ qq) + 2 * g1 + (sp >> 1)) << 4) + (sp & 1) * 8;
      f32x16 qacc[3][2];
#pragma unroll
      for (int a = 0; a < 3; ++a)
#pragma unroll
        for (int i = 0; i < 16; ++i) { qacc[a][0][i] = 0.f; qacc[a][1][i] = 0.f; }
      bf16x8 bA[3], aA[2], bB[3], aB[2];
      unsigned xb, wb;
      auto rd = [&](bf16x8 (&bb)[3], bf16x8 (&aa)[2], int s) {
        const unsigned xs = xb ^ (s << 5), wb1 = wb ^ 64;
        aa[0] = lds_tr2(lds, wb + s * 4096, wb + s * 4096 + 1024);
        bb[0] = lds_rd128(lds, xs); bb[1] = lds_rd128(lds, xs + 4096); bb[2] = lds_rd128(lds, xs + 8192);
        aa[1] = lds_tr2(lds, wb1 + s * 4096, wb1 + s * 4096 + 1024);
      };
      auto mm = [&](bf16x8 (&bb)[3], bf16x8 (&aa)[2]) {
        qacc[0][0] = MFMA32(aa[0], bb[0], qacc[0][0]);
        qacc[1][0] = MFMA32(aa[0], bb[1], qacc[1][0]);
        qacc[2][0] = MFMA32(aa[0], bb[2], qacc[2][0]);
        qacc[0][1] = MFMA32(aa[1], bb[0], qacc[0][1]);
        qacc[1][1] = MFMA32(aa[1], bb[1], qacc[1][1]);
        qacc[2][1] = MFMA32(aa[1], bb[2], qacc[2][1]);
      };
      bar();
      STAMP(1);
      xb = xa0; wb = wa0;
      asm volatile("" : "+v"(xb), "+v"(wb));
      rd(bA, aA, 0);
      auto mix = [&]() { SG_MFMA(1); SG_DSRD(2); SG_MFMA(1); SG_DSRD(2); SG_MFMA(1); SG_DSRD(1); SG_MFMA(1); SG_DSRD(1); SG_MFMA(1); SG_DSRD(1); SG_MFMA(1); SB(); };
#pragma unroll 1
      for (int st = 0; st < 8; ++st) {
        SB(); rd(bB, aB, 1); mm(bA, aA); mix();
        rd(bA, aA, 2); mm(bB, aB); mix();
        rd(bB, aB, 3); mm(bA, aA); mix();
        bar();
        const unsigned so = (unsigned)((st + 1) % 3) * QS_BYTES;
        xb = xa0 + so; wb = wa0 + so;
        asm volatile("" : "+v"(xb), "+v"(wb));
        rd(bA, aA, 0); mm(bB, aB); mix();
      }
      SB(); rd(bB, aB, 1); mm(bA, aA); mix();
      rd(bA, aA, 2); mm(bB, aB); mix();
      rd(bB, aB, 3); mm(bA, aA); mix();
      mm(bB, aB);
      bar();
      STAMP(4);
#pragma unroll
      for (int a = 0; a < 3; ++a)
#pragma unroll
        for (int d = 0; d < 2; ++d) {
#pragma unroll
          for (int i = 0; i < 16; ++i) qacc[a][d][i] *= QSCALE;
#pragma unroll
          for (int s2 = 0; s2 < 2; ++s2) {
            const unsigned blk = (3 * qh + a) * 8 + 4 * dh + 2 * d + s2;
            *(lds_bf16x8*)(lds + qblk(blk) + lane * 16) = pack8(qacc[a][d], s2);
          }
        }
      bar();
    }
#pragma unroll
    for (int i = 0; i < 8; ++i) qf[i] = lds_rd128(lds, qblk(t * 8 + i) + lane * 16);

    const unsigned grp = w >> 2;
#pragma unroll
    for (int d = 0; d < 4; ++d)
#pragma unroll
      for (int i = 0; i < 16; ++i) oacc[d][i] = 0.f;
    m = -__builtin_inff(); l0 = 0.f; l1 = 0.f;
    const unsigned ka0 = j * 8192 + r * 256 + ((h ^ (r & 15)) << 4);
    const unsigned va0 = (16384 + j * 8192 + (4 * h + qq) * 256 + ((4 * qq + 2 * g1 + (sp >> 1)) << 4) + (sp & 1) * 8) ^ (j << 7);
    const unsigned vkx = vk ^ ((w & 3) << 6);
    const unsigned sp0 = skv + ((w >> 3) & 1) * 65536u + (w & 7) * 1024u;
    const unsigned sp1 = skv + (((w + 12) >> 3) & 1) * 65536u + ((w + 12) & 7) * 1024u;
    const unsigned sp2 = skv + (((w + 24) >> 3) & 1) * 65536u + ((w + 24) & 7) * 1024u;

    bf16x8 kf[4];
    auto step = [&](auto G_, int s) {
      constexpr int G = decltype(G_)::value;
      constexpr int NP = G == 2 ? 2 : 3;
      unsigned vb = va0 + kv_slot(s), kc0 = ka0 + kv_slot(s), kb = ka0 + kv_slot(s + 1);
      asm volatile("" : "+v"(kb), "+v"(vb), "+v"(kc0));
      __builtin_amdgcn_s_setprio(1);
      f32x16 sc;
      bf16x8 vf[4][2];
      {
        bf16x8 kg[4];
#pragma unroll
        for (int ks = 0; ks < 4; ++ks) kg[ks] = lds_rd128(lds, kc0 ^ ((ks + 4) << 5));
#pragma unroll
        for (int i = 0; i < 16; ++i) sc[i] = 0.f;
#pragma unroll
        for (int ks = 0; ks < 4; ++ks) sc = MFMA32(kf[ks], qf[ks], sc);
#pragma unroll
        for (int d = 0; d < 4; ++d) { const unsigned va = vb ^ (d << 6); vf[d][0] = lds_tr2(lds, va, va + 2048); }
#pragma unroll
        for (int ks = 0; ks < 4; ++ks) sc = MFMA32(kg[ks], qf[ks + 4], sc);
        SG_MFMA(1); SG_DSRD(2); SG_MFMA(1); SG_DSRD(2); SG_MFMA(1); SG_DSRD(2); SG_MFMA(1); SG_DSRD(2);
        SG_MFMA(1); SG_DSRD(2); SG_MFMA(1); SG_DSRD(2); SG_MFMA(2);
        SB();
        asm volatile("" : "+v"(sc));
      }
      __builtin_amdgcn_s_setprio(0);
      if constexpr (G == 1) { if (s <= 5) wait_vm<NP>(); else wait_vm<0>(); }
      bar();
#pragma unroll
      for (int d = 0; d < 4; ++d) { const unsigned va = vb ^ (d << 6); vf[d][1] = lds_tr2(lds, va + 4096, va + 4096 + 2048); }
      SB();
      {
        const float t0 = fmaxf(fmaxf(sc[0], sc[1]), sc[2]), t1 = fmaxf(fmaxf(sc[3], sc[4]), sc[5]), t2 = fmaxf(fmaxf(sc[6], sc[7]), sc[8]);
        const float t3 = fmaxf(fmaxf(sc[9], sc[10]), sc[11]), t4 = fmaxf(fmaxf(sc[12], sc[13]), sc[14]);
        float tm = fmaxf(fmaxf(fmaxf(t0, t1), t2), fmaxf(fmaxf(t3, t4), sc[15]));
        tm = pl32_max(tm);
        if (__any(tm > m + THR)) {
          const float mn = fmaxf(m, tm);
          const float al = __builtin_amdgcn_exp2f(m - mn);
          l0 *= al; l1 *= al; m = mn;
#pragma unroll
          for (int d = 0; d < 4; ++d)
#pragma unroll
            for (int i = 0; i < 16; ++i) oacc[d][i] *= al;
        }
      }
#pragma unroll
      for (int i = 0; i < 16; ++i) sc[i] -= m;
      SB();
#pragma unroll
      for (int i = 0; i < 16; ++i) sc[i] = __builtin_amdgcn_exp2f(sc[i]);
      bf16x8 p0 = pack8(sc, 0), p1 = pack8(sc, 1);
#pragma unroll
      for (int i = 0; i < 16; i += 2) { l0 += sc[i]; l1 += sc[i + 1]; }
      asm volatile("" : "+v"(p0), "+v"(p1), "+v"(l0), "+v"(l1));
      if constexpr (G == 0) { if (s <= 5) wait_vm<NP>(); else wait_vm<0>(); }
      bar();
      __builtin_amdgcn_s_setprio(1);
#pragma unroll
      for (int d = 0; d < 4; ++d) oacc[d] = MFMA32(vf[d][0], p0, oacc[d]);
#pragma unroll
      for (int ks = 0; ks < 4; ++ks) kf[ks] = lds_rd128(lds, kb ^ (ks << 5));
#pragma unroll
      for (int d = 0; d < 4; ++d) oacc[d] = MFMA32(vf[d][1], p1, oacc[d]);
      SG_MFMA(2); SG_DSRD(1); SG_MFMA(1); SG_DSRD(1); SG_MFMA(1); SG_DSRD(1); SG_MFMA(1); SG_DSRD(1); SG_MFMA(3);
      SB();
      asm volatile("" : "+v"(oacc[0]), "+v"(oacc[1]), "+v"(oacc[2]), "+v"(oacc[3]));
      __builtin_amdgcn_s_setprio(0);
      SB();
      if (s <= 4) {
        const unsigned dst = ldsb + kv_slot(s + 3) + w * 1024u, sofs = (unsigned)(s + 3) * 8192u;
        dma16<1>(rk, vkx, sp0 + sofs, dst);
        if constexpr (G == 0) dma16<1>(rk, vkx, sp1 + sofs, dst + 12288u); else dma16<1>(rv, vv, sp1 + sofs, dst + 12288u);
        if constexpr (G != 2) dma16<1>(rv, vv, sp2 + sofs, dst + 24576u);
      }
      if constexpr (G == 2) { if (s <= 4) wait_vm<NP>(); else wait_vm<0>(); }
      bar();
    };
    auto run = [&](auto G_) {
      constexpr int G = decltype(G_)::value;
      {
        unsigned kb0 = ka0 + kv_slot(0);
        asm volatile("" : "+v"(kb0));
#pragma unroll
        for (int ks = 0; ks < 4; ++ks) kf[ks] = lds_rd128(lds, kb0 ^ (ks << 5));
      }
      if constexpr (G >= 1) bar();
      if constexpr (G == 2) { wait_vm<4>(); bar(); }
#pragma unroll 1
      for (int s = 0; s < 8; ++s) step(G_, s);
      if constexpr (G == 0) bar();
      if constexpr (G <= 1) bar();
    };
    bar(); STAMP(5);
    if (grp == 0) run(IC<0>{});
    else if (grp == 1) run(IC<1>{});
    else run(IC<2>{});
  }
  STAMP(13);

  const unsigned lane = lane_id();
  const unsigned hg = hd >> 3, hl = hd & 7, rh = hl & 1, cq = hl >> 1;
  const unsigned soff2 = (4 * cq + 2 * (b & 3) + 8 * rh) & 15;
  const unsigned long long tag = epoch + 1;
  const unsigned wg = b * NH + hd;
  const __amdgpu_buffer_rsrc_t rwo = __builtin_amdgcn_make_buffer_rsrc((void*)wo, (short)0, NH * DH * DM * 2, 0x00020000);
  auto wo_voff = [&](int i) {
    const unsigned n = 64 * i + lane, p = n / 18, ch = n - 18 * p;
    const unsigned k = (p & 19) | ((p & 4) << 1) | ((p & 8) >> 1);
    return k * 1152 + ch * 16;
  };
  auto wo_issue = [&](const unsigned (&vo)[9], unsigned i) {
    const unsigned sg = (i + soff2) & 15, kh = w & 1;
    const unsigned so = ((8 * hg + 4 * kh + (sg >> 2)) * 128 + (sg & 3) * 32) * 1152u + cq * 288u;
    const unsigned dst = ldsb + p2_wslot(i) + kh * 9216u;
#pragma unroll
    for (int p = 0; p < 9; ++p) dma16<POL_WO>(rwo, vo[p], so, dst + p * 1024u);
  };
  unsigned wvo[9];
  if (w == 8 || w == 9) {
#pragma unroll
    for (int p = 0; p < 9; ++p) wvo[p] = wo_voff(p);
  }

  {
    const unsigned r = lane & 31, h = lane >> 5, t = w >> 1, j = w & 1;
    const float l = pl32_sum(l0 + l1);
    const unsigned xw = w * 8192 + lane * 16;
#pragma unroll
    for (int dd = 0; dd < 2; ++dd)
#pragma unroll
      for (int g = 0; g < 4; ++g) {
        f32x4 v = {oacc[2 + dd][4 * g], oacc[2 + dd][4 * g + 1], oacc[2 + dd][4 * g + 2], oacc[2 + dd][4 * g + 3]};
        *(lds_f32x4*)(lds + xw + (dd * 4 + g) * 1024) = v;
      }
    if (h == 0) { f32x2 ml = {m, l}; *(lds_f32x2*)(lds + ML_OFF + w * 256 + r * 8) = ml; }
    if (w == 8 || w == 9) wo_issue(wvo, 0);
    bar();
    f32x2 o = *(lds_f32x2*)(lds + ML_OFF + (w ^ 1) * 256 + r * 8);
    const float mn = fmaxf(m, o[0]);
    const float f = __builtin_amdgcn_exp2f(m - mn), fo = __builtin_amdgcn_exp2f(o[0] - mn);
    const float inv = 1.0f / (l * f + o[1] * fo);
    const float sa = f * inv, sb = fo * inv;
    const unsigned xr = (w ^ 1) * 8192 + lane * 16;
#pragma unroll
    for (int dd = 0; dd < 2; ++dd)
#pragma unroll
      for (int g = 0; g < 4; ++g) {
        f32x4 v = *(lds_f32x4*)(lds + xr + (dd * 4 + g) * 1024);
#pragma unroll
        for (int e = 0; e < 4; ++e) oacc[dd][4 * g + e] = oacc[dd][4 * g + e] * sa + v[e] * sb;
      }
    const __amdgpu_buffer_rsrc_t ro = __builtin_amdgcn_make_buffer_rsrc((void*)(ws + WS_O), (short)0, O_BYTES, 0x00020000);
    const unsigned so = ((wg * 6 + t) * 8 + 4 * j) * 1024u;
#pragma unroll
    for (int i = 0; i < 4; ++i)
      __builtin_amdgcn_raw_buffer_store_b128(__builtin_bit_cast(u32x4, pack8(oacc[i >> 1], i & 1)), ro, so + lane * 16 + i * 1024, 0, O_ST_AUX);
    if (w == 8 || w == 9) { wo_issue(wvo, 1); wait_vm<9>(); } else wait_vm<0>();
    bar();
    if (tid == 0) __hip_atomic_store(flags + (size_t)wg * 16, tag, __ATOMIC_RELAXED, __HIP_MEMORY_SCOPE_AGENT);
  }
  STAMP(14);

  f32x4 acc[3][5];
#pragma unroll
  for (int a = 0; a < 3; ++a)
#pragma unroll
    for (int n = 0; n < 5; ++n) acc[a][n] = f32x4{0.f, 0.f, 0.f, 0.f};
  const unsigned nh = (w >> 2) & 1, kh = w & 1, qh = (w >> 1) & 1;
  if (w >= 10) {
    poll_flags(flags, b * NH + 8 * hg, 1, 8, 64, tag, lane);
    STAMPW(10, 20);
    const __amdgpu_buffer_rsrc_t ro = __builtin_amdgcn_make_buffer_rsrc((void*)(ws + WS_O), (short)0, O_BYTES, 0x00020000);
    auto o_issue = [&](unsigned i) {
      const unsigned sg = (i + soff2) & 15;
      const unsigned head = 8 * hg + 4 * kh + (sg >> 2);
      const unsigned so = (((b * NH + head) * 6 + 3 * rh) * 8 + 2 * (sg & 3)) * 1024u;
      const unsigned dst = ldsb + p2_oslot(i) + kh * 6144u;
#pragma unroll
      for (int q = 0; q < 3; ++q) { dma16<POL_O>(ro, lane * 16, so + q * 8192u, dst + q * 2048u); dma16<POL_O>(ro, lane * 16, so + q * 8192u + 1024u, dst + q * 2048u + 1024u); }
    };
    o_issue(0); o_issue(1);
    STAMPW(10, 22);
#pragma unroll 1
    for (unsigned i = 0; i < 16; ++i) {
      if (i <= 14) wait_vm<6>(); else wait_vm<0>();
      if (i == 0) STAMPW(10, 23);
      bar();
      if (i + 2 < 16) o_issue(i + 2);
    }
  } else if (w >= 8) {
    wo_issue(wvo, 2);
    STAMPW(8, 24);
#pragma unroll 1
    for (unsigned i = 0; i < 16; ++i) {
      if (i <= 13) wait_vm<18>(); else if (i == 14) wait_vm<9>(); else wait_vm<0>();
      if (i == 0) STAMPW(8, 25);
      bar();
      if (i + 3 < 16) wo_issue(wvo, i + 3);
    }
  } else {
    const unsigned c = lane & 15, g = lane >> 4;
    unsigned ba[3];
#pragma unroll
    for (int a = 0; a < 3; ++a) {
      const unsigned qi = 3 * qh + a;
      ba[a] = kh * 6144 + ((qi >> 1) * 2 + (g >> 1)) * 1024 + (16 * (qi & 1) + c + 32 * (g & 1)) * 16;
    }
    const unsigned aa = kh * 9216 + ((g >> 1) * 16 + (g & 1) * 4 + (c >> 2)) * 288 + 5 * nh * 32 + (c & 3) * 8;
    bf16x8 bX[3], aX[5], bY[3], aY[5];
    auto rd = [&](auto NT_, bf16x8 (&bb)[3], bf16x8 (&av)[5], unsigned i) {
      constexpr int NT = decltype(NT_)::value;
      unsigned ob = p2_oslot(i), wb = aa + p2_wslot(i);
      asm volatile("" : "+v"(wb));
#pragma unroll
      for (int a = 0; a < 3; ++a) bb[a] = lds_rd128(lds, ba[a] + ob);
#pragma unroll
      for (int n = 0; n < NT; ++n) av[n] = lds_tr2(lds, wb + n * 32, wb + n * 32 + 2304);
    };
    auto mm = [&](auto NT_, bf16x8 (&bb)[3], bf16x8 (&av)[5]) {
      constexpr int NT = decltype(NT_)::value;
#pragma unroll
      for (int n = 0; n < NT; ++n)
#pragma unroll
        for (int a = 0; a < 3; ++a) acc[a][n] = MFMA16(av[n], bb[a], acc[a][n]);
    };
    unsigned long long t0 = 0;
    auto mix = [&](auto NT_) {
      constexpr int NT = decltype(NT_)::value;
#pragma unroll
      for (int k = 0; k < 3 + 2 * NT - (NT == 4 ? 0 : 0); ++k) {
        __builtin_amdgcn_sched_group_barrier(0x008, 1, 0);
        __builtin_amdgcn_sched_group_barrier(0x100, 1, 0);
      }
      __builtin_amdgcn_sched_group_barrier(0x008, 3 * NT, 0);
    };
    auto gemm = [&](auto NT_) {
      bar();
      STAMP(15);
      rd(NT_, bX, aX, 0);
#pragma unroll 1
      for (unsigned i = 1; i < 15; i += 2) {
        bar(); rd(NT_, bY, aY, i); mm(NT_, bX, aX); mix(NT_);
        bar(); rd(NT_, bX, aX, i + 1); mm(NT_, bY, aY); mix(NT_);
      }
      bar(); rd(NT_, bY, aY, 15); mm(NT_, bX, aX); mix(NT_);
      SB(); mm(NT_, bY, aY);
    };
    if (nh == 0) gemm(IC<5>{}); else gemm(IC<4>{});
    WREC(44, TNOW() - t0);
  }
  STAMP(16);
  bar();
  if (w < 8) {
    const unsigned c = lane & 15, g = lane >> 4;
    const unsigned base = kh * IMG_BYTES + (48 * qh + c) * IMG_STRIDE + (80 * nh + 4 * g) * 4;
#pragma unroll
    for (int a = 0; a < 3; ++a)
#pragma unroll
      for (int n = 0; n < 5; ++n)
        if (n < 4 || nh == 0) *(lds_f32x4*)(lds + base + a * 16 * IMG_STRIDE + n * 64) = acc[a][n];
  }
  bar();
  {
    const __amdgpu_buffer_rsrc_t rp = __builtin_amdgcn_make_buffer_rsrc((void*)(ws + WS_PART), (short)0, PART_BYTES, 0x00020000);
    const unsigned pbase = ((b * 8 + hl) * 4 + hg) * 4 * PART_BLK;
#pragma unroll 1
    for (unsigned n = tid; n < 72 * 36; n += 768) {
      const unsigned rr = n / 36, ch = n - rr * 36;
      unsigned dst = rr / 24; const unsigned row = rr - dst * 24;
      dst += dst >= hg ? 1 : 0;
      const unsigned a0 = (24 * dst + row) * IMG_STRIDE + ch * 16;
      const f32x4 v = *(lds_f32x4*)(lds + a0) + *(lds_f32x4*)(lds + IMG_BYTES + a0);
      __builtin_amdgcn_raw_buffer_store_b128(__builtin_bit_cast(u32x4, v), rp, dst * PART_BLK + row * 576 + ch * 16 + pbase, 0, 16);
    }
    wait_vm<0>();
    bar();
    if (tid == 0) __hip_atomic_store(flags + (size_t)(256 + wg) * 16, tag, __ATOMIC_RELAXED, __HIP_MEMORY_SCOPE_AGENT);
    STAMP(17);
    if (w == 0) poll_flags(flags, 256 + b * NH + hl, 8, 4, hg, tag, lane);
    bar();
    STAMP(18);
    const __amdgpu_buffer_rsrc_t rout = __builtin_amdgcn_make_buffer_rsrc((void*)out, (short)0, NB * SQ * DM * 2, 0x00020000);
#pragma unroll 1
    for (unsigned n = tid; n < 24 * 36; n += 768) {
      const unsigned row = n / 36, ch = n - row * 36;
      const unsigned a0 = (24 * hg + row) * IMG_STRIDE + ch * 16;
      f32x4 v = *(lds_f32x4*)(lds + a0) + *(lds_f32x4*)(lds + IMG_BYTES + a0);
      u32x4 u[3];
#pragma unroll
      for (int k = 0; k < 3; ++k) {
        const unsigned src = k + (k >= (int)hg ? 1 : 0);
        u[k] = __builtin_amdgcn_raw_buffer_load_b128(rp, (((b * 8 + hl) * 4 + src) * 4 + hg) * PART_BLK + row * 576 + ch * 16, 0, 16);
      }
#pragma unroll
      for (int k = 0; k < 3; ++k) v += __builtin_bit_cast(f32x4, u[k]);
      u32x2 o = {cvtpk(v[0], v[1]), cvtpk(v[2], v[3])};
      __builtin_amdgcn_raw_buffer_store_b64(o, rout, ((b * SQ + 96 * rh + 24 * hg + row) * DM + 144 * cq + 4 * ch) * 2, 0, OUT_AUX);
    }
    if (tid == 0 && hd == 0) __hip_atomic_store(flags + (size_t)(512 + b) * 16, tag, __ATOMIC_RELAXED, __HIP_MEMORY_SCOPE_AGENT);
  }
}

extern "C" void kernel_launch(void* const* d_in, const int* in_sizes, int n_in, void* d_out, int out_size, void* d_ws, size_t ws_size,
                              hipStream_t stream) {
  const __hip_bfloat16* x = (const __hip_bfloat16*)d_in[0];
  const __hip_bfloat16* kc = (const __hip_bfloat16*)d_in[1];
  const __hip_bfloat16* vc = (const __hip_bfloat16*)d_in[2];
  const __hip_bfloat16* wq = (const __hip_bfloat16*)d_in[3];
  const __hip_bfloat16* wo = (const __hip_bfloat16*)d_in[4];
  unsigned long long* diag = (unsigned long long*)((char*)d_ws + (64u << 20));
  mha_kernel<<<256, 768, 0, stream>>>(x, kc, vc, wq, wo, (__hip_bfloat16*)d_out, (char*)d_ws, diag);
}
```

```cpp
#include <hip/hip_runtime.h>
#include <hip/hip_bf16.h>
#include <type_traits>

typedef __bf16 bf16x8 __attribute__((ext_vector_type(8)));
typedef __bf16 bf16x4 __attribute__((ext_vector_type(4)));
typedef __bf16 bf16x2 __attribute__((ext_vector_type(2)));
typedef float f32x16 __attribute__((ext_vector_type(16)));
typedef float f32x4 __attribute__((ext_vector_type(4)));
typedef float f32x2 __attribute__((ext_vector_type(2)));
typedef unsigned u32x4 __attribute__((ext_vector_type(4)));
typedef unsigned u32x2 __attribute__((ext_vector_type(2)));
typedef __attribute__((address_space(3))) char lds_char;
typedef __attribute__((address_space(3))) bf16x4 lds_bf16x4;
typedef __attribute__((address_space(3))) bf16x8 lds_bf16x8;
typedef __attribute__((address_space(3))) f32x4 lds_f32x4;
typedef __attribute__((address_space(3))) f32x2 lds_f32x2;
typedef __attribute__((address_space(1))) unsigned long long gu64;

#define DI __device__ __forceinline__
#define MFMA32(a, b, c) __builtin_amdgcn_mfma_f32_32x32x16_bf16((a), (b), (c), 0, 0, 0)
#define MFMA16(a, b, c) __builtin_amdgcn_mfma_f32_16x16x32_bf16((a), (b), (c), 0, 0, 0)
#define SB() __builtin_amdgcn_sched_barrier(0)
template <int V> using IC = std::integral_constant<int, V>;
#define SG_MFMA(n) __builtin_amdgcn_sched_group_barrier(0x008, (n), 0)
#define SG_DSRD(n) __builtin_amdgcn_sched_group_barrier(0x100, (n), 0)

#define STAMPW(wv, i) do {} while (0)
#define STAMP(i) do {} while (0)
#define WREC(i, v) do {} while (0)
#define TNOW() 0ull

constexpr int NB = 8, SQ = 192, DM = 576, NH = 32, SK = 512, DH = 128;
constexpr float QSCALE = 0.08838834764831845f * 1.4426950408889634f;
constexpr float THR = 8.0f;

constexpr unsigned CODE_BYTES = 26 * 1024;
constexpr size_t WS_O = 0;
constexpr size_t WS_PART = 16u << 20;
constexpr size_t WS_FLAG = 32u << 20;
constexpr unsigned PART_BLK = 24 * 144 * 2;
constexpr unsigned O_BYTES = NB * NH * 6 * 8 * 1024, PART_BYTES = 8 * 8 * 4 * 4 * PART_BLK;

constexpr unsigned QS_BYTES = 40960, QS_W = 24576;
constexpr unsigned SPARE = 155648, LDS_TOTAL = 163840, ML_OFF = 98304, VOTE_OFF = 101376;
constexpr float L_MIN = 9.313225746154785e-10f, L_MAX = 1048576.0f;
DI constexpr unsigned kv_slot(int s) { return (unsigned)((s + 3) & 3) * QS_BYTES; }
DI unsigned qblk(unsigned blk) { return blk < 40 ? 2 * QS_BYTES + blk * 1024 : SPARE + (blk - 40) * 1024; }
constexpr unsigned P2_OSLOT = 12288, P2_WSLOT = 18432, P2_WBASE = 53248, IMG_STRIDE = 592, IMG_BYTES = 96 * IMG_STRIDE;
DI unsigned p2_oslot(unsigned i) { return (i & 3) * P2_OSLOT; }
DI unsigned p2_wslot(unsigned i) { return P2_WBASE + ((i + 3) % 6) * P2_WSLOT; }

DI unsigned rfl(unsigned x) { return __builtin_amdgcn_readfirstlane(x); }
DI unsigned cvtpk(float lo, float hi) { f32x2 v = {lo, hi}; bf16x2 b = __builtin_convertvector(v, bf16x2); return __builtin_bit_cast(unsigned, b); }
DI bf16x8 pack8(const f32x16& x, int s) {
  u32x4 p = {cvtpk(x[8 * s], x[8 * s + 1]), cvtpk(x[8 * s + 2], x[8 * s + 3]), cvtpk(x[8 * s + 4], x[8 * s + 5]), cvtpk(x[8 * s + 6], x[8 * s + 7])};
  return __builtin_bit_cast(bf16x8, p);
}
DI bf16x8 lds_rd128(lds_char* base, unsigned off) { return *(lds_bf16x8*)(base + off); }
DI bf16x8 lds_tr2(lds_char* base, unsigned off0, unsigned off1) {
  bf16x4 lo = __builtin_amdgcn_ds_read_tr16_b64_v4bf16((lds_bf16x4*)(base + off0));
  bf16x4 hi = __builtin_amdgcn_ds_read_tr16_b64_v4bf16((lds_bf16x4*)(base + off1));
  return __builtin_shufflevector(lo, hi, 0, 1, 2, 3, 4, 5, 6, 7);
}
template <int POL> DI void dma16(__amdgpu_buffer_rsrc_t r, unsigned voff, unsigned soff, unsigned m0v) {
  if constexpr (POL == 0) asm volatile("s_mov_b32 m0, %3\n\ts_nop 0\n\tbuffer_load_dwordx4 %0, %1, %2 offen lds" ::"v"(voff), "s"(r), "s"(soff), "s"(m0v) : "memory");
  else if constexpr (POL == 1) asm volatile("s_mov_b32 m0, %3\n\ts_nop 0\n\tbuffer_load_dwordx4 %0, %1, %2 offen nt lds" ::"v"(voff), "s"(r), "s"(soff), "s"(m0v) : "memory");
  else asm volatile("s_mov_b32 m0, %3\n\ts_nop 0\n\tbuffer_load_dwordx4 %0, %1, %2 offen sc1 lds" ::"v"(voff), "s"(r), "s"(soff), "s"(m0v) : "memory");
}
template <int N> DI void wait_vm() { asm volatile("s_waitcnt vmcnt(%0)" ::"i"(N) : "memory"); }
DI void bar() { SB(); asm volatile("s_waitcnt lgkmcnt(0)\n\ts_barrier" ::: "memory"); SB(); }
DI float pl32_max(float v) {
  auto r = __builtin_amdgcn_permlane32_swap(__float_as_uint(v), __float_as_uint(v), false, false);
  return fmaxf(__uint_as_float(r[0]), __uint_as_float(r[1]));
}
DI float pl32_sum(float v) {
  auto r = __builtin_amdgcn_permlane32_swap(__float_as_uint(v), __float_as_uint(v), false, false);
  return __uint_as_float(r[0]) + __uint_as_float(r[1]);
}
DI unsigned lane_id() { unsigned l; asm volatile("v_mbcnt_lo_u32_b32 %0, -1, 0\n\tv_mbcnt_hi_u32_b32 %0, -1, %0" : "=v"(l)); return l; }
DI void poll_flags(gu64* flags, unsigned idx0, unsigned stride, unsigned n, unsigned skip, unsigned long long tag, unsigned lane) {
  const bool mine = lane < n && lane != skip;
  gu64* p = flags + (size_t)(idx0 + (mine ? lane : 0) * stride) * 16;
  for (unsigned spins = 0; spins < 400000u; ++spins) {
    const unsigned long long v = __hip_atomic_load(p, __ATOMIC_RELAXED, __HIP_MEMORY_SCOPE_AGENT);
    if (__all(!mine || v == tag)) break;
    __builtin_amdgcn_s_sleep(1);
  }
}

#define ROTMODE 2
#define POL_X 0
#define POL_WQ 0
#define O_ST_AUX 16
#define POL_O 2
#define POL_WO 0

__global__ __launch_bounds__(768, 3) void mha_kernel(const __hip_bfloat16* __restrict__ x, const __hip_bfloat16* __restrict__ kc,
                                                     const __hip_bfloat16* __restrict__ vc, const __hip_bfloat16* __restrict__ wq,
                                                     const __hip_bfloat16* __restrict__ wo, __hip_bfloat16* __restrict__ out,
                                                     char* __restrict__ ws, unsigned long long* diag) {
  __shared__ __attribute__((aligned(1024))) char smem[LDS_TOTAL];
  lds_char* lds = (lds_char*)smem;
  const unsigned ldsb = (unsigned)(size_t)smem;
  const unsigned tid = threadIdx.x, w = rfl(tid >> 6);
  STAMP(0);
  const unsigned xcd = blockIdx.x & 7, jj = blockIdx.x >> 3;
  const unsigned b = 4 * (xcd & 1) + (jj & 3), hd = 8 * (xcd >> 1) + (jj >> 2);
  gu64* flags = (gu64*)(ws + WS_FLAG);
  unsigned long long epoch;
  auto read_epoch = [&]() {
    asm volatile("s_load_dwordx2 %0, %1, 0x0\n\ts_waitcnt lgkmcnt(0)" : "=s"(epoch) : "s"((const char*)(ws + WS_FLAG) + (512 + b) * 128) : "memory");
  };

  const __amdgpu_buffer_rsrc_t rwo = __builtin_amdgcn_make_buffer_rsrc((void*)wo, (short)0, NH * DH * DM * 2, 0x00020000);
  bf16x8 qf[8];
  f32x16 oacc[4];
  float m, l;
  {
    const unsigned lane = tid & 63, r = lane & 31, h = lane >> 5;
    const unsigned t = w >> 1, j = w & 1;
    const unsigned g1 = (lane >> 4) & 1, qq = (lane & 15) >> 2, pp = lane & 3, sp = ((pp & 1) << 1) | (pp >> 1);
    const __amdgpu_buffer_rsrc_t rk = __builtin_amdgcn_make_buffer_rsrc((void*)kc, (short)0, NB * NH * SK * DH * 2, 0x00020000);
    const __amdgpu_buffer_rsrc_t rv = __builtin_amdgcn_make_buffer_rsrc((void*)vc, (short)0, NB * NH * SK * DH * 2, 0x00020000);
    const unsigned skv = (b * NH + hd) * (SK * DH * 2);
    const unsigned vk = (lane >> 4) * 256 + ((lane & 15) ^ (lane >> 4)) * 16;
    const unsigned vv = (lane >> 4) * 256 + ((lane & 15) ^ ((lane >> 4) << 2)) * 16;

    auto kv4 = [&](int s) {
      const unsigned lw = w - 4;
      const unsigned dst = ldsb + kv_slot(s) + lw * 4096u;
      const unsigned so = skv + ((lw >> 1) & 1) * 65536u + (lw & 1) * 4096u + (unsigned)s * 8192u;
#pragma unroll
      for (int p = 0; p < 4; ++p) {
        if (lw < 4) dma16<1>(rk, vk ^ (p << 6), so + p * 1024u, dst + p * 1024u);
        else dma16<1>(rv, vv, so + p * 1024u, dst + p * 1024u);
      }
    };
    const unsigned koff = 3 * (((jj >> 2) + (jj & 3)) % 3);
    if (w >= 4) {
      const unsigned lw = w - 4;
      const __amdgpu_buffer_rsrc_t rx = __builtin_amdgcn_make_buffer_rsrc((void*)x, (short)0, NB * SQ * DM * 2, 0x00020000);
      const __amdgpu_buffer_rsrc_t rw = __builtin_amdgcn_make_buffer_rsrc((void*)wq, (short)0, DM * NH * DH * 2, 0x00020000);
      const unsigned vx = (lane >> 3) * 1152 + (((lane & 7) ^ (lane >> 4) ^ ((lw & 1) << 2)) * 16);
      const unsigned sx = (b * SQ + 8 * lw) * 1152u;
      const unsigned vw = (lane >> 4) * 8192 + (((lane & 15) ^ ((lane >> 4) << 2)) * 16);
      const unsigned sw = 4 * lw * 8192u + hd * 256u;
      auto issue_q = [&](int i) {
        unsigned st = koff + i; st = st >= 9 ? st - 9 : st;
        const unsigned slot = ldsb + (unsigned)(i % 3) * QS_BYTES;
#pragma unroll
        for (int p = 0; p < 3; ++p) dma16<POL_X>(rx, vx, sx + (unsigned)(64 * p) * 1152u + st * 128u, slot + (lw + 8 * p) * 1024u);
#pragma unroll
        for (int p = 0; p < 2; ++p) dma16<POL_WQ>(rw, vw, sw + (st * 64 + 32 * p) * 8192u, slot + QS_W + (lw + 8 * p) * 1024u);
      };
      issue_q(0); issue_q(1);
      wait_vm<5>(); bar(); issue_q(2);
      wait_vm<5>(); bar(); issue_q(3);
      wait_vm<5>(); bar(); issue_q(4); kv4(0);
      wait_vm<9>(); bar(); issue_q(5);
      wait_vm<9>(); bar(); issue_q(6);
      wait_vm<5>(); bar(); issue_q(7);
      wait_vm<5>(); bar(); issue_q(8);
      wait_vm<5>(); bar(); kv4(1);
      wait_vm<4>(); bar(); kv4(2);
      read_epoch();
      bar();
      bar();
    } else {
      read_epoch();
      if (w < 2) {
        unsigned long long pc;
        asm volatile("s_getpc_b64 %0" : "=s"(pc));
        const unsigned off = (jj * 2 + w) * 1024u;
        if (off + 1024u <= CODE_BYTES) {
          u32x4 t;
          const char* p = (const char*)(pc & ~127ull) + off + lane * 16;
          asm volatile("global_load_dwordx4 %0, %1, off\n\ts_waitcnt vmcnt(0)" : "=v"(t) : "v"(p) : "memory");
        }
      }
      const unsigned qh = w >> 1, dh = w & 1;
      const unsigned xa0 = (96 * qh + r) * 128 + ((h ^ ((r >> 1) & 7)) << 4);
      const unsigned wa0 = QS_W + (8 * h + qq) * 256 + ((4 * ((2 * dh) ^ qq) + 2 * g1 + (sp >> 1)) << 4) + (sp & 1) * 8;
      f32x16 qacc[3][2];
#pragma unroll
      for (int a = 0; a < 3; ++a)
#pragma unroll
        for (int i = 0; i < 16; ++i) { qacc[a][0][i] = 0.f; qacc[a][1][i] = 0.f; }
      bf16x8 bA[3], aA[2], bB[3], aB[2];
      unsigned xb, wb;
      auto rd = [&](bf16x8 (&bb)[3], bf16x8 (&aa)[2], int s) {
        const unsigned xs = xb ^ (s << 5), wb1 = wb ^ 64;
        aa[0] = lds_tr2(lds, wb + s * 4096, wb + s * 4096 + 1024);
        bb[0] = lds_rd128(lds, xs); bb[1] = lds_rd128(lds, xs + 4096); bb[2] = lds_rd128(lds, xs + 8192);
        aa[1] = lds_tr2(lds, wb1 + s * 4096, wb1 + s * 4096 + 1024);
      };
      auto mm = [&](bf16x8 (&bb)[3], bf16x8 (&aa)[2]) {
        qacc[0][0] = MFMA32(aa[0], bb[0], qacc[0][0]);
        qacc[1][0] = MFMA32(aa[0], bb[1], qacc[1][0]);
        qacc[2][0] = MFMA32(aa[0], bb[2], qacc[2][0]);
        qacc[0][1] = MFMA32(aa[1], bb[0], qacc[0][1]);
        qacc[1][1] = MFMA32(aa[1], bb[1], qacc[1][1]);
        qacc[2][1] = MFMA32(aa[1], bb[2], qacc[2][1]);
      };
      bar();
      STAMP(1);
      xb = xa0; wb = wa0;
      asm volatile("" : "+v"(xb), "+v"(wb));
      rd(bA, aA, 0);
      auto mix = [&]() { SG_MFMA(1); SG_DSRD(2); SG_MFMA(1); SG_DSRD(2); SG_MFMA(1); SG_DSRD(1); SG_MFMA(1); SG_DSRD(1); SG_MFMA(1); SG_DSRD(1); SG_MFMA(1); SB(); };
#pragma unroll 1
      for (int st = 0; st < 8; ++st) {
        SB(); rd(bB, aB, 1); mm(bA, aA); mix();
        rd(bA, aA, 2); mm(bB, aB); mix();
        rd(bB, aB, 3); mm(bA, aA); mix();
        bar();
        const unsigned so = (unsigned)((st + 1) % 3) * QS_BYTES;
        xb = xa0 + so; wb = wa0 + so;
        asm volatile("" : "+v"(xb), "+v"(wb));
        rd(bA, aA, 0); mm(bB, aB); mix();
      }
      SB(); rd(bB, aB, 1); mm(bA, aA); mix();
      rd(bA, aA, 2); mm(bB, aB); mix();
      rd(bB, aB, 3); mm(bA, aA); mix();
      mm(bB, aB);
      bar();
      STAMP(4);
#pragma unroll
      for (int a = 0; a < 3; ++a)
#pragma unroll
        for (int d = 0; d < 2; ++d) {
#pragma unroll
          for (int i = 0; i < 16; ++i) qacc[a][d][i] *= QSCALE;
#pragma unroll
          for (int s2 = 0; s2 < 2; ++s2) {
            const unsigned blk = (3 * qh + a) * 8 + 4 * dh + 2 * d + s2;
            *(lds_bf16x8*)(lds + qblk(blk) + lane * 16) = pack8(qacc[a][d], s2);
          }
        }
      bar();
    }
#pragma unroll
    for (int i = 0; i < 8; ++i) qf[i] = lds_rd128(lds, qblk(t * 8 + i) + lane * 16);

    const unsigned grp = w >> 2;
    float l0, l1;
    bool safe = false;
    const unsigned ka0 = j * 8192 + r * 256 + ((h ^ (r & 15)) << 4);
    const unsigned va0 = (16384 + j * 8192 + (4 * h + qq) * 256 + ((4 * qq + 2 * g1 + (sp >> 1)) << 4) + (sp & 1) * 8) ^ (j << 7);
    const unsigned vkx = vk ^ ((w & 3) << 6);
    const unsigned sp0 = skv + ((w >> 3) & 1) * 65536u + (w & 7) * 1024u;
    const unsigned sp1 = skv + (((w + 12) >> 3) & 1) * 65536u + ((w + 12) & 7) * 1024u;
    const unsigned sp2 = skv + (((w + 24) >> 3) & 1) * 65536u + ((w + 24) & 7) * 1024u;

    bf16x8 kf[4];
    auto step = [&](auto G_, int s) {
      constexpr int G = decltype(G_)::value;
      constexpr int NP = G == 2 ? 2 : 3;
      unsigned vb = va0 + kv_slot(s), kc0 = ka0 + kv_slot(s), kb = ka0 + kv_slot(s + 1);
      asm volatile("" : "+v"(kb), "+v"(vb), "+v"(kc0));
      __builtin_amdgcn_s_setprio(1);
      f32x16 sc;
      bf16x8 vf[4][2];
      {
        bf16x8 kg[4];
#pragma unroll
        for (int ks = 0; ks < 4; ++ks) kg[ks] = lds_rd128(lds, kc0 ^ ((ks + 4) << 5));
#pragma unroll
        for (int i = 0; i < 16; ++i) sc[i] = 0.f;
#pragma unroll
        for (int ks = 0; ks < 4; ++ks) sc = MFMA32(kf[ks], qf[ks], sc);
#pragma unroll
        for (int d = 0; d < 4; ++d) { const unsigned va = vb ^ (d << 6); vf[d][0] = lds_tr2(lds, va, va + 2048); }
#pragma unroll
        for (int ks = 0; ks < 4; ++ks) sc = MFMA32(kg[ks], qf[ks + 4], sc);
        SG_MFMA(1); SG_DSRD(2); SG_MFMA(1); SG_DSRD(2); SG_MFMA(1); SG_DSRD(2); SG_MFMA(1); SG_DSRD(2);
        SG_MFMA(1); SG_DSRD(2); SG_MFMA(1); SG_DSRD(2); SG_MFMA(2);
        SB();
        asm volatile("" : "+v"(sc));
      }
      __builtin_amdgcn_s_setprio(0);
      if constexpr (G == 1) { if (s <= 5) wait_vm<NP>(); else if (s == 6) wait_vm<0>(); }
      bar();
#pragma unroll
      for (int d = 0; d < 4; ++d) { const unsigned va = vb ^ (d << 6); vf[d][1] = lds_tr2(lds, va + 4096, va + 4096 + 2048); }
      SB();
      if (safe) {
        const float t0 = fmaxf(fmaxf(sc[0], sc[1]), sc[2]), t1 = fmaxf(fmaxf(sc[3], sc[4]), sc[5]), t2 = fmaxf(fmaxf(sc[6], sc[7]), sc[8]);
        const float t3 = fmaxf(fmaxf(sc[9], sc[10]), sc[11]), t4 = fmaxf(fmaxf(sc[12], sc[13]), sc[14]);
        float tm = fmaxf(fmaxf(fmaxf(t0, t1), t2), fmaxf(fmaxf(t3, t4), sc[15]));
        tm = pl32_max(tm);
        if (__any(tm > m + THR)) {
          const float mn = fmaxf(m, tm);
          const float al = __builtin_amdgcn_exp2f(m - mn);
          l0 *= al; l1 *= al; m = mn;
#pragma unroll
          for (int d = 0; d < 4; ++d)
#pragma unroll
            for (int i = 0; i < 16; ++i) oacc[d][i] *= al;
        }
#pragma unroll
        for (int i = 0; i < 16; ++i) sc[i] -= m;
      }
      SB();
#pragma unroll
      for (int i = 0; i < 16; ++i) sc[i] = __builtin_amdgcn_exp2f(sc[i]);
      bf16x8 p0 = pack8(sc, 0), p1 = pack8(sc, 1);
#pragma unroll
      for (int i = 0; i < 16; i += 2) { l0 += sc[i]; l1 += sc[i + 1]; }
      asm volatile("" : "+v"(p0), "+v"(p1), "+v"(l0), "+v"(l1));
      if constexpr (G == 0) { if (s <= 5) wait_vm<NP>(); else if (s == 6) wait_vm<0>(); }
      bar();
      __builtin_amdgcn_s_setprio(1);
#pragma unroll
      for (int d = 0; d < 4; ++d) oacc[d] = MFMA32(vf[d][0], p0, oacc[d]);
#pragma unroll
      for (int ks = 0; ks < 4; ++ks) kf[ks] = lds_rd128(lds, kb ^ (ks << 5));
#pragma unroll
      for (int d = 0; d < 4; ++d) oacc[d] = MFMA32(vf[d][1], p1, oacc[d]);
      SG_MFMA(2); SG_DSRD(1); SG_MFMA(1); SG_DSRD(1); SG_MFMA(1); SG_DSRD(1); SG_MFMA(1); SG_DSRD(1); SG_MFMA(3);
      SB();
      asm volatile("" : "+v"(oacc[0]), "+v"(oacc[1]), "+v"(oacc[2]), "+v"(oacc[3]));
      __builtin_amdgcn_s_setprio(0);
      SB();
      if (s <= 4) {
        const unsigned dst = ldsb + kv_slot(s + 3) + w * 1024u, sofs = (unsigned)(s + 3) * 8192u;
        dma16<1>(rk, vkx, sp0 + sofs, dst);
        if constexpr (G == 0) dma16<1>(rk, vkx, sp1 + sofs, dst + 12288u); else dma16<1>(rv, vv, sp1 + sofs, dst + 12288u);
        if constexpr (G != 2) dma16<1>(rv, vv, sp2 + sofs, dst + 24576u);
      }
      if constexpr (G == 2) { if (s <= 4) wait_vm<NP>(); else if (s == 5) wait_vm<0>(); }
      if (s >= 6 && !safe) {
        const unsigned pc = ((b & 3) * 8 + (hd & 7)) * 36 + w * 3 + (s - 6) * 2;
        const unsigned so = (hd >> 3) * (1024u * 1152u) + pc * 1024u;
        dma16<POL_WO>(rwo, lane * 16, so, ldsb + SPARE + (w & 7) * 1024u);
        if (s == 6) dma16<POL_WO>(rwo, lane * 16, so + 1024u, ldsb + SPARE + (w & 7) * 1024u);
      }
      bar();
    };
    auto run = [&](auto G_) {
      constexpr int G = decltype(G_)::value;
      {
        unsigned kb0 = ka0 + kv_slot(0);
        asm volatile("" : "+v"(kb0));
#pragma unroll
        for (int ks = 0; ks < 4; ++ks) kf[ks] = lds_rd128(lds, kb0 ^ (ks << 5));
      }
      if constexpr (G >= 1) bar();
      if constexpr (G == 2) { wait_vm<4>(); bar(); }
#pragma unroll 1
      for (int s = 0; s < 8; ++s) step(G_, s);
      if constexpr (G == 0) bar();
      if constexpr (G <= 1) bar();
    };
#pragma unroll 1
    for (;;) {
#pragma unroll
      for (int d = 0; d < 4; ++d)
#pragma unroll
        for (int i = 0; i < 16; ++i) oacc[d][i] = 0.f;
      m = safe ? -__builtin_inff() : 0.f; l0 = 0.f; l1 = 0.f;
      bar(); STAMP(5);
      if (grp == 0) run(IC<0>{});
      else if (grp == 1) run(IC<1>{});
      else run(IC<2>{});
      STAMP(13);
      const unsigned ln = lane_id();
      l = pl32_sum(l0 + l1);
      const unsigned xw = w * 4096 + ln * 16;
#pragma unroll
      for (int i = 0; i < 4; ++i) *(lds_bf16x8*)(lds + xw + i * 1024) = pack8(oacc[2 + (i >> 1)], i & 1);
      if (ln < 32) { f32x2 ml = {m, l}; *(lds_f32x2*)(lds + ML_OFF + w * 256 + ln * 8) = ml; }
      const bool bad = __any(!(l > L_MIN && l < L_MAX));
      if (ln == 0) *(__attribute__((address_space(3))) unsigned*)(lds + VOTE_OFF + w * 4) = bad ? 1u : 0u;
      bar();
      STAMP(6);
      if (safe) break;
      const unsigned vote = *(__attribute__((address_space(3))) unsigned*)(lds + VOTE_OFF + (ln < 12 ? ln : 0) * 4);
      if (!__any(vote != 0)) break;
      safe = true;
      bar();
      if (w >= 4) {
        const unsigned lw = rfl(w - 4), sk = rfl((b * NH + hd) * (SK * DH * 2));
        const unsigned vo = (ln >> 4) * 256 + (lw < 4 ? ((ln & 15) ^ (ln >> 4)) : ((ln & 15) ^ ((ln >> 4) << 2))) * 16;
        const __amdgpu_buffer_rsrc_t rr = __builtin_amdgcn_make_buffer_rsrc((void*)(lw < 4 ? kc : vc), (short)0, NB * NH * SK * DH * 2, 0x00020000);
#pragma unroll 1
        for (unsigned q = 0; q < 12; ++q) {
          const unsigned st = q >> 2, p = q & 3;
          dma16<1>(rr, lw < 4 ? vo ^ (p << 6) : vo, rfl(sk + ((lw >> 1) & 1) * 65536u + (lw & 1) * 4096u + st * 8192u + p * 1024u), rfl(ldsb + kv_slot(st) + lw * 4096u + p * 1024u));
        }
        wait_vm<0>();
      }
    }
  }

  const unsigned lane = lane_id();
  const unsigned hg = hd >> 3, hl = hd & 7, rh = hl & 1, cq = hl >> 1;
  const unsigned soff2 = (4 * cq + 2 * (b & 3) + 8 * rh) & 15;
  const unsigned long long tag = epoch + 1;
  const unsigned wg = b * NH + hd;
  auto wo_voff = [&](int i) {
    const unsigned n = 64 * i + lane, p = n / 18, ch = n - 18 * p;
    const unsigned k = (p & 19) | ((p & 4) << 1) | ((p & 8) >> 1);
    return k * 1152 + ch * 16;
  };
  auto wo_issue = [&](const unsigned (&vo)[9], unsigned i) {
    const unsigned sg = (i + soff2) & 15, kh = w & 1;
    const unsigned so = ((8 * hg + 4 * kh + (sg >> 2)) * 128 + (sg & 3) * 32) * 1152u + cq * 288u;
    const unsigned dst = ldsb + p2_wslot(i) + kh * 9216u;
#pragma unroll
    for (int p = 0; p < 9; ++p) dma16<POL_WO>(rwo, vo[p], so, dst + p * 1024u);
  };
  unsigned wvo[9];
  if (w == 8 || w == 9) {
#pragma unroll
    for (int p = 0; p < 9; ++p) wvo[p] = wo_voff(p);
  }

  {
    const unsigned r = lane & 31, h = lane >> 5, t = w >> 1, j = w & 1;
    f32x2 o = *(lds_f32x2*)(lds + ML_OFF + (w ^ 1) * 256 + r * 8);
    const float mn = fmaxf(m, o[0]);
    const float f = __builtin_amdgcn_exp2f(m - mn), fo = __builtin_amdgcn_exp2f(o[0] - mn);
    const float inv = 1.0f / (l * f + o[1] * fo);
    const float sa = f * inv, sb = fo * inv;
    const unsigned xr = (w ^ 1) * 4096 + lane * 16;
#pragma unroll
    for (int i = 0; i < 4; ++i) {
      const u32x4 v = __builtin_bit_cast(u32x4, lds_rd128(lds, xr + i * 1024));
#pragma unroll
      for (int e = 0; e < 4; ++e) {
        oacc[i >> 1][8 * (i & 1) + 2 * e] = oacc[i >> 1][8 * (i & 1) + 2 * e] * sa + __uint_as_float(v[e] << 16) * sb;
        oacc[i >> 1][8 * (i & 1) + 2 * e + 1] = oacc[i >> 1][8 * (i & 1) + 2 * e + 1] * sa + __uint_as_float(v[e] & 0xffff0000u) * sb;
      }
    }
    const __amdgpu_buffer_rsrc_t ro = __builtin_amdgcn_make_buffer_rsrc((void*)(ws + WS_O), (short)0, O_BYTES, 0x00020000);
    const unsigned so = ((wg * 6 + t) * 8 + 4 * j) * 1024u;
#pragma unroll
    for (int i = 0; i < 4; ++i)
      __builtin_amdgcn_raw_buffer_store_b128(__builtin_bit_cast(u32x4, pack8(oacc[i >> 1], i & 1)), ro, so + lane * 16 + i * 1024, 0, O_ST_AUX);
    STAMP(7);
    if (w == 8 || w == 9) { wo_issue(wvo, 0); wo_issue(wvo, 1); wait_vm<18>(); } else wait_vm<0>();
    STAMP(8);
    bar();
    if (tid == 0) __hip_atomic_store(flags + (size_t)wg * 16, tag, __ATOMIC_RELAXED, __HIP_MEMORY_SCOPE_AGENT);
  }
  STAMP(14);

  f32x4 acc[3][5];
#pragma unroll
  for (int a = 0; a < 3; ++a)
#pragma unroll
    for (int n = 0; n < 5; ++n) acc[a][n] = f32x4{0.f, 0.f, 0.f, 0.f};
  const unsigned nh = (w >> 2) & 1, kh = w & 1, qh = (w >> 1) & 1;
  if (w >= 10) {
    poll_flags(flags, b * NH + 8 * hg, 1, 8, 64, tag, lane);
    STAMPW(10, 20);
    const __amdgpu_buffer_rsrc_t ro = __builtin_amdgcn_make_buffer_rsrc((void*)(ws + WS_O), (short)0, O_BYTES, 0x00020000);
    auto o_issue = [&](unsigned i) {
      const unsigned sg = (i + soff2) & 15;
      const unsigned head = 8 * hg + 4 * kh + (sg >> 2);
      const unsigned so = (((b * NH + head) * 6 + 3 * rh) * 8 + 2 * (sg & 3)) * 1024u;
      const unsigned dst = ldsb + p2_oslot(i) + kh * 6144u;
#pragma unroll
      for (int q = 0; q < 3; ++q) { dma16<POL_O>(ro, lane * 16, so + q * 8192u, dst + q * 2048u); dma16<POL_O>(ro, lane * 16, so + q * 8192u + 1024u, dst + q * 2048u + 1024u); }
    };
    o_issue(0); o_issue(1);
    STAMPW(10, 22);
#pragma unroll 1
    for (unsigned i = 0; i < 16; ++i) {
      if (i <= 14) wait_vm<6>(); else wait_vm<0>();
      if (i == 0) STAMPW(10, 23);
      bar();
      if (i + 2 < 16) o_issue(i + 2);
    }
  } else if (w >= 8) {
    wo_issue(wvo, 2);
    STAMPW(8, 24);
#pragma unroll 1
    for (unsigned i = 0; i < 16; ++i) {
      if (i <= 13) wait_vm<18>(); else if (i == 14) wait_vm<9>(); else wait_vm<0>();
      if (i == 0) STAMPW(8, 25);
      bar();
      if (i + 3 < 16) wo_issue(wvo, i + 3);
    }
  } else {
    const unsigned c = lane & 15, g = lane >> 4;
    unsigned ba[3];
#pragma unroll
    for (int a = 0; a < 3; ++a) {
      const unsigned qi = 3 * qh + a;
      ba[a] = kh * 6144 + ((qi >> 1) * 2 + (g >> 1)) * 1024 + (16 * (qi & 1) + c + 32 * (g & 1)) * 16;
    }
    const unsigned aa = kh * 9216 + ((g >> 1) * 16 + (g & 1) * 4 + (c >> 2)) * 288 + 5 * nh * 32 + (c & 3) * 8;
    bf16x8 bX[3], aX[5], bY[3], aY[5];
    auto rd = [&](auto NT_, bf16x8 (&bb)[3], bf16x8 (&av)[5], unsigned i) {
      constexpr int NT = decltype(NT_)::value;
      unsigned ob = p2_oslot(i), wb = aa + p2_wslot(i);
      asm volatile("" : "+v"(wb));
#pragma unroll
      for (int a = 0; a < 3; ++a) bb[a] = lds_rd128(lds, ba[a] + ob);
#pragma unroll
      for (int n = 0; n < NT; ++n) av[n] = lds_tr2(lds, wb + n * 32, wb + n * 32 + 2304);
    };
    auto mm = [&](auto NT_, bf16x8 (&bb)[3], bf16x8 (&av)[5]) {
      constexpr int NT = decltype(NT_)::value;
#pragma unroll
      for (int n = 0; n < NT; ++n)
#pragma unroll
        for (int a = 0; a < 3; ++a) acc[a][n] = MFMA16(av[n], bb[a], acc[a][n]);
    };
    unsigned long long t0 = 0;
    auto mix = [&](auto NT_) {
      constexpr int NT = decltype(NT_)::value;
#pragma unroll
      for (int k = 0; k < 3 + 2 * NT - (NT == 4 ? 0 : 0); ++k) {
        __builtin_amdgcn_sched_group_barrier(0x008, 1, 0);
        __builtin_amdgcn_sched_group_barrier(0x100, 1, 0);
      }
      __builtin_amdgcn_sched_group_barrier(0x008, 3 * NT, 0);
    };
    auto gemm = [&](auto NT_) {
      bar();
      STAMP(15);
      rd(NT_, bX, aX, 0);
#pragma unroll 1
      for (unsigned i = 1; i < 15; i += 2) {
        bar(); rd(NT_, bY, aY, i); mm(NT_, bX, aX); mix(NT_);
        bar(); rd(NT_, bX, aX, i + 1); mm(NT_, bY, aY); mix(NT_);
      }
      bar(); rd(NT_, bY, aY, 15); mm(NT_, bX, aX); mix(NT_);
      SB(); mm(NT_, bY, aY);
    };
    if (nh == 0) gemm(IC<5>{}); else gemm(IC<4>{});
    WREC(44, TNOW() - t0);
  }
  STAMP(16);
  bar();
  if (w < 8) {
    const unsigned c = lane & 15, g = lane >> 4;
    const unsigned base = kh * IMG_BYTES + (48 * qh + c) * IMG_STRIDE + (80 * nh + 4 * g) * 4;
#pragma unroll
    for (int a = 0; a < 3; ++a)
#pragma unroll
      for (int n = 0; n < 5; ++n)
        if (n < 4 || nh == 0) *(lds_f32x4*)(lds + base + a * 16 * IMG_STRIDE + n * 64) = acc[a][n];
  }
  bar();
  {
    const __amdgpu_buffer_rsrc_t rp = __builtin_amdgcn_make_buffer_rsrc((void*)(ws + WS_PART), (short)0, PART_BYTES, 0x00020000);
    const unsigned pbase = ((b * 8 + hl) * 4 + hg) * 4 * PART_BLK;
#pragma unroll 1
    for (unsigned n = tid; n < 72 * 18; n += 768) {
      const unsigned rr = n / 18, ch = n - rr * 18;
      unsigned dst = rr / 24; const unsigned row = rr - dst * 24;
      dst += dst >= hg ? 1 : 0;
      const unsigned a0 = (24 * dst + row) * IMG_STRIDE + ch * 32;
      const f32x4 v0 = *(lds_f32x4*)(lds + a0) + *(lds_f32x4*)(lds + IMG_BYTES + a0);
      const f32x4 v1 = *(lds_f32x4*)(lds + a0 + 16) + *(lds_f32x4*)(lds + IMG_BYTES + a0 + 16);
      const u32x4 pk = {cvtpk(v0[0], v0[1]), cvtpk(v0[2], v0[3]), cvtpk(v1[0], v1[1]), cvtpk(v1[2], v1[3])};
      __builtin_amdgcn_raw_buffer_store_b128(pk, rp, dst * PART_BLK + row * 288 + ch * 16 + pbase, 0, 16);
    }
    wait_vm<0>();
    bar();
    if (tid == 0) __hip_atomic_store(flags + (size_t)(256 + wg) * 16, tag, __ATOMIC_RELAXED, __HIP_MEMORY_SCOPE_AGENT);
    STAMP(17);
    if (w == 0) poll_flags(flags, 256 + b * NH + hl, 8, 4, hg, tag, lane);
    bar();
    STAMP(18);
    if (tid < 24 * 18) {
      const unsigned row = tid / 18, ch = tid - row * 18;
      u32x4 u[3];
#pragma unroll
      for (int k = 0; k < 3; ++k) {
        const unsigned src = k + (k >= (int)hg ? 1 : 0);
        u[k] = __builtin_amdgcn_raw_buffer_load_b128(rp, (((b * 8 + hl) * 4 + src) * 4 + hg) * PART_BLK + row * 288 + ch * 16, 0, 16);
      }
      const unsigned a0 = (24 * hg + row) * IMG_STRIDE + ch * 32;
      f32x4 v0 = *(lds_f32x4*)(lds + a0) + *(lds_f32x4*)(lds + IMG_BYTES + a0);
      f32x4 v1 = *(lds_f32x4*)(lds + a0 + 16) + *(lds_f32x4*)(lds + IMG_BYTES + a0 + 16);
#pragma unroll
      for (int k = 0; k < 3; ++k) {
        v0[0] += __uint_as_float(u[k][0] << 16); v0[1] += __uint_as_float(u[k][0] & 0xffff0000u);
        v0[2] += __uint_as_float(u[k][1] << 16); v0[3] += __uint_as_float(u[k][1] & 0xffff0000u);
        v1[0] += __uint_as_float(u[k][2] << 16); v1[1] += __uint_as_float(u[k][2] & 0xffff0000u);
        v1[2] += __uint_as_float(u[k][3] << 16); v1[3] += __uint_as_float(u[k][3] & 0xffff0000u);
      }
      const u32x4 o = {cvtpk(v0[0], v0[1]), cvtpk(v0[2], v0[3]), cvtpk(v1[0], v1[1]), cvtpk(v1[2], v1[3])};
      *(u32x4*)((char*)out + ((size_t)(b * SQ + 96 * rh + 24 * hg + row) * DM + 144 * cq + 8 * ch) * 2) = o;
    }
    if (tid == 0 && hd == 0) __hip_atomic_store(flags + (size_t)(512 + b) * 16, tag, __ATOMIC_RELAXED, __HIP_MEMORY_SCOPE_AGENT);
  }
}

extern "C" void kernel_launch(void* const* d_in, const int* in_sizes, int n_in, void* d_out, int out_size, void* d_ws, size_t ws_size,
                              hipStream_t stream) {
  const __hip_bfloat16* x = (const __hip_bfloat16*)d_in[0];
  const __hip_bfloat16* kc = (const __hip_bfloat16*)d_in[1];
  const __hip_bfloat16* vc = (const __hip_bfloat16*)d_in[2];
  const __hip_bfloat16* wq = (const __hip_bfloat16*)d_in[3];
  const __hip_bfloat16* wo = (const __hip_bfloat16*)d_in[4];
  unsigned long long* diag = (unsigned long long*)((char*)d_ws + (64u << 20));
  mha_kernel<<<256, 768, 0, stream>>>(x, kc, vc, wq, wo, (__hip_bfloat16*)d_out, (char*)d_ws, diag);
}
```

```cpp
#include <hip/hip_runtime.h>
#include <hip/hip_bf16.h>
#include <type_traits>

typedef __bf16 bf16x8 __attribute__((ext_vector_type(8)));
typedef __bf16 bf16x4 __attribute__((ext_vector_type(4)));
typedef __bf16 bf16x2 __attribute__((ext_vector_type(2)));
typedef float f32x16 __attribute__((ext_vector_type(16)));
typedef float f32x4 __attribute__((ext_vector_type(4)));
typedef float f32x2 __attribute__((ext_vector_type(2)));
typedef unsigned u32x4 __attribute__((ext_vector_type(4)));
typedef unsigned u32x2 __attribute__((ext_vector_type(2)));
typedef __attribute__((address_space(3))) char lds_char;
typedef __attribute__((address_space(3))) bf16x4 lds_bf16x4;
typedef __attribute__((address_space(3))) bf16x8 lds_bf16x8;
typedef __attribute__((address_space(3))) f32x4 lds_f32x4;
typedef __attribute__((address_space(3))) f32x2 lds_f32x2;
typedef __attribute__((address_space(1))) unsigned long long gu64;

#define DI __device__ __forceinline__
#define MFMA32(a, b, c) __builtin_amdgcn_mfma_f32_32x32x16_bf16((a), (b), (c), 0, 0, 0)
#define MFMA16(a, b, c) __builtin_amdgcn_mfma_f32_16x16x32_bf16((a), (b), (c), 0, 0, 0)
#define SB() __builtin_amdgcn_sched_barrier(0)
template <int V> using IC = std::integral_constant<int, V>;
#define SG_MFMA(n) __builtin_amdgcn_sched_group_barrier(0x008, (n), 0)
#define SG_DSRD(n) __builtin_amdgcn_sched_group_barrier(0x100, (n), 0)

#define STAMPW(wv, i) do {} while (0)
#define STAMP(i) do {} while (0)
#define WREC(i, v) do {} while (0)
#define TNOW() 0ull

constexpr int NB = 8, SQ = 192, DM = 576, NH = 32, SK = 512, DH = 128;
constexpr float QSCALE = 0.08838834764831845f * 1.4426950408889634f;
constexpr float THR = 8.0f;

constexpr unsigned CODE_BYTES = 26 * 1024;
constexpr size_t WS_O = 0;
constexpr size_t WS_PART = 16u << 20;
constexpr size_t WS_FLAG = 32u << 20;
constexpr unsigned PART_BLK = 24 * 144 * 2;
constexpr unsigned O_BYTES = NB * NH * 6 * 8 * 1024, PART_BYTES = 8 * 8 * 4 * 4 * PART_BLK;

constexpr unsigned QS_BYTES = 40960, QS_W = 24576;
constexpr unsigned SPARE = 155648, LDS_TOTAL = 163840, ML_OFF = 98304, VOTE_OFF = 101376;
constexpr float L_MIN = 9.313225746154785e-10f, L_MAX = 1048576.0f;
DI constexpr unsigned kv_slot(int s) { return (unsigned)((s + 3) & 3) * QS_BYTES; }
DI unsigned qblk(unsigned blk) { return blk < 40 ? 2 * QS_BYTES + blk * 1024 : SPARE + (blk - 40) * 1024; }
constexpr unsigned P2_OSLOT = 12288, P2_WSLOT = 18432, P2_WBASE = 53248, IMG_STRIDE = 592, IMG_BYTES = 96 * IMG_STRIDE;
DI unsigned p2_oslot(unsigned i) { return (i & 3) * P2_OSLOT; }
DI unsigned p2_wslot(unsigned i) { return P2_WBASE + ((i + 3) % 6) * P2_WSLOT; }

DI unsigned rfl(unsigned x) { return __builtin_amdgcn_readfirstlane(x); }
DI unsigned cvtpk(float lo, float hi) { f32x2 v = {lo, hi}; bf16x2 b = __builtin_convertvector(v, bf16x2); return __builtin_bit_cast(unsigned, b); }
DI bf16x8 pack8(const f32x16& x, int s) {
  u32x4 p = {cvtpk(x[8 * s], x[8 * s + 1]), cvtpk(x[8 * s + 2], x[8 * s + 3]), cvtpk(x[8 * s + 4], x[8 * s + 5]), cvtpk(x[8 * s + 6], x[8 * s + 7])};
  return __builtin_bit_cast(bf16x8, p);
}
DI bf16x8 lds_rd128(lds_char* base, unsigned off) { return *(lds_bf16x8*)(base + off); }
DI bf16x8 lds_tr2(lds_char* base, unsigned off0, unsigned off1) {
  bf16x4 lo = __builtin_amdgcn_ds_read_tr16_b64_v4bf16((lds_bf16x4*)(base + off0));
  bf16x4 hi = __builtin_amdgcn_ds_read_tr16_b64_v4bf16((lds_bf16x4*)(base + off1));
  return __builtin_shufflevector(lo, hi, 0, 1, 2, 3, 4, 5, 6, 7);
}
template <int POL> DI void dma16(__amdgpu_buffer_rsrc_t r, unsigned voff, unsigned soff, unsigned m0v) {
  if constexpr (POL == 0) asm volatile("s_mov_b32 m0, %3\n\ts_nop 0\n\tbuffer_load_dwordx4 %0, %1, %2 offen lds" ::"v"(voff), "s"(r), "s"(soff), "s"(m0v) : "memory");
  else if constexpr (POL == 1) asm volatile("s_mov_b32 m0, %3\n\ts_nop 0\n\tbuffer_load_dwordx4 %0, %1, %2 offen nt lds" ::"v"(voff), "s"(r), "s"(soff), "s"(m0v) : "memory");
  else asm volatile("s_mov_b32 m0, %3\n\ts_nop 0\n\tbuffer_load_dwordx4 %0, %1, %2 offen sc1 lds" ::"v"(voff), "s"(r), "s"(soff), "s"(m0v) : "memory");
}
template <int N> DI void wait_vm() { asm volatile("s_waitcnt vmcnt(%0)" ::"i"(N) : "memory"); }
DI void bar() { SB(); asm volatile("s_waitcnt lgkmcnt(0)\n\ts_barrier" ::: "memory"); SB(); }
DI float pl32_max(float v) {
  auto r = __builtin_amdgcn_permlane32_swap(__float_as_uint(v), __float_as_uint(v), false, false);
  return fmaxf(__uint_as_float(r[0]), __uint_as_float(r[1]));
}
DI float pl32_sum(float v) {
  auto r = __builtin_amdgcn_permlane32_swap(__float_as_uint(v), __float_as_uint(v), false, false);
  return __uint_as_float(r[0]) + __uint_as_float(r[1]);
}
DI unsigned lane_id() { unsigned l; asm volatile("v_mbcnt_lo_u32_b32 %0, -1, 0\n\tv_mbcnt_hi_u32_b32 %0, -1, %0" : "=v"(l)); return l; }
DI void poll_flags(gu64* flags, unsigned idx0, unsigned stride, unsigned n, unsigned skip, unsigned long long tag, unsigned lane) {
  const bool mine = lane < n && lane != skip;
  gu64* p = flags + (size_t)(idx0 + (mine ? lane : 0) * stride) * 16;
  for (unsigned spins = 0; spins < 400000u; ++spins) {
    const unsigned long long v = __hip_atomic_load(p, __ATOMIC_RELAXED, __HIP_MEMORY_SCOPE_AGENT);
    if (__all(!mine || v == tag)) break;
    __builtin_amdgcn_s_sleep(1);
  }
}

#define OUT_AUX 2
#define ROTMODE 2
#define POL_X 0
#define POL_WQ 0
#define O_ST_AUX 16
#define POL_O 2
#define POL_WO 0

__global__ __launch_bounds__(768, 3) void mha_kernel(const __hip_bfloat16* __restrict__ x, const __hip_bfloat16* __restrict__ kc,
                                                     const __hip_bfloat16* __restrict__ vc, const __hip_bfloat16* __restrict__ wq,
                                                     const __hip_bfloat16* __restrict__ wo, __hip_bfloat16* __restrict__ out,
                                                     char* __restrict__ ws, unsigned long long* diag) {
  __shared__ __attribute__((aligned(1024))) char smem[LDS_TOTAL];
  lds_char* lds = (lds_char*)smem;
  const unsigned ldsb = (unsigned)(size_t)smem;
  const unsigned tid = threadIdx.x, w = rfl(tid >> 6);
  STAMP(0);
  const unsigned xcd = blockIdx.x & 7, jj = blockIdx.x >> 3;
  const unsigned b = 4 * (xcd & 1) + (jj & 3), hd = 8 * (xcd >> 1) + (jj >> 2);
  gu64* flags = (gu64*)(ws + WS_FLAG);
  unsigned long long epoch;
  auto read_epoch = [&]() {
    asm volatile("s_load_dwordx2 %0, %1, 0x0\n\ts_waitcnt lgkmcnt(0)" : "=s"(epoch) : "s"((const char*)(ws + WS_FLAG) + (512 + b) * 128) : "memory");
  };

  const __amdgpu_buffer_rsrc_t rwo = __builtin_amdgcn_make_buffer_rsrc((void*)wo, (short)0, NH * DH * DM * 2, 0x00020000);
  bf16x8 qf[8];
  f32x16 oacc[4];
  float m, l;
  {
    const unsigned lane = tid & 63, r = lane & 31, h = lane >> 5;
    const unsigned t = w >> 1, j = w & 1;
    const unsigned g1 = (lane >> 4) & 1, qq = (lane & 15) >> 2, pp = lane & 3, sp = ((pp & 1) << 1) | (pp >> 1);
    const __amdgpu_buffer_rsrc_t rk = __builtin_amdgcn_make_buffer_rsrc((void*)kc, (short)0, NB * NH * SK * DH * 2, 0x00020000);
    const __amdgpu_buffer_rsrc_t rv = __builtin_amdgcn_make_buffer_rsrc((void*)vc, (short)0, NB * NH * SK * DH * 2, 0x00020000);
    const unsigned skv = (b * NH + hd) * (SK * DH * 2);
    const unsigned vk = (lane >> 4) * 256 + ((lane & 15) ^ (lane >> 4)) * 16;
    const unsigned vv = (lane >> 4) * 256 + ((lane & 15) ^ ((lane >> 4) << 2)) * 16;

    auto kv4 = [&](int s) {
      const unsigned lw = w - 4;
      const unsigned dst = ldsb + kv_slot(s) + lw * 4096u;
      const unsigned so = skv + ((lw >> 1) & 1) * 65536u + (lw & 1) * 4096u + (unsigned)s * 8192u;
#pragma unroll
      for (int p = 0; p < 4; ++p) {
        if (lw < 4) dma16<1>(rk, vk ^ (p << 6), so + p * 1024u, dst + p * 1024u);
        else dma16<1>(rv, vv, so + p * 1024u, dst + p * 1024u);
      }
    };
    const unsigned koff = 3 * (((jj >> 2) + (jj & 3)) % 3);
    if (w >= 4) {
      const unsigned lw = w - 4;
      const __amdgpu_buffer_rsrc_t rx = __builtin_amdgcn_make_buffer_rsrc((void*)x, (short)0, NB * SQ * DM * 2, 0x00020000);
      const __amdgpu_buffer_rsrc_t rw = __builtin_amdgcn_make_buffer_rsrc((void*)wq, (short)0, DM * NH * DH * 2, 0x00020000);
      const unsigned vx = (lane >> 3) * 1152 + (((lane & 7) ^ (lane >> 4) ^ ((lw & 1) << 2)) * 16);
      const unsigned sx = (b * SQ + 8 * lw) * 1152u;
      const unsigned vw = (lane >> 4) * 8192 + (((lane & 15) ^ ((lane >> 4) << 2)) * 16);
      const unsigned sw = 4 * lw * 8192u + hd * 256u;
      auto issue_q = [&](int i) {
        unsigned st = koff + i; st = st >= 9 ? st - 9 : st;
        const unsigned slot = ldsb + (unsigned)(i % 3) * QS_BYTES;
#pragma unroll
        for (int p = 0; p < 3; ++p) dma16<POL_X>(rx, vx, sx + (unsigned)(64 * p) * 1152u + st * 128u, slot + (lw + 8 * p) * 1024u);
#pragma unroll
        for (int p = 0; p < 2; ++p) dma16<POL_WQ>(rw, vw, sw + (st * 64 + 32 * p) * 8192u, slot + QS_W + (lw + 8 * p) * 1024u);
      };
      issue_q(0); issue_q(1);
      wait_vm<5>(); bar(); issue_q(2);
      wait_vm<5>(); bar(); issue_q(3);
      wait_vm<5>(); bar(); issue_q(4); kv4(0);
      wait_vm<9>(); bar(); issue_q(5);
      wait_vm<9>(); bar(); issue_q(6);
      wait_vm<5>(); bar(); issue_q(7);
      wait_vm<5>(); bar(); issue_q(8);
      wait_vm<5>(); bar(); kv4(1);
      wait_vm<4>(); bar(); kv4(2);
      read_epoch();
      bar();
      bar();
    } else {
      read_epoch();
      if (w < 2) {
        unsigned long long pc;
        asm volatile("s_getpc_b64 %0" : "=s"(pc));
        const unsigned off = (jj * 2 + w) * 1024u;
        if (off + 1024u <= CODE_BYTES) {
          u32x4 t;
          const char* p = (const char*)(pc & ~127ull) + off + lane * 16;
          asm volatile("global_load_dwordx4 %0, %1, off\n\ts_waitcnt vmcnt(0)" : "=v"(t) : "v"(p) : "memory");
        }
      }
      const unsigned qh = w >> 1, dh = w & 1;
      const unsigned xa0 = (96 * qh + r) * 128 + ((h ^ ((r >> 1) & 7)) << 4);
      const unsigned wa0 = QS_W + (8 * h + qq) * 256 + ((4 * ((2 * dh) ^ qq) + 2 * g1 + (sp >> 1)) << 4) + (sp & 1) * 8;
      f32x16 qacc[3][2];
#pragma unroll
      for (int a = 0; a < 3; ++a)
#pragma unroll
        for (int i = 0; i < 16; ++i) { qacc[a][0][i] = 0.f; qacc[a][1][i] = 0.f; }
      bf16x8 bA[3], aA[2], bB[3], aB[2];
      unsigned xb, wb;
      auto rd = [&](bf16x8 (&bb)[3], bf16x8 (&aa)[2], int s) {
        const unsigned xs = xb ^ (s << 5), wb1 = wb ^ 64;
        aa[0] = lds_tr2(lds, wb + s * 4096, wb + s * 4096 + 1024);
        bb[0] = lds_rd128(lds, xs); bb[1] = lds_rd128(lds, xs + 4096); bb[2] = lds_rd128(lds, xs + 8192);
        aa[1] = lds_tr2(lds, wb1 + s * 4096, wb1 + s * 4096 + 1024);
      };
      auto mm = [&](bf16x8 (&bb)[3], bf16x8 (&aa)[2]) {
        qacc[0][0] = MFMA32(aa[0], bb[0], qacc[0][0]);
        qacc[1][0] = MFMA32(aa[0], bb[1], qacc[1][0]);
        qacc[2][0] = MFMA32(aa[0], bb[2], qacc[2][0]);
        qacc[0][1] = MFMA32(aa[1], bb[0], qacc[0][1]);
        qacc[1][1] = MFMA32(aa[1], bb[1], qacc[1][1]);
        qacc[2][1] = MFMA32(aa[1], bb[2], qacc[2][1]);
      };
      bar();
      STAMP(1);
      xb = xa0; wb = wa0;
      asm volatile("" : "+v"(xb), "+v"(wb));
      rd(bA, aA, 0);
      auto mix = [&]() { SG_MFMA(1); SG_DSRD(2); SG_MFMA(1); SG_DSRD(2); SG_MFMA(1); SG_DSRD(1); SG_MFMA(1); SG_DSRD(1); SG_MFMA(1); SG_DSRD(1); SG_MFMA(1); SB(); };
#pragma unroll 1
      for (int st = 0; st < 8; ++st) {
        SB(); rd(bB, aB, 1); mm(bA, aA); mix();
        rd(bA, aA, 2); mm(bB, aB); mix();
        rd(bB, aB, 3); mm(bA, aA); mix();
        bar();
        const unsigned so = (unsigned)((st + 1) % 3) * QS_BYTES;
        xb = xa0 + so; wb = wa0 + so;
        asm volatile("" : "+v"(xb), "+v"(wb));
        rd(bA, aA, 0); mm(bB, aB); mix();
      }
      SB(); rd(bB, aB, 1); mm(bA, aA); mix();
      rd(bA, aA, 2); mm(bB, aB); mix();
      rd(bB, aB, 3); mm(bA, aA); mix();
      mm(bB, aB);
      bar();
      STAMP(4);
#pragma unroll
      for (int a = 0; a < 3; ++a)
#pragma unroll
        for (int d = 0; d < 2; ++d) {
#pragma unroll
          for (int i = 0; i < 16; ++i) qacc[a][d][i] *= QSCALE;
#pragma unroll
          for (int s2 = 0; s2 < 2; ++s2) {
            const unsigned blk = (3 * qh + a) * 8 + 4 * dh + 2 * d + s2;
            *(lds_bf16x8*)(lds + qblk(blk) + lane * 16) = pack8(qacc[a][d], s2);
          }
        }
      bar();
    }
#pragma unroll
    for (int i = 0; i < 8; ++i) qf[i] = lds_rd128(lds, qblk(t * 8 + i) + lane * 16);

    const unsigned grp = w >> 2;
    float l0, l1;
    bool safe = false;
    const unsigned ka0 = j * 8192 + r * 256 + ((h ^ (r & 15)) << 4);
    const unsigned va0 = (16384 + j * 8192 + (4 * h + qq) * 256 + ((4 * qq + 2 * g1 + (sp >> 1)) << 4) + (sp & 1) * 8) ^ (j << 7);
    const unsigned vkx = vk ^ ((w & 3) << 6);
    const unsigned sp0 = skv + ((w >> 3) & 1) * 65536u + (w & 7) * 1024u;
    const unsigned sp1 = skv + (((w + 12) >> 3) & 1) * 65536u + ((w + 12) & 7) * 1024u;
    const unsigned sp2 = skv + (((w + 24) >> 3) & 1) * 65536u + ((w + 24) & 7) * 1024u;

    bf16x8 kf[4];
    auto step = [&](auto G_, int s) {
      constexpr int G = decltype(G_)::value;
      constexpr int NP = G == 2 ? 2 : 3;
      unsigned vb = va0 + kv_slot(s), kc0 = ka0 + kv_slot(s), kb = ka0 + kv_slot(s + 1);
      asm volatile("" : "+v"(kb), "+v"(vb), "+v"(kc0));
      __builtin_amdgcn_s_setprio(1);
      f32x16 sc;
      bf16x8 vf[4][2];
      {
        bf16x8 kg[4];
#pragma unroll
        for (int ks = 0; ks < 4; ++ks) kg[ks] = lds_rd128(lds, kc0 ^ ((ks + 4) << 5));
#pragma unroll
        for (int i = 0; i < 16; ++i) sc[i] = 0.f;
#pragma unroll
        for (int ks = 0; ks < 4; ++ks) sc = MFMA32(kf[ks], qf[ks], sc);
#pragma unroll
        for (int d = 0; d < 4; ++d) { const unsigned va = vb ^ (d << 6); vf[d][0] = lds_tr2(lds, va, va + 2048); }
#pragma unroll
        for (int ks = 0; ks < 4; ++ks) sc = MFMA32(kg[ks], qf[ks + 4], sc);
        SG_MFMA(1); SG_DSRD(2); SG_MFMA(1); SG_DSRD(2); SG_MFMA(1); SG_DSRD(2); SG_MFMA(1); SG_DSRD(2);
        SG_MFMA(1); SG_DSRD(2); SG_MFMA(1); SG_DSRD(2); SG_MFMA(2);
        SB();
        asm volatile("" : "+v"(sc));
      }
      __builtin_amdgcn_s_setprio(0);
      if constexpr (G == 1) { if (s <= 5) wait_vm<NP>(); else if (s == 6) wait_vm<0>(); }
      bar();
#pragma unroll
      for (int d = 0; d < 4; ++d) { const unsigned va = vb ^ (d << 6); vf[d][1] = lds_tr2(lds, va + 4096, va + 4096 + 2048); }
      SB();
      if (safe) {
        const float t0 = fmaxf(fmaxf(sc[0], sc[1]), sc[2]), t1 = fmaxf(fmaxf(sc[3], sc[4]), sc[5]), t2 = fmaxf(fmaxf(sc[6], sc[7]), sc[8]);
        const float t3 = fmaxf(fmaxf(sc[9], sc[10]), sc[11]), t4 = fmaxf(fmaxf(sc[12], sc[13]), sc[14]);
        float tm = fmaxf(fmaxf(fmaxf(t0, t1), t2), fmaxf(fmaxf(t3, t4), sc[15]));
        tm = pl32_max(tm);
        if (__any(tm > m + THR)) {
          const float mn = fmaxf(m, tm);
          const float al = __builtin_amdgcn_exp2f(m - mn);
          l0 *= al; l1 *= al; m = mn;
#pragma unroll
          for (int d = 0; d < 4; ++d)
#pragma unroll
            for (int i = 0; i < 16; ++i) oacc[d][i] *= al;
        }
#pragma unroll
        for (int i = 0; i < 16; ++i) sc[i] -= m;
      }
      SB();
#pragma unroll
      for (int i = 0; i < 16; ++i) sc[i] = __builtin_amdgcn_exp2f(sc[i]);
      bf16x8 p0 = pack8(sc, 0), p1 = pack8(sc, 1);
#pragma unroll
      for (int i = 0; i < 16; i += 2) { l0 += sc[i]; l1 += sc[i + 1]; }
      asm volatile("" : "+v"(p0), "+v"(p1), "+v"(l0), "+v"(l1));
      if constexpr (G == 0) { if (s <= 5) wait_vm<NP>(); else if (s == 6) wait_vm<0>(); }
      bar();
      __builtin_amdgcn_s_setprio(1);
#pragma unroll
      for (int d = 0; d < 4; ++d) oacc[d] = MFMA32(vf[d][0], p0, oacc[d]);
#pragma unroll
      for (int ks = 0; ks < 4; ++ks) kf[ks] = lds_rd128(lds, kb ^ (ks << 5));
#pragma unroll
      for (int d = 0; d < 4; ++d) oacc[d] = MFMA32(vf[d][1], p1, oacc[d]);
      SG_MFMA(2); SG_DSRD(1); SG_MFMA(1); SG_DSRD(1); SG_MFMA(1); SG_DSRD(1); SG_MFMA(1); SG_DSRD(1); SG_MFMA(3);
      SB();
      asm volatile("" : "+v"(oacc[0]), "+v"(oacc[1]), "+v"(oacc[2]), "+v"(oacc[3]));
      __builtin_amdgcn_s_setprio(0);
      SB();
      if (s <= 4) {
        const unsigned dst = ldsb + kv_slot(s + 3) + w * 1024u, sofs = (unsigned)(s + 3) * 8192u;
        dma16<1>(rk, vkx, sp0 + sofs, dst);
        if constexpr (G == 0) dma16<1>(rk, vkx, sp1 + sofs, dst + 12288u); else dma16<1>(rv, vv, sp1 + sofs, dst + 12288u);
        if constexpr (G != 2) dma16<1>(rv, vv, sp2 + sofs, dst + 24576u);
      }
      if constexpr (G == 2) { if (s <= 4) wait_vm<NP>(); else if (s == 5) wait_vm<0>(); }
      if (s >= 6 && !safe) {
        const unsigned pc = ((b & 3) * 8 + (hd & 7)) * 36 + w * 3 + (s - 6) * 2;
        const unsigned so = (hd >> 3) * (1024u * 1152u) + pc * 1024u;
        dma16<POL_WO>(rwo, lane * 16, so, ldsb + SPARE + (w & 7) * 1024u);
        if (s == 6) dma16<POL_WO>(rwo, lane * 16, so + 1024u, ldsb + SPARE + (w & 7) * 1024u);
      }
      bar();
    };
    auto run = [&](auto G_) {
      constexpr int G = decltype(G_)::value;
      {
        unsigned kb0 = ka0 + kv_slot(0);
        asm volatile("" : "+v"(kb0));
#pragma unroll
        for (int ks = 0; ks < 4; ++ks) kf[ks] = lds_rd128(lds, kb0 ^ (ks << 5));
      }
      if constexpr (G >= 1) bar();
      if constexpr (G == 2) { wait_vm<4>(); bar(); }
#pragma unroll 1
      for (int s = 0; s < 8; ++s) step(G_, s);
      if constexpr (G == 0) bar();
      if constexpr (G <= 1) bar();
    };
#pragma unroll 1
    for (;;) {
#pragma unroll
      for (int d = 0; d < 4; ++d)
#pragma unroll
        for (int i = 0; i < 16; ++i) oacc[d][i] = 0.f;
      m = safe ? -__builtin_inff() : 0.f; l0 = 0.f; l1 = 0.f;
      bar(); STAMP(5);
      if (grp == 0) run(IC<0>{});
      else if (grp == 1) run(IC<1>{});
      else run(IC<2>{});
      STAMP(13);
      const unsigned ln = lane_id();
      l = pl32_sum(l0 + l1);
      const unsigned xw = w * 4096 + ln * 16;
#pragma unroll
      for (int i = 0; i < 4; ++i) *(lds_bf16x8*)(lds + xw + i * 1024) = pack8(oacc[2 + (i >> 1)], i & 1);
      if (ln < 32) { f32x2 ml = {m, l}; *(lds_f32x2*)(lds + ML_OFF + w * 256 + ln * 8) = ml; }
      const bool bad = __any(!(l > L_MIN && l < L_MAX));
      if (ln == 0) *(__attribute__((address_space(3))) unsigned*)(lds + VOTE_OFF + w * 4) = bad ? 1u : 0u;
      bar();
      STAMP(6);
      if (safe) break;
      const unsigned vote = *(__attribute__((address_space(3))) unsigned*)(lds + VOTE_OFF + (ln < 12 ? ln : 0) * 4);
      if (!__any(vote != 0)) break;
      safe = true;
      bar();
      if (w >= 4) {
        const unsigned lw = rfl(w - 4), sk = rfl((b * NH + hd) * (SK * DH * 2));
        const unsigned vo = (ln >> 4) * 256 + (lw < 4 ? ((ln & 15) ^ (ln >> 4)) : ((ln & 15) ^ ((ln >> 4) << 2))) * 16;
        const __amdgpu_buffer_rsrc_t rr = __builtin_amdgcn_make_buffer_rsrc((void*)(lw < 4 ? kc : vc), (short)0, NB * NH * SK * DH * 2, 0x00020000);
#pragma unroll 1
        for (unsigned q = 0; q < 12; ++q) {
          const unsigned st = q >> 2, p = q & 3;
          dma16<1>(rr, lw < 4 ? vo ^ (p << 6) : vo, rfl(sk + ((lw >> 1) & 1) * 65536u + (lw & 1) * 4096u + st * 8192u + p * 1024u), rfl(ldsb + kv_slot(st) + lw * 4096u + p * 1024u));
        }
        wait_vm<0>();
      }
    }
  }

  const unsigned lane = lane_id();
  const unsigned hg = hd >> 3, hl = hd & 7, rh = hl & 1, cq = hl >> 1;
  const unsigned soff2 = (4 * cq + 2 * (b & 3) + 8 * rh) & 15;
  const unsigned long long tag = epoch + 1;
  const unsigned wg = b * NH + hd;
  auto wo_voff = [&](int i) {
    const unsigned n = 64 * i + lane, p = n / 18, ch = n - 18 * p;
    const unsigned k = (p & 19) | ((p & 4) << 1) | ((p & 8) >> 1);
    return k * 1152 + ch * 16;
  };
  auto wo_issue = [&](const unsigned (&vo)[9], unsigned i) {
    const unsigned sg = (i + soff2) & 15, kh = w & 1;
    const unsigned so = ((8 * hg + 4 * kh + (sg >> 2)) * 128 + (sg & 3) * 32) * 1152u + cq * 288u;
    const unsigned dst = ldsb + p2_wslot(i) + kh * 9216u;
#pragma unroll
    for (int p = 0; p < 9; ++p) dma16<POL_WO>(rwo, vo[p], so, dst + p * 1024u);
  };
  unsigned wvo[9];
  if (w == 8 || w == 9) {
#pragma unroll
    for (int p = 0; p < 9; ++p) wvo[p] = wo_voff(p);
  }

  {
    const unsigned r = lane & 31, h = lane >> 5, t = w >> 1, j = w & 1;
    f32x2 o = *(lds_f32x2*)(lds + ML_OFF + (w ^ 1) * 256 + r * 8);
    const float mn = fmaxf(m, o[0]);
    const float f = __builtin_amdgcn_exp2f(m - mn), fo = __builtin_amdgcn_exp2f(o[0] - mn);
    const float inv = 1.0f / (l * f + o[1] * fo);
    const float sa = f * inv, sb = fo * inv;
    const unsigned xr = (w ^ 1) * 4096 + lane * 16;
#pragma unroll
    for (int i = 0; i < 4; ++i) {
      const u32x4 v = __builtin_bit_cast(u32x4, lds_rd128(lds, xr + i * 1024));
#pragma unroll
      for (int e = 0; e < 4; ++e) {
        oacc[i >> 1][8 * (i & 1) + 2 * e] = oacc[i >> 1][8 * (i & 1) + 2 * e] * sa + __uint_as_float(v[e] << 16) * sb;
        oacc[i >> 1][8 * (i & 1) + 2 * e + 1] = oacc[i >> 1][8 * (i & 1) + 2 * e + 1] * sa + __uint_as_float(v[e] & 0xffff0000u) * sb;
      }
    }
    const __amdgpu_buffer_rsrc_t ro = __builtin_amdgcn_make_buffer_rsrc((void*)(ws + WS_O), (short)0, O_BYTES, 0x00020000);
    const unsigned so = ((wg * 6 + t) * 8 + 4 * j) * 1024u;
#pragma unroll
    for (int i = 0; i < 4; ++i)
      __builtin_amdgcn_raw_buffer_store_b128(__builtin_bit_cast(u32x4, pack8(oacc[i >> 1], i & 1)), ro, so + lane * 16 + i * 1024, 0, O_ST_AUX);
    STAMP(7);
    if (w == 8 || w == 9) { wo_issue(wvo, 0); wo_issue(wvo, 1); wait_vm<18>(); } else wait_vm<0>();
    STAMP(8);
    bar();
    if (tid == 0) __hip_atomic_store(flags + (size_t)wg * 16, tag, __ATOMIC_RELAXED, __HIP_MEMORY_SCOPE_AGENT);
  }
  STAMP(14);

  f32x4 acc[3][5];
#pragma unroll
  for (int a = 0; a < 3; ++a)
#pragma unroll
    for (int n = 0; n < 5; ++n) acc[a][n] = f32x4{0.f, 0.f, 0.f, 0.f};
  const unsigned nh = (w >> 2) & 1, kh = w & 1, qh = (w >> 1) & 1;
  if (w >= 10) {
    poll_flags(flags, b * NH + 8 * hg, 1, 8, 64, tag, lane);
    STAMPW(10, 20);
    const __amdgpu_buffer_rsrc_t ro = __builtin_amdgcn_make_buffer_rsrc((void*)(ws + WS_O), (short)0, O_BYTES, 0x00020000);
    auto o_issue = [&](unsigned i) {
      const unsigned sg = (i + soff2) & 15;
      const unsigned head = 8 * hg + 4 * kh + (sg >> 2);
      const unsigned so = (((b * NH + head) * 6 + 3 * rh) * 8 + 2 * (sg & 3)) * 1024u;
      const unsigned dst = ldsb + p2_oslot(i) + kh * 6144u;
#pragma unroll
      for (int q = 0; q < 3; ++q) { dma16<POL_O>(ro, lane * 16, so + q * 8192u, dst + q * 2048u); dma16<POL_O>(ro, lane * 16, so + q * 8192u + 1024u, dst + q * 2048u + 1024u); }
    };
    o_issue(0); o_issue(1);
    STAMPW(10, 22);
#pragma unroll 1
    for (unsigned i = 0; i < 16; ++i) {
      if (i <= 14) wait_vm<6>(); else wait_vm<0>();
      if (i == 0) STAMPW(10, 23);
      bar();
      if (i + 2 < 16) o_issue(i + 2);
    }
  } else if (w >= 8) {
    wo_issue(wvo, 2); wo_issue(wvo, 3); wo_issue(wvo, 4);
#pragma unroll 1
    for (unsigned i = 0; i < 16; ++i) {
      if (i <= 11) wait_vm<36>(); else if (i == 12) wait_vm<27>(); else if (i == 13) wait_vm<18>(); else if (i == 14) wait_vm<9>(); else wait_vm<0>();
      bar();
      if (i + 5 < 16) wo_issue(wvo, i + 5);
    }
  } else {
    const unsigned c = lane & 15, g = lane >> 4;
    unsigned ba[3];
#pragma unroll
    for (int a = 0; a < 3; ++a) {
      const unsigned qi = 3 * qh + a;
      ba[a] = kh * 6144 + ((qi >> 1) * 2 + (g >> 1)) * 1024 + (16 * (qi & 1) + c + 32 * (g & 1)) * 16;
    }
    const unsigned aa = kh * 9216 + ((g >> 1) * 16 + (g & 1) * 4 + (c >> 2)) * 288 + 5 * nh * 32 + (c & 3) * 8;
    bf16x8 bX[3], aX[5], bY[3], aY[5];
    auto rd = [&](auto NT_, bf16x8 (&bb)[3], bf16x8 (&av)[5], unsigned i) {
      constexpr int NT = decltype(NT_)::value;
      unsigned ob = p2_oslot(i), wb = aa + p2_wslot(i);
      asm volatile("" : "+v"(wb));
#pragma unroll
      for (int a = 0; a < 3; ++a) bb[a] = lds_rd128(lds, ba[a] + ob);
#pragma unroll
      for (int n = 0; n < NT; ++n) av[n] = lds_tr2(lds, wb + n * 32, wb + n * 32 + 2304);
    };
    auto mm = [&](auto NT_, bf16x8 (&bb)[3], bf16x8 (&av)[5]) {
      constexpr int NT = decltype(NT_)::value;
#pragma unroll
      for (int n = 0; n < NT; ++n)
#pragma unroll
        for (int a = 0; a < 3; ++a) acc[a][n] = MFMA16(av[n], bb[a], acc[a][n]);
    };
    unsigned long long t0 = 0;
    auto mix = [&](auto NT_) {
      constexpr int NT = decltype(NT_)::value;
#pragma unroll
      for (int k = 0; k < 3 + 2 * NT - (NT == 4 ? 0 : 0); ++k) {
        __builtin_amdgcn_sched_group_barrier(0x008, 1, 0);
        __builtin_amdgcn_sched_group_barrier(0x100, 1, 0);
      }
      __builtin_amdgcn_sched_group_barrier(0x008, 3 * NT, 0);
    };
    auto gemm = [&](auto NT_) {
      bar();
      STAMP(15);
      rd(NT_, bX, aX, 0);
#pragma unroll 1
      for (unsigned i = 1; i < 15; i += 2) {
        bar(); rd(NT_, bY, aY, i); mm(NT_, bX, aX); mix(NT_);
        bar(); rd(NT_, bX, aX, i + 1); mm(NT_, bY, aY); mix(NT_);
      }
      bar(); rd(NT_, bY, aY, 15); mm(NT_, bX, aX); mix(NT_);
      SB(); mm(NT_, bY, aY);
    };
    if (nh == 0) gemm(IC<5>{}); else gemm(IC<4>{});
    WREC(44, TNOW() - t0);
  }
  STAMP(16);
  bar();
  if (w < 8) {
    const unsigned c = lane & 15, g = lane >> 4;
    const unsigned base = kh * IMG_BYTES + (48 * qh + c) * IMG_STRIDE + (80 * nh + 4 * g) * 4;
#pragma unroll
    for (int a = 0; a < 3; ++a)
#pragma unroll
      for (int n = 0; n < 5; ++n)
        if (n < 4 || nh == 0) *(lds_f32x4*)(lds + base + a * 16 * IMG_STRIDE + n * 64) = acc[a][n];
  }
  bar();
  {
    const __amdgpu_buffer_rsrc_t rp = __builtin_amdgcn_make_buffer_rsrc((void*)(ws + WS_PART), (short)0, PART_BYTES, 0x00020000);
    const unsigned pbase = ((b * 8 + hl) * 4 + hg) * 4 * PART_BLK;
#pragma unroll 1
    for (unsigned n = tid; n < 72 * 18; n += 768) {
      const unsigned rr = n / 18, ch = n - rr * 18;
      unsigned dst = rr / 24; const unsigned row = rr - dst * 24;
      dst += dst >= hg ? 1 : 0;
      const unsigned a0 = (24 * dst + row) * IMG_STRIDE + ch * 32;
      const f32x4 v0 = *(lds_f32x4*)(lds + a0) + *(lds_f32x4*)(lds + IMG_BYTES + a0);
      const f32x4 v1 = *(lds_f32x4*)(lds + a0 + 16) + *(lds_f32x4*)(lds + IMG_BYTES + a0 + 16);
      const u32x4 pk = {cvtpk(v0[0], v0[1]), cvtpk(v0[2], v0[3]), cvtpk(v1[0], v1[1]), cvtpk(v1[2], v1[3])};
      __builtin_amdgcn_raw_buffer_store_b128(pk, rp, dst * PART_BLK + row * 288 + ch * 16 + pbase, 0, 16);
    }
    wait_vm<0>();
    bar();
    if (tid == 0) __hip_atomic_store(flags + (size_t)(256 + wg) * 16, tag, __ATOMIC_RELAXED, __HIP_MEMORY_SCOPE_AGENT);
    STAMP(17);
    if (w == 0) poll_flags(flags, 256 + b * NH + hl, 8, 4, hg, tag, lane);
    bar();
    STAMP(18);
    if (tid < 24 * 18) {
      const unsigned row = tid / 18, ch = tid - row * 18;
      u32x4 u[3];
#pragma unroll
      for (int k = 0; k < 3; ++k) {
        const unsigned src = k + (k >= (int)hg ? 1 : 0);
        u[k] = __builtin_amdgcn_raw_buffer_load_b128(rp, (((b * 8 + hl) * 4 + src) * 4 + hg) * PART_BLK + row * 288 + ch * 16, 0, 16);
      }
      const unsigned a0 = (24 * hg + row) * IMG_STRIDE + ch * 32;
      f32x4 v0 = *(lds_f32x4*)(lds + a0) + *(lds_f32x4*)(lds + IMG_BYTES + a0);
      f32x4 v1 = *(lds_f32x4*)(lds + a0 + 16) + *(lds_f32x4*)(lds + IMG_BYTES + a0 + 16);
#pragma unroll
      for (int k = 0; k < 3; ++k) {
        v0[0] += __uint_as_float(u[k][0] << 16); v0[1] += __uint_as_float(u[k][0] & 0xffff0000u);
        v0[2] += __uint_as_float(u[k][1] << 16); v0[3] += __uint_as_float(u[k][1] & 0xffff0000u);
        v1[0] += __uint_as_float(u[k][2] << 16); v1[1] += __uint_as_float(u[k][2] & 0xffff0000u);
        v1[2] += __uint_as_float(u[k][3] << 16); v1[3] += __uint_as_float(u[k][3] & 0xffff0000u);
      }
      const u32x4 o = {cvtpk(v0[0], v0[1]), cvtpk(v0[2], v0[3]), cvtpk(v1[0], v1[1]), cvtpk(v1[2], v1[3])};
      const __amdgpu_buffer_rsrc_t rout = __builtin_amdgcn_make_buffer_rsrc((void*)out, (short)0, NB * SQ * DM * 2, 0x00020000);
      __builtin_amdgcn_raw_buffer_store_b128(o, rout, ((b * SQ + 96 * rh + 24 * hg + row) * DM + 144 * cq + 8 * ch) * 2, 0, OUT_AUX);
    }
    if (tid == 0 && hd == 0) __hip_atomic_store(flags + (size_t)(512 + b) * 16, tag, __ATOMIC_RELAXED, __HIP_MEMORY_SCOPE_AGENT);
  }
}

extern "C" void kernel_launch(void* const* d_in, const int* in_sizes, int n_in, void* d_out, int out_size, void* d_ws, size_t ws_size,
                              hipStream_t stream) {
  const __hip_bfloat16* x = (const __hip_bfloat16*)d_in[0];
  const __hip_bfloat16* kc = (const __hip_bfloat16*)d_in[1];
  const __hip_bfloat16* vc = (const __hip_bfloat16*)d_in[2];
  const __hip_bfloat16* wq = (const __hip_bfloat16*)d_in[3];
  const __hip_bfloat16* wo = (const __hip_bfloat16*)d_in[4];
  unsigned long long* diag = (unsigned long long*)((char*)d_ws + (64u << 20));
  mha_kernel<<<256, 768, 0, stream>>>(x, kc, vc, wq, wo, (__hip_bfloat16*)d_out, (char*)d_ws, diag);
}
```

```cpp
#include <hip/hip_runtime.h>
#include <hip/hip_bf16.h>
#include <type_traits>

typedef __bf16 bf16x8 __attribute__((ext_vector_type(8)));
typedef __bf16 bf16x4 __attribute__((ext_vector_type(4)));
typedef __bf16 bf16x2 __attribute__((ext_vector_type(2)));
typedef float f32x16 __attribute__((ext_vector_type(16)));
typedef float f32x4 __attribute__((ext_vector_type(4)));
typedef float f32x2 __attribute__((ext_vector_type(2)));
typedef unsigned u32x4 __attribute__((ext_vector_type(4)));
typedef unsigned u32x2 __attribute__((ext_vector_type(2)));
typedef __attribute__((address_space(3))) char lds_char;
typedef __attribute__((address_space(3))) bf16x4 lds_bf16x4;
typedef __attribute__((address_space(3))) bf16x8 lds_bf16x8;
typedef __attribute__((address_space(3))) f32x4 lds_f32x4;
typedef __attribute__((address_space(3))) f32x2 lds_f32x2;
typedef __attribute__((address_space(1))) unsigned long long gu64;

#define DI __device__ __forceinline__
#define MFMA32(a, b, c) __builtin_amdgcn_mfma_f32_32x32x16_bf16((a), (b), (c), 0, 0, 0)
#define MFMA16(a, b, c) __builtin_amdgcn_mfma_f32_16x16x32_bf16((a), (b), (c), 0, 0, 0)
#define SB() __builtin_amdgcn_sched_barrier(0)
template <int V> using IC = std::integral_constant<int, V>;
#define SG_MFMA(n) __builtin_amdgcn_sched_group_barrier(0x008, (n), 0)
#define SG_DSRD(n) __builtin_amdgcn_sched_group_barrier(0x100, (n), 0)

#define STAMPW(wv, i) do {} while (0)
#define STAMP(i) do {} while (0)
#define WREC(i, v) do {} while (0)
#define TNOW() 0ull

constexpr int NB = 8, SQ = 192, DM = 576, NH = 32, SK = 512, DH = 128;
constexpr float QSCALE = 0.08838834764831845f * 1.4426950408889634f;
constexpr float THR = 8.0f;

constexpr unsigned CODE_BYTES = 26 * 1024;
constexpr size_t WS_O = 0;
constexpr size_t WS_PART = 16u << 20;
constexpr size_t WS_FLAG = 32u << 20;
constexpr unsigned PART_BLK = 24 * 144 * 2;
constexpr unsigned O_BYTES = NB * NH * 6 * 8 * 1024, PART_BYTES = 8 * 8 * 4 * 4 * PART_BLK;

constexpr unsigned QS_BYTES = 40960, QS_W = 24576;
constexpr unsigned SPARE = 155648, LDS_TOTAL = 163840, ML_OFF = 98304, VOTE_OFF = 101376;
constexpr float L_MIN = 9.313225746154785e-10f, L_MAX = 1048576.0f;
DI constexpr unsigned kv_slot(int s) { return (unsigned)((s + 3) & 3) * QS_BYTES; }
DI unsigned qblk(unsigned blk) { return blk < 40 ? 2 * QS_BYTES + blk * 1024 : SPARE + (blk - 40) * 1024; }
constexpr unsigned P2_OSLOT = 12288, P2_WSLOT = 18432, P2_WBASE = 53248, IMG_STRIDE = 592, IMG_BYTES = 96 * IMG_STRIDE;
DI unsigned p2_oslot(unsigned i) { return (i & 3) * P2_OSLOT; }
DI unsigned p2_wslot(unsigned i) { return P2_WBASE + ((i + 3) % 6) * P2_WSLOT; }

DI unsigned rfl(unsigned x) { return __builtin_amdgcn_readfirstlane(x); }
DI unsigned cvtpk(float lo, float hi) { f32x2 v = {lo, hi}; bf16x2 b = __builtin_convertvector(v, bf16x2); return __builtin_bit_cast(unsigned, b); }
DI bf16x8 pack8(const f32x16& x, int s) {
  u32x4 p = {cvtpk(x[8 * s], x[8 * s + 1]), cvtpk(x[8 * s + 2], x[8 * s + 3]), cvtpk(x[8 * s + 4], x[8 * s + 5]), cvtpk(x[8 * s + 6], x[8 * s + 7])};
  return __builtin_bit_cast(bf16x8, p);
}
DI bf16x8 lds_rd128(lds_char* base, unsigned off) { return *(lds_bf16x8*)(base + off); }
DI bf16x8 lds_tr2(lds_char* base, unsigned off0, unsigned off1) {
  bf16x4 lo = __builtin_amdgcn_ds_read_tr16_b64_v4bf16((lds_bf16x4*)(base + off0));
  bf16x4 hi = __builtin_amdgcn_ds_read_tr16_b64_v4bf16((lds_bf16x4*)(base + off1));
  return __builtin_shufflevector(lo, hi, 0, 1, 2, 3, 4, 5, 6, 7);
}
template <int POL> DI void dma16(__amdgpu_buffer_rsrc_t r, unsigned voff, unsigned soff, unsigned m0v) {
  if constexpr (POL == 0) asm volatile("s_mov_b32 m0, %3\n\ts_nop 0\n\tbuffer_load_dwordx4 %0, %1, %2 offen lds" ::"v"(voff), "s"(r), "s"(soff), "s"(m0v) : "memory");
  else if constexpr (POL == 1) asm volatile("s_mov_b32 m0, %3\n\ts_nop 0\n\tbuffer_load_dwordx4 %0, %1, %2 offen nt lds" ::"v"(voff), "s"(r), "s"(soff), "s"(m0v) : "memory");
  else asm volatile("s_mov_b32 m0, %3\n\ts_nop 0\n\tbuffer_load_dwordx4 %0, %1, %2 offen sc1 lds" ::"v"(voff), "s"(r), "s"(soff), "s"(m0v) : "memory");
}
template <int N> DI void wait_vm() { asm volatile("s_waitcnt vmcnt(%0)" ::"i"(N) : "memory"); }
DI void bar() { SB(); asm volatile("s_waitcnt lgkmcnt(0)\n\ts_barrier" ::: "memory"); SB(); }
DI float pl32_max(float v) {
  auto r = __builtin_amdgcn_permlane32_swap(__float_as_uint(v), __float_as_uint(v), false, false);
  return fmaxf(__uint_as_float(r[0]), __uint_as_float(r[1]));
}
DI float pl32_sum(float v) {
  auto r = __builtin_amdgcn_permlane32_swap(__float_as_uint(v), __float_as_uint(v), false, false);
  return __uint_as_float(r[0]) + __uint_as_float(r[1]);
}
DI unsigned lane_id() { unsigned l; asm volatile("v_mbcnt_lo_u32_b32 %0, -1, 0\n\tv_mbcnt_hi_u32_b32 %0, -1, %0" : "=v"(l)); return l; }
DI void poll_flags(gu64* flags, unsigned idx0, unsigned stride, unsigned n, unsigned skip, unsigned long long tag, unsigned lane) {
  const bool mine = lane < n && lane != skip;
  gu64* p = flags + (size_t)(idx0 + (mine ? lane : 0) * stride) * 16;
  for (unsigned spins = 0; spins < 400000u; ++spins) {
    const unsigned long long v = __hip_atomic_load(p, __ATOMIC_RELAXED, __HIP_MEMORY_SCOPE_AGENT);
    if (__all(!mine || v == tag)) break;
    __builtin_amdgcn_s_sleep(1);
  }
}

#define OUT_AUX 2
#define ROTMODE 2
#define POL_X 0
#define POL_WQ 0
#define O_ST_AUX 16
#define POL_O 2
#define POL_WO 0

__global__ __launch_bounds__(768, 3) void mha_kernel(const __hip_bfloat16* __restrict__ x, const __hip_bfloat16* __restrict__ kc,
                                                     const __hip_bfloat16* __restrict__ vc, const __hip_bfloat16* __restrict__ wq,
                                                     const __hip_bfloat16* __restrict__ wo, __hip_bfloat16* __restrict__ out,
                                                     char* __restrict__ ws, unsigned long long* diag) {
  __shared__ __attribute__((aligned(1024))) char smem[LDS_TOTAL];
  lds_char* lds = (lds_char*)smem;
  const unsigned ldsb = (unsigned)(size_t)smem;
  const unsigned tid = threadIdx.x, w = rfl(tid >> 6);
  STAMP(0);
  const unsigned xcd = blockIdx.x & 7, jj = blockIdx.x >> 3;
  const unsigned b = 4 * (xcd & 1) + (jj & 3), hd = 8 * (xcd >> 1) + (jj >> 2);
  gu64* flags = (gu64*)(ws + WS_FLAG);
  unsigned long long epoch;
  auto read_epoch = [&]() {
    asm volatile("s_load_dwordx2 %0, %1, 0x0\n\ts_waitcnt lgkmcnt(0)" : "=s"(epoch) : "s"((const char*)(ws + WS_FLAG) + (512 + b) * 128) : "memory");
  };

  const __amdgpu_buffer_rsrc_t rwo = __builtin_amdgcn_make_buffer_rsrc((void*)wo, (short)0, NH * DH * DM * 2, 0x00020000);
  bf16x8 qf[8];
  f32x16 oacc[4];
  float m, l;
  {
    const unsigned lane = tid & 63, r = lane & 31, h = lane >> 5;
    const unsigned t = w >> 1, j = w & 1;
    const unsigned g1 = (lane >> 4) & 1, qq = (lane & 15) >> 2, pp = lane & 3, sp = ((pp & 1) << 1) | (pp >> 1);
    const __amdgpu_buffer_rsrc_t rk = __builtin_amdgcn_make_buffer_rsrc((void*)kc, (short)0, NB * NH * SK * DH * 2, 0x00020000);
    const __amdgpu_buffer_rsrc_t rv = __builtin_amdgcn_make_buffer_rsrc((void*)vc, (short)0, NB * NH * SK * DH * 2, 0x00020000);
    const unsigned skv = (b * NH + hd) * (SK * DH * 2);
    const unsigned vk = (lane >> 4) * 256 + ((lane & 15) ^ (lane >> 4)) * 16;
    const unsigned vv = (lane >> 4) * 256 + ((lane & 15) ^ ((lane >> 4) << 2)) * 16;

    auto kv4 = [&](int s) {
      const unsigned lw = w - 4;
      const unsigned dst = ldsb + kv_slot(s) + lw * 4096u;
      const unsigned so = skv + ((lw >> 1) & 1) * 65536u + (lw & 1) * 4096u + (unsigned)s * 8192u;
#pragma unroll
      for (int p = 0; p < 4; ++p) {
        if (lw < 4) dma16<1>(rk, vk ^ (p << 6), so + p * 1024u, dst + p * 1024u);
        else dma16<1>(rv, vv, so + p * 1024u, dst + p * 1024u);
      }
    };
    const unsigned koff = 3 * (((jj >> 2) + (jj & 3)) % 3);
    if (w >= 4) {
      const unsigned lw = w - 4;
      const __amdgpu_buffer_rsrc_t rx = __builtin_amdgcn_make_buffer_rsrc((void*)x, (short)0, NB * SQ * DM * 2, 0x00020000);
      const __amdgpu_buffer_rsrc_t rw = __builtin_amdgcn_make_buffer_rsrc((void*)wq, (short)0, DM * NH * DH * 2, 0x00020000);
      const unsigned vx = (lane >> 3) * 1152 + (((lane & 7) ^ (lane >> 4) ^ ((lw & 1) << 2)) * 16);
      const unsigned sx = (b * SQ + 8 * lw) * 1152u;
      const unsigned vw = (lane >> 4) * 8192 + (((lane & 15) ^ ((lane >> 4) << 2) ^ (lw & 2)) * 16);
      const unsigned sw = 4 * lw * 8192u + hd * 256u;
      auto issue_q = [&](int i) {
        unsigned st = koff + i; st = st >= 9 ? st - 9 : st;
        const unsigned slot = ldsb + (unsigned)(i % 3) * QS_BYTES;
#pragma unroll
        for (int p = 0; p < 3; ++p) dma16<POL_X>(rx, vx, sx + (unsigned)(64 * p) * 1152u + st * 128u, slot + (lw + 8 * p) * 1024u);
#pragma unroll
        for (int p = 0; p < 2; ++p) dma16<POL_WQ>(rw, vw, sw + (st * 64 + 32 * p) * 8192u, slot + QS_W + (lw + 8 * p) * 1024u);
      };
      issue_q(0); issue_q(1);
      wait_vm<5>(); bar(); issue_q(2);
      wait_vm<5>(); bar(); issue_q(3);
      wait_vm<5>(); bar(); issue_q(4); kv4(0);
      wait_vm<9>(); bar(); issue_q(5);
      wait_vm<9>(); bar(); issue_q(6);
      wait_vm<5>(); bar(); issue_q(7);
      wait_vm<5>(); bar(); issue_q(8);
      wait_vm<5>(); bar(); kv4(1);
      wait_vm<4>(); bar(); kv4(2);
      read_epoch();
      bar();
      bar();
    } else {
      read_epoch();
      if (w < 2) {
        unsigned long long pc;
        asm volatile("s_getpc_b64 %0" : "=s"(pc));
        const unsigned off = (jj * 2 + w) * 1024u;
        if (off + 1024u <= CODE_BYTES) {
          u32x4 t;
          const char* p = (const char*)(pc & ~127ull) + off + lane * 16;
          asm volatile("global_load_dwordx4 %0, %1, off\n\ts_waitcnt vmcnt(0)" : "=v"(t) : "v"(p) : "memory");
        }
      }
      const unsigned qh = w >> 1, dh = w & 1;
      const unsigned c16 = lane & 15, g4 = lane >> 4;
      const unsigned xa0 = (96 * qh + c16) * 128 + ((g4 ^ ((c16 >> 1) & 7)) << 4);
      const unsigned wa0 = QS_W + (8 * g4 + (c16 >> 2)) * 256 + (((8 * dh + ((c16 & 3) >> 1)) ^ ((c16 >> 2) << 2) ^ ((g4 & 1) << 1)) << 4) + (c16 & 1) * 8;
      f32x4 qacc[6][4];
#pragma unroll
      for (int a = 0; a < 6; ++a)
#pragma unroll
        for (int d = 0; d < 4; ++d) qacc[a][d] = f32x4{0.f, 0.f, 0.f, 0.f};
      bf16x8 bA[3], bB[3], aA[4], aB[4];
      unsigned xb, wb;
      auto rdb = [&](bf16x8 (&bb)[3], int kk, int hf) {
        const unsigned xs = xb ^ (kk << 6);
#pragma unroll
        for (int a = 0; a < 3; ++a) bb[a] = lds_rd128(lds, xs + (3 * hf + a) * 2048);
      };
      auto rda = [&](bf16x8 (&av)[4], int kk, int d0) {
#pragma unroll
        for (int d = d0; d < d0 + 2; ++d) { const unsigned wd = (wb ^ (d << 5)) + kk * 8192; av[d] = lds_tr2(lds, wd, wd + 1024); }
      };
      auto mm = [&](bf16x8 (&bb)[3], bf16x8 (&av)[4], int hf) {
#pragma unroll
        for (int a = 0; a < 3; ++a)
#pragma unroll
          for (int d = 0; d < 4; ++d) qacc[3 * hf + a][d] = MFMA16(av[d], bb[a], qacc[3 * hf + a][d]);
      };
      auto mix = [&]() {
        SG_MFMA(1); SG_DSRD(1); SG_MFMA(2); SG_DSRD(1); SG_MFMA(1); SG_DSRD(1); SG_MFMA(2); SG_DSRD(1); SG_MFMA(1); SG_DSRD(1);
        SG_MFMA(2); SG_DSRD(1); SG_MFMA(1); SG_DSRD(1); SG_MFMA(2); SB();
      };
      bar();
      STAMP(1);
      xb = xa0; wb = wa0;
      asm volatile("" : "+v"(xb), "+v"(wb));
      rdb(bA, 0, 0); rda(aA, 0, 0); rda(aA, 0, 2);
#pragma unroll 1
      for (int st = 0; st < 8; ++st) {
        SB(); rdb(bB, 0, 1); rda(aB, 1, 0); mm(bA, aA, 0); mix();
        rdb(bA, 1, 0); rda(aB, 1, 2); mm(bB, aA, 1); mix();
        rdb(bB, 1, 1); mm(bA, aB, 0);
        SG_MFMA(1); SG_DSRD(1); SG_MFMA(2); SG_DSRD(1); SG_MFMA(1); SG_DSRD(1); SG_MFMA(8); SB();
        bar();
        const unsigned so = (unsigned)((st + 1) % 3) * QS_BYTES;
        xb = xa0 + so; wb = wa0 + so;
        asm volatile("" : "+v"(xb), "+v"(wb));
        rdb(bA, 0, 0); rda(aA, 0, 0); rda(aA, 0, 2); mm(bB, aB, 1);
        SG_MFMA(1); SG_DSRD(1); SG_MFMA(1); SG_DSRD(1); SG_MFMA(1); SG_DSRD(1); SG_MFMA(1); SG_DSRD(1); SG_MFMA(1); SG_DSRD(1); SG_MFMA(1); SG_DSRD(1);
        SG_MFMA(1); SG_DSRD(1); SG_MFMA(1); SG_DSRD(1); SG_MFMA(1); SG_DSRD(1); SG_MFMA(1); SG_DSRD(1); SG_MFMA(1); SG_DSRD(1); SG_MFMA(1); SB();
      }
      SB(); rdb(bB, 0, 1); rda(aB, 1, 0); mm(bA, aA, 0); mix();
      rdb(bA, 1, 0); rda(aB, 1, 2); mm(bB, aA, 1); mix();
      rdb(bB, 1, 1); mm(bA, aB, 0); SB();
      mm(bB, aB, 1);
      bar();
      STAMP(4);
#pragma unroll
      for (int a = 0; a < 6; a += 2)
#pragma unroll
        for (int d = 0; d < 4; ++d) {
          const f32x4 vx = qacc[a][d] * QSCALE, vy = qacc[a + 1][d] * QSCALE;
          const auto s0 = __builtin_amdgcn_permlane16_swap(cvtpk(vx[0], vx[1]), cvtpk(vy[0], vy[1]), false, false);
          const auto s1 = __builtin_amdgcn_permlane16_swap(cvtpk(vx[2], vx[3]), cvtpk(vy[2], vy[3]), false, false);
          const u32x4 pk = {s0[0], s1[0], s0[1], s1[1]};
          *(__attribute__((address_space(3))) u32x4*)(lds + qblk((3 * qh + (a >> 1)) * 8 + 4 * dh + d) + lane * 16) = pk;
        }
      bar();
    }
#pragma unroll
    for (int i = 0; i < 8; ++i) qf[i] = lds_rd128(lds, qblk(t * 8 + i) + lane * 16);

    const unsigned grp = w >> 2;
    float l0, l1;
    bool safe = false;
    const unsigned ka0 = j * 8192 + r * 256 + ((h ^ (r & 15)) << 4);
    const unsigned va0 = (16384 + j * 8192 + (4 * h + qq) * 256 + ((4 * qq + 2 * g1 + (sp >> 1)) << 4) + (sp & 1) * 8) ^ (j << 7);
    const unsigned vkx = vk ^ ((w & 3) << 6);
    const unsigned sp0 = skv + ((w >> 3) & 1) * 65536u + (w & 7) * 1024u;
    const unsigned sp1 = skv + (((w + 12) >> 3) & 1) * 65536u + ((w + 12) & 7) * 1024u;
    const unsigned sp2 = skv + (((w + 24) >> 3) & 1) * 65536u + ((w + 24) & 7) * 1024u;

    bf16x8 kf[4];
    auto step = [&](auto G_, int s) {
      constexpr int G = decltype(G_)::value;
      constexpr int NP = G == 2 ? 2 : 3;
      unsigned vb = va0 + kv_slot(s), kc0 = ka0 + kv_slot(s), kb = ka0 + kv_slot(s + 1);
      asm volatile("" : "+v"(kb), "+v"(vb), "+v"(kc0));
      __builtin_amdgcn_s_setprio(1);
      f32x16 sc;
      bf16x8 vf[4][2];
      {
        bf16x8 kg[4];
#pragma unroll
        for (int ks = 0; ks < 4; ++ks) kg[ks] = lds_rd128(lds, kc0 ^ ((ks + 4) << 5));
#pragma unroll
        for (int i = 0; i < 16; ++i) sc[i] = 0.f;
#pragma unroll
        for (int ks = 0; ks < 4; ++ks) sc = MFMA32(kf[ks], qf[ks], sc);
#pragma unroll
        for (int d = 0; d < 4; ++d) { const unsigned va = vb ^ (d << 6); vf[d][0] = lds_tr2(lds, va, va + 2048); }
#pragma unroll
        for (int ks = 0; ks < 4; ++ks) sc = MFMA32(kg[ks], qf[ks + 4], sc);
        SG_MFMA(1); SG_DSRD(2); SG_MFMA(1); SG_DSRD(2); SG_MFMA(1); SG_DSRD(2); SG_MFMA(1); SG_DSRD(2);
        SG_MFMA(1); SG_DSRD(2); SG_MFMA(1); SG_DSRD(2); SG_MFMA(2);
        SB();
        asm volatile("" : "+v"(sc));
      }
      __builtin_amdgcn_s_setprio(0);
      if constexpr (G == 1) { if (s <= 5) wait_vm<NP>(); else if (s == 6) wait_vm<0>(); }
      bar();
#pragma unroll
      for (int d = 0; d < 4; ++d) { const unsigned va = vb ^ (d << 6); vf[d][1] = lds_tr2(lds, va + 4096, va + 4096 + 2048); }
      SB();
      if (safe) {
        const float t0 = fmaxf(fmaxf(sc[0], sc[1]), sc[2]), t1 = fmaxf(fmaxf(sc[3], sc[4]), sc[5]), t2 = fmaxf(fmaxf(sc[6], sc[7]), sc[8]);
        const float t3 = fmaxf(fmaxf(sc[9], sc[10]), sc[11]), t4 = fmaxf(fmaxf(sc[12], sc[13]), sc[14]);
        float tm = fmaxf(fmaxf(fmaxf(t0, t1), t2), fmaxf(fmaxf(t3, t4), sc[15]));
        tm = pl32_max(tm);
        if (__any(tm > m + THR)) {
          const float mn = fmaxf(m, tm);
          const float al = __builtin_amdgcn_exp2f(m - mn);
          l0 *= al; l1 *= al; m = mn;
#pragma unroll
          for (int d = 0; d < 4; ++d)
#pragma unroll
            for (int i = 0; i < 16; ++i) oacc[d][i] *= al;
        }
#pragma unroll
        for (int i = 0; i < 16; ++i) sc[i] -= m;
      }
      SB();
#pragma unroll
      for (int i = 0; i < 16; ++i) sc[i] = __builtin_amdgcn_exp2f(sc[i]);
      bf16x8 p0 = pack8(sc, 0), p1 = pack8(sc, 1);
#pragma unroll
      for (int i = 0; i < 16; i += 2) { l0 += sc[i]; l1 += sc[i + 1]; }
      asm volatile("" : "+v"(p0), "+v"(p1), "+v"(l0), "+v"(l1));
      if constexpr (G == 0) { if (s <= 5) wait_vm<NP>(); else if (s == 6) wait_vm<0>(); }
      bar();
      __builtin_amdgcn_s_setprio(1);
#pragma unroll
      for (int d = 0; d < 4; ++d) oacc[d] = MFMA32(vf[d][0], p0, oacc[d]);
#pragma unroll
      for (int ks = 0; ks < 4; ++ks) kf[ks] = lds_rd128(lds, kb ^ (ks << 5));
#pragma unroll
      for (int d = 0; d < 4; ++d) oacc[d] = MFMA32(vf[d][1], p1, oacc[d]);
      SG_MFMA(2); SG_DSRD(1); SG_MFMA(1); SG_DSRD(1); SG_MFMA(1); SG_DSRD(1); SG_MFMA(1); SG_DSRD(1); SG_MFMA(3);
      SB();
      asm volatile("" : "+v"(oacc[0]), "+v"(oacc[1]), "+v"(oacc[2]), "+v"(oacc[3]));
      __builtin_amdgcn_s_setprio(0);
      SB();
      if (s <= 4) {
        const unsigned dst = ldsb + kv_slot(s + 3) + w * 1024u, sofs = (unsigned)(s + 3) * 8192u;
        dma16<1>(rk, vkx, sp0 + sofs, dst);
        if constexpr (G == 0) dma16<1>(rk, vkx, sp1 + sofs, dst + 12288u); else dma16<1>(rv, vv, sp1 + sofs, dst + 12288u);
        if constexpr (G != 2) dma16<1>(rv, vv, sp2 + sofs, dst + 24576u);
      }
      if constexpr (G == 2) { if (s <= 4) wait_vm<NP>(); else if (s == 5) wait_vm<0>(); }
      if (s >= 6 && !safe) {
        const unsigned pc = ((b & 3) * 8 + (hd & 7)) * 36 + w * 3 + (s - 6) * 2;
        const unsigned so = (hd >> 3) * (1024u * 1152u) + pc * 1024u;
        dma16<POL_WO>(rwo, lane * 16, so, ldsb + SPARE + (w & 7) * 1024u);
        if (s == 6) dma16<POL_WO>(rwo, lane * 16, so + 1024u, ldsb + SPARE + (w & 7) * 1024u);
      }
      bar();
    };
    auto run = [&](auto G_) {
      constexpr int G = decltype(G_)::value;
      {
        unsigned kb0 = ka0 + kv_slot(0);
        asm volatile("" : "+v"(kb0));
#pragma unroll
        for (int ks = 0; ks < 4; ++ks) kf[ks] = lds_rd128(lds, kb0 ^ (ks << 5));
      }
      if constexpr (G >= 1) bar();
      if constexpr (G == 2) { wait_vm<4>(); bar(); }
#pragma unroll 1
      for (int s = 0; s < 8; ++s) step(G_, s);
      if constexpr (G == 0) bar();
      if constexpr (G <= 1) bar();
    };
#pragma unroll 1
    for (;;) {
#pragma unroll
      for (int d = 0; d < 4; ++d)
#pragma unroll
        for (int i = 0; i < 16; ++i) oacc[d][i] = 0.f;
      m = safe ? -__builtin_inff() : 0.f; l0 = 0.f; l1 = 0.f;
      bar(); STAMP(5);
      if (grp == 0) run(IC<0>{});
      else if (grp == 1) run(IC<1>{});
      else run(IC<2>{});
      STAMP(13);
      const unsigned ln = lane_id();
      l = pl32_sum(l0 + l1);
      const unsigned xw = w * 4096 + ln * 16;
#pragma unroll
      for (int i = 0; i < 4; ++i) *(lds_bf16x8*)(lds + xw + i * 1024) = pack8(oacc[2 + (i >> 1)], i & 1);
      if (ln < 32) { f32x2 ml = {m, l}; *(lds_f32x2*)(lds + ML_OFF + w * 256 + ln * 8) = ml; }
      const bool bad = __any(!(l > L_MIN && l < L_MAX));
      if (ln == 0) *(__attribute__((address_space(3))) unsigned*)(lds + VOTE_OFF + w * 4) = bad ? 1u : 0u;
      bar();
      STAMP(6);
      if (safe) break;
      const unsigned vote = *(__attribute__((address_space(3))) unsigned*)(lds + VOTE_OFF + (ln < 12 ? ln : 0) * 4);
      if (!__any(vote != 0)) break;
      safe = true;
      bar();
      if (w >= 4) {
        const unsigned lw = rfl(w - 4), sk = rfl((b * NH + hd) * (SK * DH * 2));
        const unsigned vo = (ln >> 4) * 256 + (lw < 4 ? ((ln & 15) ^ (ln >> 4)) : ((ln & 15) ^ ((ln >> 4) << 2))) * 16;
        const __amdgpu_buffer_rsrc_t rr = __builtin_amdgcn_make_buffer_rsrc((void*)(lw < 4 ? kc : vc), (short)0, NB * NH * SK * DH * 2, 0x00020000);
#pragma unroll 1
        for (unsigned q = 0; q < 12; ++q) {
          const unsigned st = q >> 2, p = q & 3;
          dma16<1>(rr, lw < 4 ? vo ^ (p << 6) : vo, rfl(sk + ((lw >> 1) & 1) * 65536u + (lw & 1) * 4096u + st * 8192u + p * 1024u), rfl(ldsb + kv_slot(st) + lw * 4096u + p * 1024u));
        }
        wait_vm<0>();
      }
    }
  }

  const unsigned lane = lane_id();
  const unsigned hg = hd >> 3, hl = hd & 7, rh = hl & 1, cq = hl >> 1;
  const unsigned soff2 = (4 * cq + 2 * (b & 3) + 8 * rh) & 15;
  const unsigned long long tag = epoch + 1;
  const unsigned wg = b * NH + hd;
  auto wo_voff = [&](int i) {
    const unsigned n = 64 * i + lane, p = n / 18, ch = n - 18 * p;
    const unsigned k = (p & 19) | ((p & 4) << 1) | ((p & 8) >> 1);
    return k * 1152 + ch * 16;
  };
  auto wo_issue = [&](const unsigned (&vo)[9], unsigned i) {
    const unsigned sg = (i + soff2) & 15, kh = w & 1;
    const unsigned so = ((8 * hg + 4 * kh + (sg >> 2)) * 128 + (sg & 3) * 32) * 1152u + cq * 288u;
    const unsigned dst = ldsb + p2_wslot(i) + kh * 9216u;
#pragma unroll
    for (int p = 0; p < 9; ++p) dma16<POL_WO>(rwo, vo[p], so, dst + p * 1024u);
  };
  unsigned wvo[9];
  if (w == 8 || w == 9) {
#pragma unroll
    for (int p = 0; p < 9; ++p) wvo[p] = wo_voff(p);
  }

  {
    const unsigned r = lane & 31, h = lane >> 5, t = w >> 1, j = w & 1;
    f32x2 o = *(lds_f32x2*)(lds + ML_OFF + (w ^ 1) * 256 + r * 8);
    const float mn = fmaxf(m, o[0]);
    const float f = __builtin_amdgcn_exp2f(m - mn), fo = __builtin_amdgcn_exp2f(o[0] - mn);
    const float inv = 1.0f / (l * f + o[1] * fo);
    const float sa = f * inv, sb = fo * inv;
    const unsigned xr = (w ^ 1) * 4096 + lane * 16;
#pragma unroll
    for (int i = 0; i < 4; ++i) {
      const u32x4 v = __builtin_bit_cast(u32x4, lds_rd128(lds, xr + i * 1024));
#pragma unroll
      for (int e = 0; e < 4; ++e) {
        oacc[i >> 1][8 * (i & 1) + 2 * e] = oacc[i >> 1][8 * (i & 1) + 2 * e] * sa + __uint_as_float(v[e] << 16) * sb;
        oacc[i >> 1][8 * (i & 1) + 2 * e + 1] = oacc[i >> 1][8 * (i & 1) + 2 * e + 1] * sa + __uint_as_float(v[e] & 0xffff0000u) * sb;
      }
    }
    const __amdgpu_buffer_rsrc_t ro = __builtin_amdgcn_make_buffer_rsrc((void*)(ws + WS_O), (short)0, O_BYTES, 0x00020000);
    const unsigned so = ((wg * 6 + t) * 8 + 4 * j) * 1024u;
#pragma unroll
    for (int i = 0; i < 4; ++i)
      __builtin_amdgcn_raw_buffer_store_b128(__builtin_bit_cast(u32x4, pack8(oacc[i >> 1], i & 1)), ro, so + lane * 16 + i * 1024, 0, O_ST_AUX);
    STAMP(7);
    if (w == 8 || w == 9) { wo_issue(wvo, 0); wo_issue(wvo, 1); wait_vm<18>(); } else wait_vm<0>();
    STAMP(8);
    bar();
    if (tid == 0) __hip_atomic_store(flags + (size_t)wg * 16, tag, __ATOMIC_RELAXED, __HIP_MEMORY_SCOPE_AGENT);
  }
  STAMP(14);

  f32x4 acc[3][5];
#pragma unroll
  for (int a = 0; a < 3; ++a)
#pragma unroll
    for (int n = 0; n < 5; ++n) acc[a][n] = f32x4{0.f, 0.f, 0.f, 0.f};
  const unsigned nh = (w >> 2) & 1, kh = w & 1, qh = (w >> 1) & 1;
  if (w >= 10) {
    poll_flags(flags, b * NH + 8 * hg, 1, 8, 64, tag, lane);
    STAMPW(10, 20);
    const __amdgpu_buffer_rsrc_t ro = __builtin_amdgcn_make_buffer_rsrc((void*)(ws + WS_O), (short)0, O_BYTES, 0x00020000);
    auto o_issue = [&](unsigned i) {
      const unsigned sg = (i + soff2) & 15;
      const unsigned head = 8 * hg + 4 * kh + (sg >> 2);
      const unsigned so = (((b * NH + head) * 6 + 3 * rh) * 8 + 2 * (sg & 3)) * 1024u;
      const unsigned dst = ldsb + p2_oslot(i) + kh * 6144u;
#pragma unroll
      for (int q = 0; q < 3; ++q) { dma16<POL_O>(ro, lane * 16, so + q * 8192u, dst + q * 2048u); dma16<POL_O>(ro, lane * 16, so + q * 8192u + 1024u, dst + q * 2048u + 1024u); }
    };
    o_issue(0); o_issue(1);
    STAMPW(10, 22);
#pragma unroll 1
    for (unsigned i = 0; i < 16; ++i) {
      if (i <= 14) wait_vm<6>(); else wait_vm<0>();
      if (i == 0) STAMPW(10, 23);
      bar();
      if (i + 2 < 16) o_issue(i + 2);
    }
  } else if (w >= 8) {
    wo_issue(wvo, 2); wo_issue(wvo, 3); wo_issue(wvo, 4);
#pragma unroll 1
    for (unsigned i = 0; i < 16; ++i) {
      if (i <= 11) wait_vm<36>(); else if (i == 12) wait_vm<27>(); else if (i == 13) wait_vm<18>(); else if (i == 14) wait_vm<9>(); else wait_vm<0>();
      bar();
      if (i + 5 < 16) wo_issue(wvo, i + 5);
    }
  } else {
    const unsigned c = lane & 15, g = lane >> 4;
    unsigned ba[3];
#pragma unroll
    for (int a = 0; a < 3; ++a) {
      const unsigned qi = 3 * qh + a;
      ba[a] = kh * 6144 + ((qi >> 1) * 2 + (g >> 1)) * 1024 + (16 * (qi & 1) + c + 32 * (g & 1)) * 16;
    }
    const unsigned aa = kh * 9216 + ((g >> 1) * 16 + (g & 1) * 4 + (c >> 2)) * 288 + 5 * nh * 32 + (c & 3) * 8;
    bf16x8 bX[3], aX[5], bY[3], aY[5];
    auto rd = [&](auto NT_, bf16x8 (&bb)[3], bf16x8 (&av)[5], unsigned i) {
      constexpr int NT = decltype(NT_)::value;
      unsigned ob = p2_oslot(i), wb = aa + p2_wslot(i);
      asm volatile("" : "+v"(wb));
#pragma unroll
      for (int a = 0; a < 3; ++a) bb[a] = lds_rd128(lds, ba[a] + ob);
#pragma unroll
      for (int n = 0; n < NT; ++n) av[n] = lds_tr2(lds, wb + n * 32, wb + n * 32 + 2304);
    };
    auto mm = [&](auto NT_, bf16x8 (&bb)[3], bf16x8 (&av)[5]) {
      constexpr int NT = decltype(NT_)::value;
#pragma unroll
      for (int n = 0; n < NT; ++n)
#pragma unroll
        for (int a = 0; a < 3; ++a) acc[a][n] = MFMA16(av[n], bb[a], acc[a][n]);
    };
    unsigned long long t0 = 0;
    auto mix = [&](auto NT_) {
      constexpr int NT = decltype(NT_)::value;
#pragma unroll
      for (int k = 0; k < 3 + 2 * NT - (NT == 4 ? 0 : 0); ++k) {
        __builtin_amdgcn_sched_group_barrier(0x008, 1, 0);
        __builtin_amdgcn_sched_group_barrier(0x100, 1, 0);
      }
      __builtin_amdgcn_sched_group_barrier(0x008, 3 * NT, 0);
    };
    auto gemm = [&](auto NT_) {
      bar();
      STAMP(15);
      rd(NT_, bX, aX, 0);
#pragma unroll 1
      for (unsigned i = 1; i < 15; i += 2) {
        bar(); rd(NT_, bY, aY, i); mm(NT_, bX, aX); mix(NT_);
        bar(); rd(NT_, bX, aX, i + 1); mm(NT_, bY, aY); mix(NT_);
      }
      bar(); rd(NT_, bY, aY, 15); mm(NT_, bX, aX); mix(NT_);
      SB(); mm(NT_, bY, aY);
    };
    if (nh == 0) gemm(IC<5>{}); else gemm(IC<4>{});
    WREC(44, TNOW() - t0);
  }
  STAMP(16);
  bar();
  if (w < 8) {
    const unsigned c = lane & 15, g = lane >> 4;
    const unsigned base = kh * IMG_BYTES + (48 * qh + c) * IMG_STRIDE + (80 * nh + 4 * g) * 4;
#pragma unroll
    for (int a = 0; a < 3; ++a)
#pragma unroll
      for (int n = 0; n < 5; ++n)
        if (n < 4 || nh == 0) *(lds_f32x4*)(lds + base + a * 16 * IMG_STRIDE + n * 64) = acc[a][n];
  }
  bar();
  {
    const __amdgpu_buffer_rsrc_t rp = __builtin_amdgcn_make_buffer_rsrc((void*)(ws + WS_PART), (short)0, PART_BYTES, 0x00020000);
    const unsigned pbase = ((b * 8 + hl) * 4 + hg) * 4 * PART_BLK;
#pragma unroll 1
    for (unsigned n = tid; n < 72 * 18; n += 768) {
      const unsigned rr = n / 18, ch = n - rr * 18;
      unsigned dst = rr / 24; const unsigned row = rr - dst * 24;
      dst += dst >= hg ? 1 : 0;
      const unsigned a0 = (24 * dst + row) * IMG_STRIDE + ch * 32;
      const f32x4 v0 = *(lds_f32x4*)(lds + a0) + *(lds_f32x4*)(lds + IMG_BYTES + a0);
      const f32x4 v1 = *(lds_f32x4*)(lds + a0 + 16) + *(lds_f32x4*)(lds + IMG_BYTES + a0 + 16);
      const u32x4 pk = {cvtpk(v0[0], v0[1]), cvtpk(v0[2], v0[3]), cvtpk(v1[0], v1[1]), cvtpk(v1[2], v1[3])};
      __builtin_amdgcn_raw_buffer_store_b128(pk, rp, dst * PART_BLK + row * 288 + ch * 16 + pbase, 0, 16);
    }
    wait_vm<0>();
    bar();
    if (tid == 0) __hip_atomic_store(flags + (size_t)(256 + wg) * 16, tag, __ATOMIC_RELAXED, __HIP_MEMORY_SCOPE_AGENT);
    STAMP(17);
    if (w == 0) poll_flags(flags, 256 + b * NH + hl, 8, 4, hg, tag, lane);
    bar();
    STAMP(18);
    if (tid < 24 * 18) {
      const unsigned row = tid / 18, ch = tid - row * 18;
      u32x4 u[3];
#pragma unroll
      for (int k = 0; k < 3; ++k) {
        const unsigned src = k + (k >= (int)hg ? 1 : 0);
        u[k] = __builtin_amdgcn_raw_buffer_load_b128(rp, (((b * 8 + hl) * 4 + src) * 4 + hg) * PART_BLK + row * 288 + ch * 16, 0, 16);
      }
      const unsigned a0 = (24 * hg + row) * IMG_STRIDE + ch * 32;
      f32x4 v0 = *(lds_f32x4*)(lds + a0) + *(lds_f32x4*)(lds + IMG_BYTES + a0);
      f32x4 v1 = *(lds_f32x4*)(lds + a0 + 16) + *(lds_f32x4*)(lds + IMG_BYTES + a0 + 16);
#pragma unroll
      for (int k = 0; k < 3; ++k) {
        v0[0] += __uint_as_float(u[k][0] << 16); v0[1] += __uint_as_float(u[k][0] & 0xffff0000u);
        v0[2] += __uint_as_float(u[k][1] << 16); v0[3] += __uint_as_float(u[k][1] & 0xffff0000u);
        v1[0] += __uint_as_float(u[k][2] << 16); v1[1] += __uint_as_float(u[k][2] & 0xffff0000u);
        v1[2] += __uint_as_float(u[k][3] << 16); v1[3] += __uint_as_float(u[k][3] & 0xffff0000u);
      }
      const u32x4 o = {cvtpk(v0[0], v0[1]), cvtpk(v0[2], v0[3]), cvtpk(v1[0], v1[1]), cvtpk(v1[2], v1[3])};
      const __amdgpu_buffer_rsrc_t rout = __builtin_amdgcn_make_buffer_rsrc((void*)out, (short)0, NB * SQ * DM * 2, 0x00020000);
      __builtin_amdgcn_raw_buffer_store_b128(o, rout, ((b * SQ + 96 * rh + 24 * hg + row) * DM + 144 * cq + 8 * ch) * 2, 0, OUT_AUX);
    }
    if (tid == 0 && hd == 0) __hip_atomic_store(flags + (size_t)(512 + b) * 16, tag, __ATOMIC_RELAXED, __HIP_MEMORY_SCOPE_AGENT);
  }
}

extern "C" void kernel_launch(void* const* d_in, const int* in_sizes, int n_in, void* d_out, int out_size, void* d_ws, size_t ws_size,
                              hipStream_t stream) {
  const __hip_bfloat16* x = (const __hip_bfloat16*)d_in[0];
  const __hip_bfloat16* kc = (const __hip_bfloat16*)d_in[1];
  const __hip_bfloat16* vc = (const __hip_bfloat16*)d_in[2];
  const __hip_bfloat16* wq = (const __hip_bfloat16*)d_in[3];
  const __hip_bfloat16* wo = (const __hip_bfloat16*)d_in[4];
  unsigned long long* diag = (unsigned long long*)((char*)d_ws + (64u << 20));
  mha_kernel<<<256, 768, 0, stream>>>(x, kc, vc, wq, wo, (__hip_bfloat16*)d_out, (char*)d_ws, diag);
}
```

```cpp
#include <hip/hip_runtime.h>
#include <hip/hip_bf16.h>
#include <type_traits>

typedef __bf16 bf16x8 __attribute__((ext_vector_type(8)));
typedef __bf16 bf16x4 __attribute__((ext_vector_type(4)));
typedef __bf16 bf16x2 __attribute__((ext_vector_type(2)));
typedef float f32x16 __attribute__((ext_vector_type(16)));
typedef float f32x4 __attribute__((ext_vector_type(4)));
typedef float f32x2 __attribute__((ext_vector_type(2)));
typedef unsigned u32x4 __attribute__((ext_vector_type(4)));
typedef unsigned u32x2 __attribute__((ext_vector_type(2)));
typedef __attribute__((address_space(3))) char lds_char;
typedef __attribute__((address_space(3))) bf16x4 lds_bf16x4;
typedef __attribute__((address_space(3))) bf16x8 lds_bf16x8;
typedef __attribute__((address_space(3))) f32x4 lds_f32x4;
typedef __attribute__((address_space(3))) f32x2 lds_f32x2;
typedef __attribute__((address_space(1))) unsigned long long gu64;

#define DI __device__ __forceinline__
#define MFMA32(a, b, c) __builtin_amdgcn_mfma_f32_32x32x16_bf16((a), (b), (c), 0, 0, 0)
#define MFMA16(a, b, c) __builtin_amdgcn_mfma_f32_16x16x32_bf16((a), (b), (c), 0, 0, 0)
#define SB() __builtin_amdgcn_sched_barrier(0)
template <int V> using IC = std::integral_constant<int, V>;
#define SG_MFMA(n) __builtin_amdgcn_sched_group_barrier(0x008, (n), 0)
#define SG_DSRD(n) __builtin_amdgcn_sched_group_barrier(0x100, (n), 0)

#define STAMPW(wv, i) do {} while (0)
#define STAMP(i) do {} while (0)
#define WREC(i, v) do {} while (0)
#define TNOW() 0ull

constexpr int NB = 8, SQ = 192, DM = 576, NH = 32, SK = 512, DH = 128;
constexpr float QSCALE = 0.08838834764831845f * 1.4426950408889634f;
constexpr float THR = 8.0f;

constexpr unsigned CODE_BYTES = 26 * 1024;
constexpr size_t WS_O = 0;
constexpr size_t WS_PART = 16u << 20;
constexpr size_t WS_FLAG = 32u << 20;
constexpr unsigned PART_BLK = 24 * 144 * 2;
constexpr unsigned O_BYTES = NB * NH * 6 * 8 * 1024, PART_BYTES = 8 * 8 * 4 * 4 * PART_BLK;

constexpr unsigned QS_BYTES = 40960, QS_W = 24576;
constexpr unsigned SPARE = 155648, LDS_TOTAL = 163840, ML_OFF = 98304, VOTE_OFF = 101376;
constexpr float L_MIN = 9.313225746154785e-10f, L_MAX = 1048576.0f;
DI constexpr unsigned kv_slot(int s) { return (unsigned)((s + 3) & 3) * QS_BYTES; }
DI unsigned qblk(unsigned blk) { return blk < 40 ? 2 * QS_BYTES + blk * 1024 : SPARE + (blk - 40) * 1024; }
constexpr unsigned P2_OSLOT = 12288, P2_WSLOT = 18432, P2_WBASE = 53248, IMG_STRIDE = 592, IMG_BYTES = 96 * IMG_STRIDE;
DI unsigned p2_oslot(unsigned i) { return (i & 3) * P2_OSLOT; }
DI unsigned p2_wslot(unsigned i) { return P2_WBASE + ((i + 3) % 6) * P2_WSLOT; }

DI unsigned rfl(unsigned x) { return __builtin_amdgcn_readfirstlane(x); }
DI unsigned cvtpk(float lo, float hi) { f32x2 v = {lo, hi}; bf16x2 b = __builtin_convertvector(v, bf16x2); return __builtin_bit_cast(unsigned, b); }
DI bf16x8 pack8(const f32x16& x, int s) {
  u32x4 p = {cvtpk(x[8 * s], x[8 * s + 1]), cvtpk(x[8 * s + 2], x[8 * s + 3]), cvtpk(x[8 * s + 4], x[8 * s + 5]), cvtpk(x[8 * s + 6], x[8 * s + 7])};
  return __builtin_bit_cast(bf16x8, p);
}
DI bf16x8 lds_rd128(lds_char* base, unsigned off) { return *(lds_bf16x8*)(base + off); }
DI bf16x8 lds_tr2(lds_char* base, unsigned off0, unsigned off1) {
  bf16x4 lo = __builtin_amdgcn_ds_read_tr16_b64_v4bf16((lds_bf16x4*)(base + off0));
  bf16x4 hi = __builtin_amdgcn_ds_read_tr16_b64_v4bf16((lds_bf16x4*)(base + off1));
  return __builtin_shufflevector(lo, hi, 0, 1, 2, 3, 4, 5, 6, 7);
}
template <int POL> DI void dma16(__amdgpu_buffer_rsrc_t r, unsigned voff, unsigned soff, unsigned m0v) {
  if constexpr (POL == 0) asm volatile("s_mov_b32 m0, %3\n\ts_nop 0\n\tbuffer_load_dwordx4 %0, %1, %2 offen lds" ::"v"(voff), "s"(r), "s"(soff), "s"(m0v) : "memory");
  else if constexpr (POL == 1) asm volatile("s_mov_b32 m0, %3\n\ts_nop 0\n\tbuffer_load_dwordx4 %0, %1, %2 offen nt lds" ::"v"(voff), "s"(r), "s"(soff), "s"(m0v) : "memory");
  else asm volatile("s_mov_b32 m0, %3\n\ts_nop 0\n\tbuffer_load_dwordx4 %0, %1, %2 offen sc1 lds" ::"v"(voff), "s"(r), "s"(soff), "s"(m0v) : "memory");
}
template <int N> DI void wait_vm() { asm volatile("s_waitcnt vmcnt(%0)" ::"i"(N) : "memory"); }
DI void bar() { SB(); asm volatile("s_waitcnt lgkmcnt(0)\n\ts_barrier" ::: "memory"); SB(); }
DI float pl32_max(float v) {
  auto r = __builtin_amdgcn_permlane32_swap(__float_as_uint(v), __float_as_uint(v), false, false);
  return fmaxf(__uint_as_float(r[0]), __uint_as_float(r[1]));
}
DI float pl32_sum(float v) {
  auto r = __builtin_amdgcn_permlane32_swap(__float_as_uint(v), __float_as_uint(v), false, false);
  return __uint_as_float(r[0]) + __uint_as_float(r[1]);
}
DI unsigned lane_id() { unsigned l; asm volatile("v_mbcnt_lo_u32_b32 %0, -1, 0\n\tv_mbcnt_hi_u32_b32 %0, -1, %0" : "=v"(l)); return l; }
DI void poll_flags(gu64* flags, unsigned idx0, unsigned stride, unsigned n, unsigned skip, unsigned long long tag, unsigned lane) {
  const bool mine = lane < n && lane != skip;
  gu64* p = flags + (size_t)(idx0 + (mine ? lane : 0) * stride) * 16;
  for (unsigned spins = 0; spins < 400000u; ++spins) {
    const unsigned long long v = __hip_atomic_load(p, __ATOMIC_RELAXED, __HIP_MEMORY_SCOPE_AGENT);
    if (__all(!mine || v == tag)) break;
    __builtin_amdgcn_s_sleep(1);
  }
}

#define OUT_AUX 2
#define ROTMODE 2
#define POL_X 0
#define POL_WQ 0
#define O_ST_AUX 16
#define POL_O 2
#define POL_WO 0

__global__ __launch_bounds__(768, 3) void mha_kernel(const __hip_bfloat16* __restrict__ x, const __hip_bfloat16* __restrict__ kc,
                                                     const __hip_bfloat16* __restrict__ vc, const __hip_bfloat16* __restrict__ wq,
                                                     const __hip_bfloat16* __restrict__ wo, __hip_bfloat16* __restrict__ out,
                                                     char* __restrict__ ws, unsigned long long* diag) {
  __shared__ __attribute__((aligned(1024))) char smem[LDS_TOTAL];
  lds_char* lds = (lds_char*)smem;
  const unsigned ldsb = (unsigned)(size_t)smem;
  const unsigned tid = threadIdx.x, w = rfl(tid >> 6);
  STAMP(0);
  const unsigned xcd = blockIdx.x & 7, jj = blockIdx.x >> 3;
  const unsigned b = 4 * (xcd & 1) + (jj & 3), hd = 8 * (xcd >> 1) + (jj >> 2);
  gu64* flags = (gu64*)(ws + WS_FLAG);
  unsigned long long epoch;
  auto read_epoch = [&]() {
    asm volatile("s_load_dwordx2 %0, %1, 0x0\n\ts_waitcnt lgkmcnt(0)" : "=s"(epoch) : "s"((const char*)(ws + WS_FLAG) + (512 + b) * 128) : "memory");
  };

  const __amdgpu_buffer_rsrc_t rwo = __builtin_amdgcn_make_buffer_rsrc((void*)wo, (short)0, NH * DH * DM * 2, 0x00020000);
  bf16x8 qf[8];
  f32x16 oacc[4];
  float m, l;
  {
    const unsigned lane = tid & 63, r = lane & 31, h = lane >> 5;
    const unsigned t = w >> 1, j = w & 1;
    const unsigned g1 = (lane >> 4) & 1, qq = (lane & 15) >> 2, pp = lane & 3, sp = ((pp & 1) << 1) | (pp >> 1);
    const __amdgpu_buffer_rsrc_t rk = __builtin_amdgcn_make_buffer_rsrc((void*)kc, (short)0, NB * NH * SK * DH * 2, 0x00020000);
    const __amdgpu_buffer_rsrc_t rv = __builtin_amdgcn_make_buffer_rsrc((void*)vc, (short)0, NB * NH * SK * DH * 2, 0x00020000);
    const unsigned skv = (b * NH + hd) * (SK * DH * 2);
    const unsigned vk = (lane >> 4) * 256 + ((lane & 15) ^ (lane >> 4)) * 16;
    const unsigned vv = (lane >> 4) * 256 + ((lane & 15) ^ ((lane >> 4) << 2)) * 16;

    auto kv4 = [&](int s) {
      const unsigned lw = w - 4;
      const unsigned dst = ldsb + kv_slot(s) + lw * 4096u;
      const unsigned so = skv + ((lw >> 1) & 1) * 65536u + (lw & 1) * 4096u + (unsigned)s * 8192u;
#pragma unroll
      for (int p = 0; p < 4; ++p) {
        if (lw < 4) dma16<1>(rk, vk ^ (p << 6), so + p * 1024u, dst + p * 1024u);
        else dma16<1>(rv, vv, so + p * 1024u, dst + p * 1024u);
      }
    };
    const unsigned koff = 3 * (((jj >> 2) + (jj & 3)) % 3);
    if (w >= 4) {
      const unsigned lw = w - 4;
      const __amdgpu_buffer_rsrc_t rx = __builtin_amdgcn_make_buffer_rsrc((void*)x, (short)0, NB * SQ * DM * 2, 0x00020000);
      const __amdgpu_buffer_rsrc_t rw = __builtin_amdgcn_make_buffer_rsrc((void*)wq, (short)0, DM * NH * DH * 2, 0x00020000);
      const unsigned vx = (lane >> 3) * 1152 + (((lane & 7) ^ (lane >> 4) ^ ((lw & 1) << 2)) * 16);
      const unsigned sx = (b * SQ + 8 * lw) * 1152u;
      const unsigned vw = (lane >> 4) * 8192 + (((lane & 15) ^ ((lane >> 4) << 2)) * 16);
      const unsigned sw = 4 * lw * 8192u + hd * 256u;
      auto issue_q = [&](int i) {
        unsigned st = koff + i; st = st >= 9 ? st - 9 : st;
        const unsigned slot = ldsb + (unsigned)(i % 3) * QS_BYTES;
#pragma unroll
        for (int p = 0; p < 3; ++p) dma16<POL_X>(rx, vx, sx + (unsigned)(64 * p) * 1152u + st * 128u, slot + (lw + 8 * p) * 1024u);
#pragma unroll
        for (int p = 0; p < 2; ++p) dma16<POL_WQ>(rw, vw, sw + (st * 64 + 32 * p) * 8192u, slot + QS_W + (lw + 8 * p) * 1024u);
      };
      issue_q(0); issue_q(1);
      wait_vm<5>(); bar(); issue_q(2);
      wait_vm<5>(); bar(); issue_q(3);
      wait_vm<5>(); bar(); issue_q(4); kv4(0);
      wait_vm<9>(); bar(); issue_q(5);
      wait_vm<9>(); bar(); issue_q(6);
      wait_vm<5>(); bar(); issue_q(7);
      wait_vm<5>(); bar(); issue_q(8);
      wait_vm<5>(); bar(); kv4(1);
      wait_vm<4>(); bar(); kv4(2);
      read_epoch();
      bar();
      bar();
    } else {
      read_epoch();
      if (w < 2) {
        unsigned long long pc;
        asm volatile("s_getpc_b64 %0" : "=s"(pc));
        const unsigned off = (jj * 2 + w) * 1024u;
        if (off + 1024u <= CODE_BYTES) {
          u32x4 t;
          const char* p = (const char*)(pc & ~127ull) + off + lane * 16;
          asm volatile("global_load_dwordx4 %0, %1, off\n\ts_waitcnt vmcnt(0)" : "=v"(t) : "v"(p) : "memory");
        }
      }
      const unsigned qh = w >> 1, dh = w & 1;
      const unsigned xa0 = (96 * qh + r) * 128 + ((h ^ ((r >> 1) & 7)) << 4);
      const unsigned wa0 = QS_W + (8 * h + qq) * 256 + ((4 * ((2 * dh) ^ qq) + 2 * g1 + (sp >> 1)) << 4) + (sp & 1) * 8;
      f32x16 qacc[3][2];
#pragma unroll
      for (int a = 0; a < 3; ++a)
#pragma unroll
        for (int i = 0; i < 16; ++i) { qacc[a][0][i] = 0.f; qacc[a][1][i] = 0.f; }
      bf16x8 bA[3], aA[2], bB[3], aB[2];
      unsigned xb, wb;
      auto rd = [&](bf16x8 (&bb)[3], bf16x8 (&aa)[2], int s) {
        const unsigned xs = xb ^ (s << 5), wb1 = wb ^ 64;
        aa[0] = lds_tr2(lds, wb + s * 4096, wb + s * 4096 + 1024);
        bb[0] = lds_rd128(lds, xs); bb[1] = lds_rd128(lds, xs + 4096); bb[2] = lds_rd128(lds, xs + 8192);
        aa[1] = lds_tr2(lds, wb1 + s * 4096, wb1 + s * 4096 + 1024);
      };
      auto mm = [&](bf16x8 (&bb)[3], bf16x8 (&aa)[2]) {
        qacc[0][0] = MFMA32(aa[0], bb[0], qacc[0][0]);
        qacc[1][0] = MFMA32(aa[0], bb[1], qacc[1][0]);
        qacc[2][0] = MFMA32(aa[0], bb[2], qacc[2][0]);
        qacc[0][1] = MFMA32(aa[1], bb[0], qacc[0][1]);
        qacc[1][1] = MFMA32(aa[1], bb[1], qacc[1][1]);
        qacc[2][1] = MFMA32(aa[1], bb[2], qacc[2][1]);
      };
      bar();
      STAMP(1);
      xb = xa0; wb = wa0;
      asm volatile("" : "+v"(xb), "+v"(wb));
      rd(bA, aA, 0);
      auto mix = [&]() { SG_MFMA(1); SG_DSRD(2); SG_MFMA(1); SG_DSRD(2); SG_MFMA(1); SG_DSRD(1); SG_MFMA(1); SG_DSRD(1); SG_MFMA(1); SG_DSRD(1); SG_MFMA(1); SB(); };
#pragma unroll 1
      for (int st = 0; st < 8; ++st) {
        SB(); rd(bB, aB, 1); mm(bA, aA); mix();
        rd(bA, aA, 2); mm(bB, aB); mix();
        rd(bB, aB, 3); mm(bA, aA); mix();
        bar();
        const unsigned so = (unsigned)((st + 1) % 3) * QS_BYTES;
        xb = xa0 + so; wb = wa0 + so;
        asm volatile("" : "+v"(xb), "+v"(wb));
        rd(bA, aA, 0); mm(bB, aB); mix();
      }
      SB(); rd(bB, aB, 1); mm(bA, aA); mix();
      rd(bA, aA, 2); mm(bB, aB); mix();
      rd(bB, aB, 3); mm(bA, aA); mix();
      mm(bB, aB);
      bar();
      STAMP(4);
#pragma unroll
      for (int a = 0; a < 3; ++a)
#pragma unroll
        for (int d = 0; d < 2; ++d) {
#pragma unroll
          for (int i = 0; i < 16; ++i) qacc[a][d][i] *= QSCALE;
#pragma unroll
          for (int s2 = 0; s2 < 2; ++s2) {
            const unsigned blk = (3 * qh + a) * 8 + 4 * dh + 2 * d + s2;
            *(lds_bf16x8*)(lds + qblk(blk) + lane * 16) = pack8(qacc[a][d], s2);
          }
        }
      bar();
    }
#pragma unroll
    for (int i = 0; i < 8; ++i) qf[i] = lds_rd128(lds, qblk(t * 8 + i) + lane * 16);

    const unsigned grp = w >> 2;
    float l0, l1;
    bool safe = false;
    const unsigned ka0 = j * 8192 + r * 256 + ((h ^ (r & 15)) << 4);
    const unsigned va0 = (16384 + j * 8192 + (4 * h + qq) * 256 + ((4 * qq + 2 * g1 + (sp >> 1)) << 4) + (sp & 1) * 8) ^ (j << 7);
    const unsigned vkx = vk ^ ((w & 3) << 6);
    const unsigned sp0 = skv + ((w >> 3) & 1) * 65536u + (w & 7) * 1024u;
    const unsigned sp1 = skv + (((w + 12) >> 3) & 1) * 65536u + ((w + 12) & 7) * 1024u;
    const unsigned sp2 = skv + (((w + 24) >> 3) & 1) * 65536u + ((w + 24) & 7) * 1024u;

    bf16x8 kf[4];
    auto step = [&](auto G_, int s) {
      constexpr int G = decltype(G_)::value;
      constexpr int NP = G == 2 ? 2 : 3;
      unsigned vb = va0 + kv_slot(s), kc0 = ka0 + kv_slot(s), kb = ka0 + kv_slot(s + 1);
      asm volatile("" : "+v"(kb), "+v"(vb), "+v"(kc0));
      __builtin_amdgcn_s_setprio(1);
      f32x16 sc;
      bf16x8 vf[4][2];
      {
        bf16x8 kg[4];
#pragma unroll
        for (int ks = 0; ks < 4; ++ks) kg[ks] = lds_rd128(lds, kc0 ^ ((ks + 4) << 5));
#pragma unroll
        for (int i = 0; i < 16; ++i) sc[i] = 0.f;
#pragma unroll
        for (int ks = 0; ks < 4; ++ks) sc = MFMA32(kf[ks], qf[ks], sc);
#pragma unroll
        for (int d = 0; d < 4; ++d) { const unsigned va = vb ^ (d << 6); vf[d][0] = lds_tr2(lds, va, va + 2048); }
#pragma unroll
        for (int ks = 0; ks < 4; ++ks) sc = MFMA32(kg[ks], qf[ks + 4], sc);
        SG_MFMA(1); SG_DSRD(2); SG_MFMA(1); SG_DSRD(2); SG_MFMA(1); SG_DSRD(2); SG_MFMA(1); SG_DSRD(2);
        SG_MFMA(1); SG_DSRD(2); SG_MFMA(1); SG_DSRD(2); SG_MFMA(2);
        SB();
        asm volatile("" : "+v"(sc));
      }
      __builtin_amdgcn_s_setprio(0);
      if constexpr (G == 1) { if (s <= 5) wait_vm<NP>(); else if (s == 6) wait_vm<0>(); }
      bar();
#pragma unroll
      for (int d = 0; d < 4; ++d) { const unsigned va = vb ^ (d << 6); vf[d][1] = lds_tr2(lds, va + 4096, va + 4096 + 2048); }
      SB();
      if (safe) {
        const float t0 = fmaxf(fmaxf(sc[0], sc[1]), sc[2]), t1 = fmaxf(fmaxf(sc[3], sc[4]), sc[5]), t2 = fmaxf(fmaxf(sc[6], sc[7]), sc[8]);
        const float t3 = fmaxf(fmaxf(sc[9], sc[10]), sc[11]), t4 = fmaxf(fmaxf(sc[12], sc[13]), sc[14]);
        float tm = fmaxf(fmaxf(fmaxf(t0, t1), t2), fmaxf(fmaxf(t3, t4), sc[15]));
        tm = pl32_max(tm);
        if (__any(tm > m + THR)) {
          const float mn = fmaxf(m, tm);
          const float al = __builtin_amdgcn_exp2f(m - mn);
          l0 *= al; l1 *= al; m = mn;
#pragma unroll
          for (int d = 0; d < 4; ++d)
#pragma unroll
            for (int i = 0; i < 16; ++i) oacc[d][i] *= al;
        }
#pragma unroll
        for (int i = 0; i < 16; ++i) sc[i] -= m;
      }
      SB();
#pragma unroll
      for (int i = 0; i < 16; ++i) sc[i] = __builtin_amdgcn_exp2f(sc[i]);
      bf16x8 p0 = pack8(sc, 0), p1 = pack8(sc, 1);
#pragma unroll
      for (int i = 0; i < 16; i += 2) { l0 += sc[i]; l1 += sc[i + 1]; }
      asm volatile("" : "+v"(p0), "+v"(p1), "+v"(l0), "+v"(l1));
      if constexpr (G == 0) { if (s <= 5) wait_vm<NP>(); else if (s == 6) wait_vm<0>(); }
      bar();
      __builtin_amdgcn_s_setprio(1);
#pragma unroll
      for (int d = 0; d < 4; ++d) oacc[d] = MFMA32(vf[d][0], p0, oacc[d]);
#pragma unroll
      for (int ks = 0; ks < 4; ++ks) kf[ks] = lds_rd128(lds, kb ^ (ks << 5));
#pragma unroll
      for (int d = 0; d < 4; ++d) oacc[d] = MFMA32(vf[d][1], p1, oacc[d]);
      SG_MFMA(2); SG_DSRD(1); SG_MFMA(1); SG_DSRD(1); SG_MFMA(1); SG_DSRD(1); SG_MFMA(1); SG_DSRD(1); SG_MFMA(3);
      SB();
      asm volatile("" : "+v"(oacc[0]), "+v"(oacc[1]), "+v"(oacc[2]), "+v"(oacc[3]));
      __builtin_amdgcn_s_setprio(0);
      SB();
      if (s <= 4) {
        const unsigned dst = ldsb + kv_slot(s + 3) + w * 1024u, sofs = (unsigned)(s + 3) * 8192u;
        dma16<1>(rk, vkx, sp0 + sofs, dst);
        if constexpr (G == 0) dma16<1>(rk, vkx, sp1 + sofs, dst + 12288u); else dma16<1>(rv, vv, sp1 + sofs, dst + 12288u);
        if constexpr (G != 2) dma16<1>(rv, vv, sp2 + sofs, dst + 24576u);
      }
      if constexpr (G == 2) { if (s <= 4) wait_vm<NP>(); else if (s == 5) wait_vm<0>(); }
      if (s >= 6 && !safe) {
        const unsigned pc = ((b & 3) * 8 + (hd & 7)) * 36 + w * 3 + (s - 6) * 2;
        const unsigned so = (hd >> 3) * (1024u * 1152u) + pc * 1024u;
        dma16<POL_WO>(rwo, lane * 16, so, ldsb + SPARE + (w & 7) * 1024u);
        if (s == 6) dma16<POL_WO>(rwo, lane * 16, so + 1024u, ldsb + SPARE + (w & 7) * 1024u);
      }
      bar();
    };
    auto run = [&](auto G_) {
      constexpr int G = decltype(G_)::value;
      {
        unsigned kb0 = ka0 + kv_slot(0);
        asm volatile("" : "+v"(kb0));
#pragma unroll
        for (int ks = 0; ks < 4; ++ks) kf[ks] = lds_rd128(lds, kb0 ^ (ks << 5));
      }
      if constexpr (G >= 1) bar();
      if constexpr (G == 2) { wait_vm<4>(); bar(); }
#pragma unroll 1
      for (int s = 0; s < 8; ++s) step(G_, s);
      if constexpr (G == 0) bar();
      if constexpr (G <= 1) bar();
    };
#pragma unroll 1
    for (;;) {
#pragma unroll
      for (int d = 0; d < 4; ++d)
#pragma unroll
        for (int i = 0; i < 16; ++i) oacc[d][i] = 0.f;
      m = safe ? -__builtin_inff() : 0.f; l0 = 0.f; l1 = 0.f;
      bar(); STAMP(5);
      if (grp == 0) run(IC<0>{});
      else if (grp == 1) run(IC<1>{});
      else run(IC<2>{});
      STAMP(13);
      const unsigned ln = lane_id();
      l = pl32_sum(l0 + l1);
      const unsigned xw = w * 4096 + ln * 16;
#pragma unroll
      for (int i = 0; i < 4; ++i) *(lds_bf16x8*)(lds + xw + i * 1024) = pack8(oacc[2 + (i >> 1)], i & 1);
      if (ln < 32) { f32x2 ml = {m, l}; *(lds_f32x2*)(lds + ML_OFF + w * 256 + ln * 8) = ml; }
      const bool bad = __any(!(l > L_MIN && l < L_MAX));
      if (ln == 0) *(__attribute__((address_space(3))) unsigned*)(lds + VOTE_OFF + w * 4) = bad ? 1u : 0u;
      bar();
      STAMP(6);
      if (safe) break;
      const unsigned vote = *(__attribute__((address_space(3))) unsigned*)(lds + VOTE_OFF + (ln < 12 ? ln : 0) * 4);
      if (!__any(vote != 0)) break;
      safe = true;
      bar();
      if (w >= 4) {
        const unsigned lw = rfl(w - 4), sk = rfl((b * NH + hd) * (SK * DH * 2));
        const unsigned vo = (ln >> 4) * 256 + (lw < 4 ? ((ln & 15) ^ (ln >> 4)) : ((ln & 15) ^ ((ln >> 4) << 2))) * 16;
        const __amdgpu_buffer_rsrc_t rr = __builtin_amdgcn_make_buffer_rsrc((void*)(lw < 4 ? kc : vc), (short)0, NB * NH * SK * DH * 2, 0x00020000);
#pragma unroll 1
        for (unsigned q = 0; q < 12; ++q) {
          const unsigned st = q >> 2, p = q & 3;
          dma16<1>(rr, lw < 4 ? vo ^ (p << 6) : vo, rfl(sk + ((lw >> 1) & 1) * 65536u + (lw & 1) * 4096u + st * 8192u + p * 1024u), rfl(ldsb + kv_slot(st) + lw * 4096u + p * 1024u));
        }
        wait_vm<0>();
      }
    }
  }

  const unsigned lane = lane_id();
  const unsigned hg = hd >> 3, hl = hd & 7, rh = hl & 1, cq = hl >> 1;
  const unsigned long long tag = epoch + 1;
  const unsigned wg = b * NH + hd;
  auto wo_voff = [&](int i) {
    const unsigned n = 64 * i + lane, p = n / 18, ch = n - 18 * p;
    const unsigned k = (p & 19) | ((p & 4) << 1) | ((p & 8) >> 1);
    return k * 1152 + ch * 16;
  };
  auto wo_issue = [&](const unsigned (&vo)[9], unsigned i) {
    const unsigned kh = w & 1, hh = (hl + (i >> 1)) & 7;
    const unsigned so = ((8 * hg + hh) * 128 + (2 * (i & 1) + kh) * 32) * 1152u + cq * 288u;
    const unsigned dst = ldsb + p2_wslot(i) + kh * 9216u;
#pragma unroll
    for (int p = 0; p < 9; ++p) dma16<POL_WO>(rwo, vo[p], so, dst + p * 1024u);
  };
  unsigned wvo[9];
  if (w == 8 || w == 9) {
#pragma unroll
    for (int p = 0; p < 9; ++p) wvo[p] = wo_voff(p);
  }

  {
    const unsigned r = lane & 31, h = lane >> 5, t = w >> 1, j = w & 1;
    f32x2 o = *(lds_f32x2*)(lds + ML_OFF + (w ^ 1) * 256 + r * 8);
    const float mn = fmaxf(m, o[0]);
    const float f = __builtin_amdgcn_exp2f(m - mn), fo = __builtin_amdgcn_exp2f(o[0] - mn);
    const float inv = 1.0f / (l * f + o[1] * fo);
    const float sa = f * inv, sb = fo * inv;
    const unsigned xr = (w ^ 1) * 4096 + lane * 16;
#pragma unroll
    for (int i = 0; i < 4; ++i) {
      const u32x4 v = __builtin_bit_cast(u32x4, lds_rd128(lds, xr + i * 1024));
#pragma unroll
      for (int e = 0; e < 4; ++e) {
        oacc[i >> 1][8 * (i & 1) + 2 * e] = oacc[i >> 1][8 * (i & 1) + 2 * e] * sa + __uint_as_float(v[e] << 16) * sb;
        oacc[i >> 1][8 * (i & 1) + 2 * e + 1] = oacc[i >> 1][8 * (i & 1) + 2 * e + 1] * sa + __uint_as_float(v[e] & 0xffff0000u) * sb;
      }
    }
    const __amdgpu_buffer_rsrc_t ro = __builtin_amdgcn_make_buffer_rsrc((void*)(ws + WS_O), (short)0, O_BYTES, 0x00020000);
    const unsigned so = ((wg * 6 + t) * 8 + 4 * j) * 1024u;
#pragma unroll
    for (int i = 0; i < 4; ++i)
      __builtin_amdgcn_raw_buffer_store_b128(__builtin_bit_cast(u32x4, pack8(oacc[i >> 1], i & 1)), ro, so + lane * 16 + i * 1024, 0, O_ST_AUX);
    STAMP(7);
    if (w == 8 || w == 9) { wo_issue(wvo, 0); wo_issue(wvo, 1); wait_vm<18>(); } else wait_vm<0>();
    STAMP(8);
    bar();
    if (tid == 0) __hip_atomic_store(flags + (size_t)wg * 16, tag, __ATOMIC_RELAXED, __HIP_MEMORY_SCOPE_AGENT);
  }
  STAMP(14);

  f32x4 acc[3][5];
#pragma unroll
  for (int a = 0; a < 3; ++a)
#pragma unroll
    for (int n = 0; n < 5; ++n) acc[a][n] = f32x4{0.f, 0.f, 0.f, 0.f};
  const unsigned nh = (w >> 2) & 1, kh = w & 1, qh = (w >> 1) & 1;
  if (w >= 10) {
    const __amdgpu_buffer_rsrc_t ro = __builtin_amdgcn_make_buffer_rsrc((void*)(ws + WS_O), (short)0, O_BYTES, 0x00020000);
    auto o_issue = [&](unsigned i) {
      const unsigned hh = (hl + (i >> 1)) & 7;
      const unsigned so = (((b * NH + 8 * hg + hh) * 6 + 3 * rh) * 8 + 2 * (2 * (i & 1) + kh)) * 1024u;
      const unsigned dst = ldsb + p2_oslot(i) + kh * 6144u;
#pragma unroll
      for (int q = 0; q < 3; ++q) { dma16<POL_O>(ro, lane * 16, so + q * 8192u, dst + q * 2048u); dma16<POL_O>(ro, lane * 16, so + q * 8192u + 1024u, dst + q * 2048u + 1024u); }
    };
    gu64* fp = flags + (size_t)(b * NH + 8 * hg + (lane & 7)) * 16;
    unsigned long long pv;
    auto poll_issue = [&]() { asm volatile("global_load_dwordx2 %0, %1, off sc1" : "=v"(pv) : "v"(fp) : "memory"); };
    unsigned rdy = 1u << hl;
    auto poll_take = [&]() {
      asm volatile("s_waitcnt vmcnt(0)" : "+v"(pv) :: "memory");
      rdy |= (unsigned)__ballot(pv == tag) & 0xffu;
    };
    o_issue(0); o_issue(1);
    poll_issue();
    STAMPW(10, 22);
    bool pending = true;
    unsigned spins = 0;
#pragma unroll 1
    for (unsigned i = 0; i < 16; ++i) {
      if (pending) wait_vm<7>(); else if (i <= 14) wait_vm<6>(); else wait_vm<0>();
      if (i == 0) STAMPW(10, 23);
      bar();
      if (i + 2 < 16) {
        const unsigned hh = (hl + ((i + 2) >> 1)) & 7;
        if (pending) { poll_take(); pending = false; }
        while (!((rdy >> hh) & 1) && spins < 400000u) { __builtin_amdgcn_s_sleep(1); poll_issue(); poll_take(); ++spins; }
        o_issue(i + 2);
      }
    }
  } else if (w >= 8) {
    wo_issue(wvo, 2); wo_issue(wvo, 3); wo_issue(wvo, 4);
#pragma unroll 1
    for (unsigned i = 0; i < 16; ++i) {
      if (i <= 11) wait_vm<36>(); else if (i == 12) wait_vm<27>(); else if (i == 13) wait_vm<18>(); else if (i == 14) wait_vm<9>(); else wait_vm<0>();
      bar();
      if (i + 5 < 16) wo_issue(wvo, i + 5);
    }
  } else {
    const unsigned c = lane & 15, g = lane >> 4;
    unsigned ba[3];
#pragma unroll
    for (int a = 0; a < 3; ++a) {
      const unsigned qi = 3 * qh + a;
      ba[a] = kh * 6144 + ((qi >> 1) * 2 + (g >> 1)) * 1024 + (16 * (qi & 1) + c + 32 * (g & 1)) * 16;
    }
    const unsigned aa = kh * 9216 + ((g >> 1) * 16 + (g & 1) * 4 + (c >> 2)) * 288 + 5 * nh * 32 + (c & 3) * 8;
    bf16x8 bX[3], aX[5], bY[3], aY[5];
    auto rd = [&](auto NT_, bf16x8 (&bb)[3], bf16x8 (&av)[5], unsigned i) {
      constexpr int NT = decltype(NT_)::value;
      unsigned ob = p2_oslot(i), wb = aa + p2_wslot(i);
      asm volatile("" : "+v"(wb));
#pragma unroll
      for (int a = 0; a < 3; ++a) bb[a] = lds_rd128(lds, ba[a] + ob);
#pragma unroll
      for (int n = 0; n < NT; ++n) av[n] = lds_tr2(lds, wb + n * 32, wb + n * 32 + 2304);
    };
    auto mm = [&](auto NT_, bf16x8 (&bb)[3], bf16x8 (&av)[5]) {
      constexpr int NT = decltype(NT_)::value;
#pragma unroll
      for (int n = 0; n < NT; ++n)
#pragma unroll
        for (int a = 0; a < 3; ++a) acc[a][n] = MFMA16(av[n], bb[a], acc[a][n]);
    };
    unsigned long long t0 = 0;
    auto mix = [&](auto NT_) {
      constexpr int NT = decltype(NT_)::value;
#pragma unroll
      for (int k = 0; k < 3 + 2 * NT - (NT == 4 ? 0 : 0); ++k) {
        __builtin_amdgcn_sched_group_barrier(0x008, 1, 0);
        __builtin_amdgcn_sched_group_barrier(0x100, 1, 0);
      }
      __builtin_amdgcn_sched_group_barrier(0x008, 3 * NT, 0);
    };
    auto gemm = [&](auto NT_) {
      bar();
      STAMP(15);
      rd(NT_, bX, aX, 0);
#pragma unroll 1
      for (unsigned i = 1; i < 15; i += 2) {
        bar(); rd(NT_, bY, aY, i); mm(NT_, bX, aX); mix(NT_);
        bar(); rd(NT_, bX, aX, i + 1); mm(NT_, bY, aY); mix(NT_);
      }
      bar(); rd(NT_, bY, aY, 15); mm(NT_, bX, aX); mix(NT_);
      SB(); mm(NT_, bY, aY);
    };
    if (nh == 0) gemm(IC<5>{}); else gemm(IC<4>{});
    WREC(44, TNOW() - t0);
  }
  STAMP(16);
  bar();
  if (w < 8) {
    const unsigned c = lane & 15, g = lane >> 4;
    const unsigned base = kh * IMG_BYTES + (48 * qh + c) * IMG_STRIDE + (80 * nh + 4 * g) * 4;
#pragma unroll
    for (int a = 0; a < 3; ++a)
#pragma unroll
      for (int n = 0; n < 5; ++n)
        if (n < 4 || nh == 0) *(lds_f32x4*)(lds + base + a * 16 * IMG_STRIDE + n * 64) = acc[a][n];
  }
  bar();
  {
    const __amdgpu_buffer_rsrc_t rp = __builtin_amdgcn_make_buffer_rsrc((void*)(ws + WS_PART), (short)0, PART_BYTES, 0x00020000);
    const unsigned pbase = ((b * 8 + hl) * 4 + hg) * 4 * PART_BLK;
#pragma unroll 1
    for (unsigned n = tid; n < 72 * 18; n += 768) {
      const unsigned rr = n / 18, ch = n - rr * 18;
      unsigned dst = rr / 24; const unsigned row = rr - dst * 24;
      dst += dst >= hg ? 1 : 0;
      const unsigned a0 = (24 * dst + row) * IMG_STRIDE + ch * 32;
      const f32x4 v0 = *(lds_f32x4*)(lds + a0) + *(lds_f32x4*)(lds + IMG_BYTES + a0);
      const f32x4 v1 = *(lds_f32x4*)(lds + a0 + 16) + *(lds_f32x4*)(lds + IMG_BYTES + a0 + 16);
      const u32x4 pk = {cvtpk(v0[0], v0[1]), cvtpk(v0[2], v0[3]), cvtpk(v1[0], v1[1]), cvtpk(v1[2], v1[3])};
      __builtin_amdgcn_raw_buffer_store_b128(pk, rp, dst * PART_BLK + row * 288 + ch * 16 + pbase, 0, 16);
    }
    wait_vm<0>();
    bar();
    if (tid == 0) __hip_atomic_store(flags + (size_t)(256 + wg) * 16, tag, __ATOMIC_RELAXED, __HIP_MEMORY_SCOPE_AGENT);
    STAMP(17);
    if (w == 0) poll_flags(flags, 256 + b * NH + hl, 8, 4, hg, tag, lane);
    bar();
    STAMP(18);
    if (tid < 24 * 18) {
      const unsigned row = tid / 18, ch = tid - row * 18;
      u32x4 u[3];
#pragma unroll
      for (int k = 0; k < 3; ++k) {
        const unsigned src = k + (k >= (int)hg ? 1 : 0);
        u[k] = __builtin_amdgcn_raw_buffer_load_b128(rp, (((b * 8 + hl) * 4 + src) * 4 + hg) * PART_BLK + row * 288 + ch * 16, 0, 16);
      }
      const unsigned a0 = (24 * hg + row) * IMG_STRIDE + ch * 32;
      f32x4 v0 = *(lds_f32x4*)(lds + a0) + *(lds_f32x4*)(lds + IMG_BYTES + a0);
      f32x4 v1 = *(lds_f32x4*)(lds + a0 + 16) + *(lds_f32x4*)(lds + IMG_BYTES + a0 + 16);
#pragma unroll
      for (int k = 0; k < 3; ++k) {
        v0[0] += __uint_as_float(u[k][0] << 16); v0[1] += __uint_as_float(u[k][0] & 0xffff0000u);
        v0[2] += __uint_as_float(u[k][1] << 16); v0[3] += __uint_as_float(u[k][1] & 0xffff0000u);
        v1[0] += __uint_as_float(u[k][2] << 16); v1[1] += __uint_as_float(u[k][2] & 0xffff0000u);
        v1[2] += __uint_as_float(u[k][3] << 16); v1[3] += __uint_as_float(u[k][3] & 0xffff0000u);
      }
      const u32x4 o = {cvtpk(v0[0], v0[1]), cvtpk(v0[2], v0[3]), cvtpk(v1[0], v1[1]), cvtpk(v1[2], v1[3])};
      const __amdgpu_buffer_rsrc_t rout = __builtin_amdgcn_make_buffer_rsrc((void*)out, (short)0, NB * SQ * DM * 2, 0x00020000);
      __builtin_amdgcn_raw_buffer_store_b128(o, rout, ((b * SQ + 96 * rh + 24 * hg + row) * DM + 144 * cq + 8 * ch) * 2, 0, OUT_AUX);
    }
    if (tid == 0 && hd == 0) __hip_atomic_store(flags + (size_t)(512 + b) * 16, tag, __ATOMIC_RELAXED, __HIP_MEMORY_SCOPE_AGENT);
  }
}

extern "C" void kernel_launch(void* const* d_in, const int* in_sizes, int n_in, void* d_out, int out_size, void* d_ws, size_t ws_size,
                              hipStream_t stream) {
  const __hip_bfloat16* x = (const __hip_bfloat16*)d_in[0];
  const __hip_bfloat16* kc = (const __hip_bfloat16*)d_in[1];
  const __hip_bfloat16* vc = (const __hip_bfloat16*)d_in[2];
  const __hip_bfloat16* wq = (const __hip_bfloat16*)d_in[3];
  const __hip_bfloat16* wo = (const __hip_bfloat16*)d_in[4];
  unsigned long long* diag = (unsigned long long*)((char*)d_ws + (64u << 20));
  mha_kernel<<<256, 768, 0, stream>>>(x, kc, vc, wq, wo, (__hip_bfloat16*)d_out, (char*)d_ws, diag);
}
```

```cpp
#include <hip/hip_runtime.h>
#include <hip/hip_bf16.h>
#include <type_traits>

typedef __bf16 bf16x8 __attribute__((ext_vector_type(8)));
typedef __bf16 bf16x4 __attribute__((ext_vector_type(4)));
typedef __bf16 bf16x2 __attribute__((ext_vector_type(2)));
typedef float f32x16 __attribute__((ext_vector_type(16)));
typedef float f32x4 __attribute__((ext_vector_type(4)));
typedef float f32x2 __attribute__((ext_vector_type(2)));
typedef unsigned u32x4 __attribute__((ext_vector_type(4)));
typedef unsigned u32x2 __attribute__((ext_vector_type(2)));
typedef __attribute__((address_space(3))) char lds_char;
typedef __attribute__((address_space(3))) bf16x4 lds_bf16x4;
typedef __attribute__((address_space(3))) bf16x8 lds_bf16x8;
typedef __attribute__((address_space(3))) f32x4 lds_f32x4;
typedef __attribute__((address_space(3))) f32x2 lds_f32x2;
typedef __attribute__((address_space(1))) unsigned long long gu64;

#define DI __device__ __forceinline__
#define MFMA32(a, b, c) __builtin_amdgcn_mfma_f32_32x32x16_bf16((a), (b), (c), 0, 0, 0)
#define MFMA16(a, b, c) __builtin_amdgcn_mfma_f32_16x16x32_bf16((a), (b), (c), 0, 0, 0)
#define SB() __builtin_amdgcn_sched_barrier(0)
template <int V> using IC = std::integral_constant<int, V>;
#define SG_MFMA(n) __builtin_amdgcn_sched_group_barrier(0x008, (n), 0)
#define SG_DSRD(n) __builtin_amdgcn_sched_group_barrier(0x100, (n), 0)

#define STAMPW(wv, i) do {} while (0)
#define STAMP(i) do {} while (0)
#define WREC(i, v) do {} while (0)
#define TNOW() 0ull

constexpr int NB = 8, SQ = 192, DM = 576, NH = 32, SK = 512, DH = 128;
constexpr float QSCALE = 0.08838834764831845f * 1.4426950408889634f;
constexpr float THR = 8.0f;

constexpr unsigned CODE_BYTES = 26 * 1024;
constexpr size_t WS_O = 0;
constexpr size_t WS_PART = 16u << 20;
constexpr size_t WS_FLAG = 32u << 20;
constexpr unsigned PART_BLK = 24 * 144 * 2;
constexpr unsigned O_BYTES = NB * NH * 6 * 8 * 1024, PART_BYTES = 8 * 8 * 4 * 4 * PART_BLK;

constexpr unsigned QS_BYTES = 40960, QS_W = 24576;
constexpr unsigned SPARE = 155648, LDS_TOTAL = 163840, ML_OFF = 98304, VOTE_OFF = 101376;
constexpr float L_MIN = 9.313225746154785e-10f, L_MAX = 1048576.0f;
DI constexpr unsigned kv_slot(int s) { return (unsigned)((s + 3) & 3) * QS_BYTES; }
DI unsigned qblk(unsigned blk) { return blk < 40 ? 2 * QS_BYTES + blk * 1024 : SPARE + (blk - 40) * 1024; }
constexpr unsigned P2_OSLOT = 12288, P2_WSLOT = 18432, P2_WBASE = 53248, IMG_STRIDE = 592, IMG_BYTES = 96 * IMG_STRIDE;
DI unsigned p2_oslot(unsigned i) { return (i & 3) * P2_OSLOT; }
DI unsigned p2_wslot(unsigned i) { return P2_WBASE + ((i + 3) % 6) * P2_WSLOT; }

DI unsigned rfl(unsigned x) { return __builtin_amdgcn_readfirstlane(x); }
DI unsigned cvtpk(float lo, float hi) { f32x2 v = {lo, hi}; bf16x2 b = __builtin_convertvector(v, bf16x2); return __builtin_bit_cast(unsigned, b); }
DI bf16x8 pack8(const f32x16& x, int s) {
  u32x4 p = {cvtpk(x[8 * s], x[8 * s + 1]), cvtpk(x[8 * s + 2], x[8 * s + 3]), cvtpk(x[8 * s + 4], x[8 * s + 5]), cvtpk(x[8 * s + 6], x[8 * s + 7])};
  return __builtin_bit_cast(bf16x8, p);
}
DI bf16x8 lds_rd128(lds_char* base, unsigned off) { return *(lds_bf16x8*)(base + off); }
DI bf16x8 lds_tr2(lds_char* base, unsigned off0, unsigned off1) {
  bf16x4 lo = __builtin_amdgcn_ds_read_tr16_b64_v4bf16((lds_bf16x4*)(base + off0));
  bf16x4 hi = __builtin_amdgcn_ds_read_tr16_b64_v4bf16((lds_bf16x4*)(base + off1));
  return __builtin_shufflevector(lo, hi, 0, 1, 2, 3, 4, 5, 6, 7);
}
template <int POL> DI void dma16(__amdgpu_buffer_rsrc_t r, unsigned voff, unsigned soff, unsigned m0v) {
  if constexpr (POL == 0) asm volatile("s_mov_b32 m0, %3\n\ts_nop 0\n\tbuffer_load_dwordx4 %0, %1, %2 offen lds" ::"v"(voff), "s"(r), "s"(soff), "s"(m0v) : "memory");
  else if constexpr (POL == 1) asm volatile("s_mov_b32 m0, %3\n\ts_nop 0\n\tbuffer_load_dwordx4 %0, %1, %2 offen nt lds" ::"v"(voff), "s"(r), "s"(soff), "s"(m0v) : "memory");
  else asm volatile("s_mov_b32 m0, %3\n\ts_nop 0\n\tbuffer_load_dwordx4 %0, %1, %2 offen sc1 lds" ::"v"(voff), "s"(r), "s"(soff), "s"(m0v) : "memory");
}
template <int N> DI void wait_vm() { asm volatile("s_waitcnt vmcnt(%0)" ::"i"(N) : "memory"); }
DI void bar() { SB(); asm volatile("s_waitcnt lgkmcnt(0)\n\ts_barrier" ::: "memory"); SB(); }
DI float pl32_max(float v) {
  auto r = __builtin_amdgcn_permlane32_swap(__float_as_uint(v), __float_as_uint(v), false, false);
  return fmaxf(__uint_as_float(r[0]), __uint_as_float(r[1]));
}
DI float pl32_sum(float v) {
  auto r = __builtin_amdgcn_permlane32_swap(__float_as_uint(v), __float_as_uint(v), false, false);
  return __uint_as_float(r[0]) + __uint_as_float(r[1]);
}
DI unsigned lane_id() { unsigned l; asm volatile("v_mbcnt_lo_u32_b32 %0, -1, 0\n\tv_mbcnt_hi_u32_b32 %0, -1, %0" : "=v"(l)); return l; }
DI void poll_flags(gu64* flags, unsigned idx0, unsigned stride, unsigned n, unsigned skip, unsigned long long tag, unsigned lane) {
  const bool mine = lane < n && lane != skip;
  gu64* p = flags + (size_t)(idx0 + (mine ? lane : 0) * stride) * 16;
  for (unsigned spins = 0; spins < 400000u; ++spins) {
    const unsigned long long v = __hip_atomic_load(p, __ATOMIC_RELAXED, __HIP_MEMORY_SCOPE_AGENT);
    if (__all(!mine || v == tag)) break;
    __builtin_amdgcn_s_sleep(1);
  }
}

#define OUT_AUX 2
#define ROTMODE 2
#define POL_X 0
#define POL_WQ 0
#define O_ST_AUX 16
#define POL_O 2
#define POL_WO 0

__global__ __launch_bounds__(768, 3) void mha_kernel(const __hip_bfloat16* __restrict__ x, const __hip_bfloat16* __restrict__ kc,
                                                     const __hip_bfloat16* __restrict__ vc, const __hip_bfloat16* __restrict__ wq,
                                                     const __hip_bfloat16* __restrict__ wo, __hip_bfloat16* __restrict__ out,
                                                     char* __restrict__ ws, unsigned long long* diag) {
  __shared__ __attribute__((aligned(1024))) char smem[LDS_TOTAL];
  lds_char* lds = (lds_char*)smem;
  const unsigned ldsb = (unsigned)(size_t)smem;
  const unsigned tid = threadIdx.x, w = rfl(tid >> 6);
  STAMP(0);
  const unsigned xcd = blockIdx.x & 7, jj = blockIdx.x >> 3;
  const unsigned b = 4 * (xcd & 1) + (jj & 3), hd = 8 * (xcd >> 1) + (jj >> 2);
  gu64* flags = (gu64*)(ws + WS_FLAG);
  unsigned long long epoch;
  auto read_epoch = [&]() {
    asm volatile("s_load_dwordx2 %0, %1, 0x0\n\ts_waitcnt lgkmcnt(0)" : "=s"(epoch) : "s"((const char*)(ws + WS_FLAG) + (512 + b) * 128) : "memory");
  };

  const __amdgpu_buffer_rsrc_t rwo = __builtin_amdgcn_make_buffer_rsrc((void*)wo, (short)0, NH * DH * DM * 2, 0x00020000);
  bf16x8 qf[8];
  f32x16 oacc[4];
  float m, l;
  {
    const unsigned lane = tid & 63, r = lane & 31, h = lane >> 5;
    const unsigned t = w >> 1, j = w & 1;
    const unsigned g1 = (lane >> 4) & 1, qq = (lane & 15) >> 2, pp = lane & 3, sp = ((pp & 1) << 1) | (pp >> 1);
    const __amdgpu_buffer_rsrc_t rk = __builtin_amdgcn_make_buffer_rsrc((void*)kc, (short)0, NB * NH * SK * DH * 2, 0x00020000);
    const __amdgpu_buffer_rsrc_t rv = __builtin_amdgcn_make_buffer_rsrc((void*)vc, (short)0, NB * NH * SK * DH * 2, 0x00020000);
    const unsigned skv = (b * NH + hd) * (SK * DH * 2);
    const unsigned vk = (lane >> 4) * 256 + ((lane & 15) ^ (lane >> 4)) * 16;
    const unsigned vv = (lane >> 4) * 256 + ((lane & 15) ^ ((lane >> 4) << 2)) * 16;

    auto kv4 = [&](int s) {
      const unsigned lw = w - 4;
      const unsigned dst = ldsb + kv_slot(s) + lw * 4096u;
      const unsigned so = skv + ((lw >> 1) & 1) * 65536u + (lw & 1) * 4096u + (unsigned)s * 8192u;
#pragma unroll
      for (int p = 0; p < 4; ++p) {
        if (lw < 4) dma16<1>(rk, vk ^ (p << 6), so + p * 1024u, dst + p * 1024u);
        else dma16<1>(rv, vv, so + p * 1024u, dst + p * 1024u);
      }
    };
    const unsigned koff = 3 * (((jj >> 2) + (jj & 3)) % 3);
    if (w >= 4) {
      const unsigned lw = w - 4;
      const __amdgpu_buffer_rsrc_t rx = __builtin_amdgcn_make_buffer_rsrc((void*)x, (short)0, NB * SQ * DM * 2, 0x00020000);
      const __amdgpu_buffer_rsrc_t rw = __builtin_amdgcn_make_buffer_rsrc((void*)wq, (short)0, DM * NH * DH * 2, 0x00020000);
      const unsigned vx = (lane >> 3) * 1152 + (((lane & 7) ^ (lane >> 4) ^ ((lw & 1) << 2)) * 16);
      const unsigned sx = (b * SQ + 8 * lw) * 1152u;
      const unsigned vw = (lane >> 4) * 8192 + (((lane & 15) ^ ((lane >> 4) << 2)) * 16);
      const unsigned sw = 4 * lw * 8192u + hd * 256u;
      auto issue_q = [&](int i) {
        unsigned st = koff + i; st = st >= 9 ? st - 9 : st;
        const unsigned slot = ldsb + (unsigned)(i % 3) * QS_BYTES;
#pragma unroll
        for (int p = 0; p < 3; ++p) dma16<POL_X>(rx, vx, sx + (unsigned)(64 * p) * 1152u + st * 128u, slot + (lw + 8 * p) * 1024u);
#pragma unroll
        for (int p = 0; p < 2; ++p) dma16<POL_WQ>(rw, vw, sw + (st * 64 + 32 * p) * 8192u, slot + QS_W + (lw + 8 * p) * 1024u);
      };
      issue_q(0); issue_q(1);
      wait_vm<5>(); bar(); issue_q(2);
      wait_vm<5>(); bar(); issue_q(3);
      wait_vm<5>(); bar(); issue_q(4); kv4(0);
      wait_vm<9>(); bar(); issue_q(5);
      wait_vm<9>(); bar(); issue_q(6);
      wait_vm<5>(); bar(); issue_q(7);
      wait_vm<5>(); bar(); issue_q(8);
      wait_vm<5>(); bar(); kv4(1);
      wait_vm<4>(); bar(); kv4(2);
      read_epoch();
      bar();
      bar();
    } else {
      read_epoch();
      if (w < 2) {
        unsigned long long pc;
        asm volatile("s_getpc_b64 %0" : "=s"(pc));
        const unsigned off = (jj * 2 + w) * 1024u;
        if (off + 1024u <= CODE_BYTES) {
          u32x4 t;
          const char* p = (const char*)(pc & ~127ull) + off + lane * 16;
          asm volatile("global_load_dwordx4 %0, %1, off\n\ts_waitcnt vmcnt(0)" : "=v"(t) : "v"(p) : "memory");
        }
      }
      if (w == 2) {
        const unsigned hg_ = hd >> 3, hl_ = hd & 7, wg_ = b * NH + hd;
        const char* p = (const char*)ws + WS_FLAG;
        if (lane == 0) p = (const char*)kc + (size_t)wg_ * (SK * DH * 2);
        if (lane == 1) p = (const char*)vc + (size_t)wg_ * (SK * DH * 2);
        if (lane == 2) p = (const char*)ws + WS_O + (size_t)wg_ * 49152;
        if (lane == 3) p = (const char*)ws + WS_O + (size_t)(b * NH + 8 * hg_) * 49152;
        if (lane == 4) p = (const char*)ws + WS_O + (size_t)(b * NH + 8 * hg_ + 8) * 49152 - 64;
        if (lane >= 5 && lane < 9) p = (const char*)ws + WS_PART + (size_t)(((b * 8 + hl_) * 4 + (lane - 5)) * 4) * PART_BLK;
        if (lane == 9) p = (const char*)wo + (size_t)hg_ * (1024 * 1152);
        if (lane == 10) p = (const char*)wo + (size_t)(hg_ + 1) * (1024 * 1152) - 64;
        if (lane == 11) p = (const char*)out + (size_t)(b * SQ) * (DM * 2);
        if (lane == 12) p = (const char*)kc + (size_t)(wg_ + 1) * (SK * DH * 2) - 64;
        if (lane == 13) p = (const char*)vc + (size_t)(wg_ + 1) * (SK * DH * 2) - 64;
        unsigned t;
        asm volatile("global_load_dword %0, %1, off\n\ts_waitcnt vmcnt(0)" : "=v"(t) : "v"(p) : "memory");
      }
      const unsigned qh = w >> 1, dh = w & 1;
      const unsigned xa0 = (96 * qh + r) * 128 + ((h ^ ((r >> 1) & 7)) << 4);
      const unsigned wa0 = QS_W + (8 * h + qq) * 256 + ((4 * ((2 * dh) ^ qq) + 2 * g1 + (sp >> 1)) << 4) + (sp & 1) * 8;
      f32x16 qacc[3][2];
#pragma unroll
      for (int a = 0; a < 3; ++a)
#pragma unroll
        for (int i = 0; i < 16; ++i) { qacc[a][0][i] = 0.f; qacc[a][1][i] = 0.f; }
      bf16x8 bA[3], aA[2], bB[3], aB[2];
      unsigned xb, wb;
      auto rd = [&](bf16x8 (&bb)[3], bf16x8 (&aa)[2], int s) {
        const unsigned xs = xb ^ (s << 5), wb1 = wb ^ 64;
        aa[0] = lds_tr2(lds, wb + s * 4096, wb + s * 4096 + 1024);
        bb[0] = lds_rd128(lds, xs); bb[1] = lds_rd128(lds, xs + 4096); bb[2] = lds_rd128(lds, xs + 8192);
        aa[1] = lds_tr2(lds, wb1 + s * 4096, wb1 + s * 4096 + 1024);
      };
      auto mm = [&](bf16x8 (&bb)[3], bf16x8 (&aa)[2]) {
        qacc[0][0] = MFMA32(aa[0], bb[0], qacc[0][0]);
        qacc[1][0] = MFMA32(aa[0], bb[1], qacc[1][0]);
        qacc[2][0] = MFMA32(aa[0], bb[2], qacc[2][0]);
        qacc[0][1] = MFMA32(aa[1], bb[0], qacc[0][1]);
        qacc[1][1] = MFMA32(aa[1], bb[1], qacc[1][1]);
        qacc[2][1] = MFMA32(aa[1], bb[2], qacc[2][1]);
      };
      bar();
      STAMP(1);
      xb = xa0; wb = wa0;
      asm volatile("" : "+v"(xb), "+v"(wb));
      rd(bA, aA, 0);
      auto mix = [&]() { SG_MFMA(1); SG_DSRD(2); SG_MFMA(1); SG_DSRD(2); SG_MFMA(1); SG_DSRD(1); SG_MFMA(1); SG_DSRD(1); SG_MFMA(1); SG_DSRD(1); SG_MFMA(1); SB(); };
#pragma unroll 1
      for (int st = 0; st < 8; ++st) {
        SB(); rd(bB, aB, 1); mm(bA, aA); mix();
        rd(bA, aA, 2); mm(bB, aB); mix();
        rd(bB, aB, 3); mm(bA, aA); mix();
        bar();
        const unsigned so = (unsigned)((st + 1) % 3) * QS_BYTES;
        xb = xa0 + so; wb = wa0 + so;
        asm volatile("" : "+v"(xb), "+v"(wb));
        rd(bA, aA, 0); mm(bB, aB); mix();
      }
      SB(); rd(bB, aB, 1); mm(bA, aA); mix();
      rd(bA, aA, 2); mm(bB, aB); mix();
      rd(bB, aB, 3); mm(bA, aA); mix();
      mm(bB, aB);
      bar();
      STAMP(4);
#pragma unroll
      for (int a = 0; a < 3; ++a)
#pragma unroll
        for (int d = 0; d < 2; ++d) {
#pragma unroll
          for (int i = 0; i < 16; ++i) qacc[a][d][i] *= QSCALE;
#pragma unroll
          for (int s2 = 0; s2 < 2; ++s2) {
            const unsigned blk = (3 * qh + a) * 8 + 4 * dh + 2 * d + s2;
            *(lds_bf16x8*)(lds + qblk(blk) + lane * 16) = pack8(qacc[a][d], s2);
          }
        }
      bar();
    }
#pragma unroll
    for (int i = 0; i < 8; ++i) qf[i] = lds_rd128(lds, qblk(t * 8 + i) + lane * 16);

    const unsigned grp = w >> 2;
    float l0, l1;
    bool safe = false;
    const unsigned ka0 = j * 8192 + r * 256 + ((h ^ (r & 15)) << 4);
    const unsigned va0 = (16384 + j * 8192 + (4 * h + qq) * 256 + ((4 * qq + 2 * g1 + (sp >> 1)) << 4) + (sp & 1) * 8) ^ (j << 7);
    const unsigned vkx = vk ^ ((w & 3) << 6);
    const unsigned sp0 = skv + ((w >> 3) & 1) * 65536u + (w & 7) * 1024u;
    const unsigned sp1 = skv + (((w + 12) >> 3) & 1) * 65536u + ((w + 12) & 7) * 1024u;
    const unsigned sp2 = skv + (((w + 24) >> 3) & 1) * 65536u + ((w + 24) & 7) * 1024u;

    bf16x8 kf[4];
    auto step = [&](auto G_, int s) {
      constexpr int G = decltype(G_)::value;
      constexpr int NP = G == 2 ? 2 : 3;
      unsigned vb = va0 + kv_slot(s), kc0 = ka0 + kv_slot(s), kb = ka0 + kv_slot(s + 1);
      asm volatile("" : "+v"(kb), "+v"(vb), "+v"(kc0));
      __builtin_amdgcn_s_setprio(1);
      f32x16 sc;
      bf16x8 vf[4][2];
      {
        bf16x8 kg[4];
#pragma unroll
        for (int ks = 0; ks < 4; ++ks) kg[ks] = lds_rd128(lds, kc0 ^ ((ks + 4) << 5));
#pragma unroll
        for (int i = 0; i < 16; ++i) sc[i] = 0.f;
#pragma unroll
        for (int ks = 0; ks < 4; ++ks) sc = MFMA32(kf[ks], qf[ks], sc);
#pragma unroll
        for (int d = 0; d < 4; ++d) { const unsigned va = vb ^ (d << 6); vf[d][0] = lds_tr2(lds, va, va + 2048); }
#pragma unroll
        for (int ks = 0; ks < 4; ++ks) sc = MFMA32(kg[ks], qf[ks + 4], sc);
        SG_MFMA(1); SG_DSRD(2); SG_MFMA(1); SG_DSRD(2); SG_MFMA(1); SG_DSRD(2); SG_MFMA(1); SG_DSRD(2);
        SG_MFMA(1); SG_DSRD(2); SG_MFMA(1); SG_DSRD(2); SG_MFMA(2);
        SB();
        asm volatile("" : "+v"(sc));
      }
      __builtin_amdgcn_s_setprio(0);
      if constexpr (G == 1) { if (s <= 5) wait_vm<NP>(); else if (s == 6) wait_vm<0>(); }
      bar();
#pragma unroll
      for (int d = 0; d < 4; ++d) { const unsigned va = vb ^ (d << 6); vf[d][1] = lds_tr2(lds, va + 4096, va + 4096 + 2048); }
      SB();
      if (safe) {
        const float t0 = fmaxf(fmaxf(sc[0], sc[1]), sc[2]), t1 = fmaxf(fmaxf(sc[3], sc[4]), sc[5]), t2 = fmaxf(fmaxf(sc[6], sc[7]), sc[8]);
        const float t3 = fmaxf(fmaxf(sc[9], sc[10]), sc[11]), t4 = fmaxf(fmaxf(sc[12], sc[13]), sc[14]);
        float tm = fmaxf(fmaxf(fmaxf(t0, t1), t2), fmaxf(fmaxf(t3, t4), sc[15]));
        tm = pl32_max(tm);
        if (__any(tm > m + THR)) {
          const float mn = fmaxf(m, tm);
          const float al = __builtin_amdgcn_exp2f(m - mn);
          l0 *= al; l1 *= al; m = mn;
#pragma unroll
          for (int d = 0; d < 4; ++d)
#pragma unroll
            for (int i = 0; i < 16; ++i) oacc[d][i] *= al;
        }
#pragma unroll
        for (int i = 0; i < 16; ++i) sc[i] -= m;
      }
      SB();
#pragma unroll
      for (int i = 0; i < 16; ++i) sc[i] = __builtin_amdgcn_exp2f(sc[i]);
      bf16x8 p0 = pack8(sc, 0), p1 = pack8(sc, 1);
#pragma unroll
      for (int i = 0; i < 16; i += 2) { l0 += sc[i]; l1 += sc[i + 1]; }
      asm volatile("" : "+v"(p0), "+v"(p1), "+v"(l0), "+v"(l1));
      if constexpr (G == 0) { if (s <= 5) wait_vm<NP>(); else if (s == 6) wait_vm<0>(); }
      bar();
      __builtin_amdgcn_s_setprio(1);
#pragma unroll
      for (int d = 0; d < 4; ++d) oacc[d] = MFMA32(vf[d][0], p0, oacc[d]);
#pragma unroll
      for (int ks = 0; ks < 4; ++ks) kf[ks] = lds_rd128(lds, kb ^ (ks << 5));
#pragma unroll
      for (int d = 0; d < 4; ++d) oacc[d] = MFMA32(vf[d][1], p1, oacc[d]);
      SG_MFMA(2); SG_DSRD(1); SG_MFMA(1); SG_DSRD(1); SG_MFMA(1); SG_DSRD(1); SG_MFMA(1); SG_DSRD(1); SG_MFMA(3);
      SB();
      asm volatile("" : "+v"(oacc[0]), "+v"(oacc[1]), "+v"(oacc[2]), "+v"(oacc[3]));
      __builtin_amdgcn_s_setprio(0);
      SB();
      if (s <= 4) {
        const unsigned dst = ldsb + kv_slot(s + 3) + w * 1024u, sofs = (unsigned)(s + 3) * 8192u;
        dma16<1>(rk, vkx, sp0 + sofs, dst);
        if constexpr (G == 0) dma16<1>(rk, vkx, sp1 + sofs, dst + 12288u); else dma16<1>(rv, vv, sp1 + sofs, dst + 12288u);
        if constexpr (G != 2) dma16<1>(rv, vv, sp2 + sofs, dst + 24576u);
      }
      if constexpr (G == 2) { if (s <= 4) wait_vm<NP>(); else if (s == 5) wait_vm<0>(); }
      if (s >= 6 && !safe) {
        const unsigned pc = ((b & 3) * 8 + (hd & 7)) * 36 + w * 3 + (s - 6) * 2;
        const unsigned so = (hd >> 3) * (1024u * 1152u) + pc * 1024u;
        dma16<POL_WO>(rwo, lane * 16, so, ldsb + SPARE + (w & 7) * 1024u);
        if (s == 6) dma16<POL_WO>(rwo, lane * 16, so + 1024u, ldsb + SPARE + (w & 7) * 1024u);
      }
      bar();
    };
    auto run = [&](auto G_) {
      constexpr int G = decltype(G_)::value;
      {
        unsigned kb0 = ka0 + kv_slot(0);
        asm volatile("" : "+v"(kb0));
#pragma unroll
        for (int ks = 0; ks < 4; ++ks) kf[ks] = lds_rd128(lds, kb0 ^ (ks << 5));
      }
      if constexpr (G >= 1) bar();
      if constexpr (G == 2) { wait_vm<4>(); bar(); }
#pragma unroll 1
      for (int s = 0; s < 8; ++s) step(G_, s);
      if constexpr (G == 0) bar();
      if constexpr (G <= 1) bar();
    };
#pragma unroll 1
    for (;;) {
#pragma unroll
      for (int d = 0; d < 4; ++d)
#pragma unroll
        for (int i = 0; i < 16; ++i) oacc[d][i] = 0.f;
      m = safe ? -__builtin_inff() : 0.f; l0 = 0.f; l1 = 0.f;
      bar(); STAMP(5);
      if (grp == 0) run(IC<0>{});
      else if (grp == 1) run(IC<1>{});
      else run(IC<2>{});
      STAMP(13);
      const unsigned ln = lane_id();
      l = pl32_sum(l0 + l1);
      const unsigned xw = w * 4096 + ln * 16;
#pragma unroll
      for (int i = 0; i < 4; ++i) *(lds_bf16x8*)(lds + xw + i * 1024) = pack8(oacc[2 + (i >> 1)], i & 1);
      if (ln < 32) { f32x2 ml = {m, l}; *(lds_f32x2*)(lds + ML_OFF + w * 256 + ln * 8) = ml; }
      const bool bad = __any(!(l > L_MIN && l < L_MAX));
      if (ln == 0) *(__attribute__((address_space(3))) unsigned*)(lds + VOTE_OFF + w * 4) = bad ? 1u : 0u;
      bar();
      STAMP(6);
      if (safe) break;
      const unsigned vote = *(__attribute__((address_space(3))) unsigned*)(lds + VOTE_OFF + (ln < 12 ? ln : 0) * 4);
      if (!__any(vote != 0)) break;
      safe = true;
      bar();
      if (w >= 4) {
        const unsigned lw = rfl(w - 4), sk = rfl((b * NH + hd) * (SK * DH * 2));
        const unsigned vo = (ln >> 4) * 256 + (lw < 4 ? ((ln & 15) ^ (ln >> 4)) : ((ln & 15) ^ ((ln >> 4) << 2))) * 16;
        const __amdgpu_buffer_rsrc_t rr = __builtin_amdgcn_make_buffer_rsrc((void*)(lw < 4 ? kc : vc), (short)0, NB * NH * SK * DH * 2, 0x00020000);
#pragma unroll 1
        for (unsigned q = 0; q < 12; ++q) {
          const unsigned st = q >> 2, p = q & 3;
          dma16<1>(rr, lw < 4 ? vo ^ (p << 6) : vo, rfl(sk + ((lw >> 1) & 1) * 65536u + (lw & 1) * 4096u + st * 8192u + p * 1024u), rfl(ldsb + kv_slot(st) + lw * 4096u + p * 1024u));
        }
        wait_vm<0>();
      }
    }
  }

  const unsigned lane = lane_id();
  const unsigned hg = hd >> 3, hl = hd & 7, rh = hl & 1, cq = hl >> 1;
  const unsigned long long tag = epoch + 1;
  const unsigned wg = b * NH + hd;
  auto wo_voff = [&](int i) {
    const unsigned n = 64 * i + lane, p = n / 18, ch = n - 18 * p;
    const unsigned k = (p & 19) | ((p & 4) << 1) | ((p & 8) >> 1);
    return k * 1152 + ch * 16;
  };
  auto wo_issue = [&](const unsigned (&vo)[9], unsigned i) {
    const unsigned kh = w & 1, hh = (hl + (i >> 1)) & 7;
    const unsigned so = ((8 * hg + hh) * 128 + (2 * (i & 1) + kh) * 32) * 1152u + cq * 288u;
    const unsigned dst = ldsb + p2_wslot(i) + kh * 9216u;
#pragma unroll
    for (int p = 0; p < 9; ++p) dma16<POL_WO>(rwo, vo[p], so, dst + p * 1024u);
  };
  unsigned wvo[9];
  if (w == 8 || w == 9) {
#pragma unroll
    for (int p = 0; p < 9; ++p) wvo[p] = wo_voff(p);
  }

  {
    const unsigned r = lane & 31, h = lane >> 5, t = w >> 1, j = w & 1;
    f32x2 o = *(lds_f32x2*)(lds + ML_OFF + (w ^ 1) * 256 + r * 8);
    const float mn = fmaxf(m, o[0]);
    const float f = __builtin_amdgcn_exp2f(m - mn), fo = __builtin_amdgcn_exp2f(o[0] - mn);
    const float inv = 1.0f / (l * f + o[1] * fo);
    const float sa = f * inv, sb = fo * inv;
    const unsigned xr = (w ^ 1) * 4096 + lane * 16;
#pragma unroll
    for (int i = 0; i < 4; ++i) {
      const u32x4 v = __builtin_bit_cast(u32x4, lds_rd128(lds, xr + i * 1024));
#pragma unroll
      for (int e = 0; e < 4; ++e) {
        oacc[i >> 1][8 * (i & 1) + 2 * e] = oacc[i >> 1][8 * (i & 1) + 2 * e] * sa + __uint_as_float(v[e] << 16) * sb;
        oacc[i >> 1][8 * (i & 1) + 2 * e + 1] = oacc[i >> 1][8 * (i & 1) + 2 * e + 1] * sa + __uint_as_float(v[e] & 0xffff0000u) * sb;
      }
    }
    const __amdgpu_buffer_rsrc_t ro = __builtin_amdgcn_make_buffer_rsrc((void*)(ws + WS_O), (short)0, O_BYTES, 0x00020000);
    const unsigned so = ((wg * 6 + t) * 8 + 4 * j) * 1024u;
#pragma unroll
    for (int i = 0; i < 4; ++i)
      __builtin_amdgcn_raw_buffer_store_b128(__builtin_bit_cast(u32x4, pack8(oacc[i >> 1], i & 1)), ro, so + lane * 16 + i * 1024, 0, O_ST_AUX);
    STAMP(7);
    if (w == 8 || w == 9) { wo_issue(wvo, 0); wo_issue(wvo, 1); wait_vm<18>(); } else wait_vm<0>();
    STAMP(8);
    bar();
    if (tid == 0) __hip_atomic_store(flags + (size_t)wg * 16, tag, __ATOMIC_RELAXED, __HIP_MEMORY_SCOPE_AGENT);
  }
  STAMP(14);

  f32x4 acc[3][5];
#pragma unroll
  for (int a = 0; a < 3; ++a)
#pragma unroll
    for (int n = 0; n < 5; ++n) acc[a][n] = f32x4{0.f, 0.f, 0.f, 0.f};
  const unsigned nh = (w >> 2) & 1, kh = w & 1, qh = (w >> 1) & 1;
  if (w >= 10) {
    const __amdgpu_buffer_rsrc_t ro = __builtin_amdgcn_make_buffer_rsrc((void*)(ws + WS_O), (short)0, O_BYTES, 0x00020000);
    auto o_issue = [&](unsigned i) {
      const unsigned hh = (hl + (i >> 1)) & 7;
      const unsigned so = (((b * NH + 8 * hg + hh) * 6 + 3 * rh) * 8 + 2 * (2 * (i & 1) + kh)) * 1024u;
      const unsigned dst = ldsb + p2_oslot(i) + kh * 6144u;
#pragma unroll
      for (int q = 0; q < 3; ++q) { dma16<POL_O>(ro, lane * 16, so + q * 8192u, dst + q * 2048u); dma16<POL_O>(ro, lane * 16, so + q * 8192u + 1024u, dst + q * 2048u + 1024u); }
    };
    gu64* fp = flags + (size_t)(b * NH + 8 * hg + (lane & 7)) * 16;
    unsigned long long pv;
    auto poll_issue = [&]() { asm volatile("global_load_dwordx2 %0, %1, off sc1" : "=v"(pv) : "v"(fp) : "memory"); };
    unsigned rdy = 1u << hl;
    auto poll_take = [&]() {
      asm volatile("s_waitcnt vmcnt(0)" : "+v"(pv) :: "memory");
      rdy |= (unsigned)__ballot(pv == tag) & 0xffu;
    };
    o_issue(0); o_issue(1);
    poll_issue();
    STAMPW(10, 22);
    bool pending = true;
    unsigned spins = 0;
#pragma unroll 1
    for (unsigned i = 0; i < 16; ++i) {
      if (pending) wait_vm<7>(); else if (i <= 14) wait_vm<6>(); else wait_vm<0>();
      if (i == 0) STAMPW(10, 23);
      bar();
      if (i + 2 < 16) {
        const unsigned hh = (hl + ((i + 2) >> 1)) & 7;
        if (pending) { poll_take(); pending = false; }
        while (!((rdy >> hh) & 1) && spins < 400000u) { __builtin_amdgcn_s_sleep(1); poll_issue(); poll_take(); ++spins; }
        o_issue(i + 2);
      }
    }
  } else if (w >= 8) {
    wo_issue(wvo, 2); wo_issue(wvo, 3); wo_issue(wvo, 4);
#pragma unroll 1
    for (unsigned i = 0; i < 16; ++i) {
      if (i <= 11) wait_vm<36>(); else if (i == 12) wait_vm<27>(); else if (i == 13) wait_vm<18>(); else if (i == 14) wait_vm<9>(); else wait_vm<0>();
      bar();
      if (i + 5 < 16) wo_issue(wvo, i + 5);
    }
  } else {
    const unsigned c = lane & 15, g = lane >> 4;
    unsigned ba[3];
#pragma unroll
    for (int a = 0; a < 3; ++a) {
      const unsigned qi = 3 * qh + a;
      ba[a] = kh * 6144 + ((qi >> 1) * 2 + (g >> 1)) * 1024 + (16 * (qi & 1) + c + 32 * (g & 1)) * 16;
    }
    const unsigned aa = kh * 9216 + ((g >> 1) * 16 + (g & 1) * 4 + (c >> 2)) * 288 + 5 * nh * 32 + (c & 3) * 8;
    bf16x8 bX[3], aX[5], bY[3], aY[5];
    auto rd = [&](auto NT_, bf16x8 (&bb)[3], bf16x8 (&av)[5], unsigned i) {
      constexpr int NT = decltype(NT_)::value;
      unsigned ob = p2_oslot(i), wb = aa + p2_wslot(i);
      asm volatile("" : "+v"(wb));
#pragma unroll
      for (int a = 0; a < 3; ++a) bb[a] = lds_rd128(lds, ba[a] + ob);
#pragma unroll
      for (int n = 0; n < NT; ++n) av[n] = lds_tr2(lds, wb + n * 32, wb + n * 32 + 2304);
    };
    auto mm = [&](auto NT_, bf16x8 (&bb)[3], bf16x8 (&av)[5]) {
      constexpr int NT = decltype(NT_)::value;
#pragma unroll
      for (int n = 0; n < NT; ++n)
#pragma unroll
        for (int a = 0; a < 3; ++a) acc[a][n] = MFMA16(av[n], bb[a], acc[a][n]);
    };
    unsigned long long t0 = 0;
    auto mix = [&](auto NT_) {
      constexpr int NT = decltype(NT_)::value;
#pragma unroll
      for (int k = 0; k < 3 + 2 * NT - (NT == 4 ? 0 : 0); ++k) {
        __builtin_amdgcn_sched_group_barrier(0x008, 1, 0);
        __builtin_amdgcn_sched_group_barrier(0x100, 1, 0);
      }
      __builtin_amdgcn_sched_group_barrier(0x008, 3 * NT, 0);
    };
    auto gemm = [&](auto NT_) {
      bar();
      STAMP(15);
      rd(NT_, bX, aX, 0);
#pragma unroll 1
      for (unsigned i = 1; i < 15; i += 2) {
        bar(); rd(NT_, bY, aY, i); mm(NT_, bX, aX); mix(NT_);
        bar(); rd(NT_, bX, aX, i + 1); mm(NT_, bY, aY); mix(NT_);
      }
      bar(); rd(NT_, bY, aY, 15); mm(NT_, bX, aX); mix(NT_);
      SB(); mm(NT_, bY, aY);
    };
    if (nh == 0) gemm(IC<5>{}); else gemm(IC<4>{});
    WREC(44, TNOW() - t0);
  }
  STAMP(16);
  bar();
  if (w < 8) {
    const unsigned c = lane & 15, g = lane >> 4;
    const unsigned base = kh * IMG_BYTES + (48 * qh + c) * IMG_STRIDE + (80 * nh + 4 * g) * 4;
#pragma unroll
    for (int a = 0; a < 3; ++a)
#pragma unroll
      for (int n = 0; n < 5; ++n)
        if (n < 4 || nh == 0) *(lds_f32x4*)(lds + base + a * 16 * IMG_STRIDE + n * 64) = acc[a][n];
  }
  bar();
  {
    const __amdgpu_buffer_rsrc_t rp = __builtin_amdgcn_make_buffer_rsrc((void*)(ws + WS_PART), (short)0, PART_BYTES, 0x00020000);
    const unsigned pbase = ((b * 8 + hl) * 4 + hg) * 4 * PART_BLK;
#pragma unroll 1
    for (unsigned n = tid; n < 72 * 18; n += 768) {
      const unsigned rr = n / 18, ch = n - rr * 18;
      unsigned dst = rr / 24; const unsigned row = rr - dst * 24;
      dst += dst >= hg ? 1 : 0;
      const unsigned a0 = (24 * dst + row) * IMG_STRIDE + ch * 32;
      const f32x4 v0 = *(lds_f32x4*)(lds + a0) + *(lds_f32x4*)(lds + IMG_BYTES + a0);
      const f32x4 v1 = *(lds_f32x4*)(lds + a0 + 16) + *(lds_f32x4*)(lds + IMG_BYTES + a0 + 16);
      const u32x4 pk = {cvtpk(v0[0], v0[1]), cvtpk(v0[2], v0[3]), cvtpk(v1[0], v1[1]), cvtpk(v1[2], v1[3])};
      __builtin_amdgcn_raw_buffer_store_b128(pk, rp, dst * PART_BLK + row * 288 + ch * 16 + pbase, 0, 16);
    }
    wait_vm<0>();
    bar();
    if (tid == 0) __hip_atomic_store(flags + (size_t)(256 + wg) * 16, tag, __ATOMIC_RELAXED, __HIP_MEMORY_SCOPE_AGENT);
    STAMP(17);
    if (w == 0) poll_flags(flags, 256 + b * NH + hl, 8, 4, hg, tag, lane);
    bar();
    STAMP(18);
    if (tid < 24 * 18) {
      const unsigned row = tid / 18, ch = tid - row * 18;
      u32x4 u[3];
#pragma unroll
      for (int k = 0; k < 3; ++k) {
        const unsigned src = k + (k >= (int)hg ? 1 : 0);
        u[k] = __builtin_amdgcn_raw_buffer_load_b128(rp, (((b * 8 + hl) * 4 + src) * 4 + hg) * PART_BLK + row * 288 + ch * 16, 0, 16);
      }
      const unsigned a0 = (24 * hg + row) * IMG_STRIDE + ch * 32;
      f32x4 v0 = *(lds_f32x4*)(lds + a0) + *(lds_f32x4*)(lds + IMG_BYTES + a0);
      f32x4 v1 = *(lds_f32x4*)(lds + a0 + 16) + *(lds_f32x4*)(lds + IMG_BYTES + a0 + 16);
#pragma unroll
      for (int k = 0; k < 3; ++k) {
        v0[0] += __uint_as_float(u[k][0] << 16); v0[1] += __uint_as_float(u[k][0] & 0xffff0000u);
        v0[2] += __uint_as_float(u[k][1] << 16); v0[3] += __uint_as_float(u[k][1] & 0xffff0000u);
        v1[0] += __uint_as_float(u[k][2] << 16); v1[1] += __uint_as_float(u[k][2] & 0xffff0000u);
        v1[2] += __uint_as_float(u[k][3] << 16); v1[3] += __uint_as_float(u[k][3] & 0xffff0000u);
      }
      const u32x4 o = {cvtpk(v0[0], v0[1]), cvtpk(v0[2], v0[3]), cvtpk(v1[0], v1[1]), cvtpk(v1[2], v1[3])};
      const __amdgpu_buffer_rsrc_t rout = __builtin_amdgcn_make_buffer_rsrc((void*)out, (short)0, NB * SQ * DM * 2, 0x00020000);
      __builtin_amdgcn_raw_buffer_store_b128(o, rout, ((b * SQ + 96 * rh + 24 * hg + row) * DM + 144 * cq + 8 * ch) * 2, 0, OUT_AUX);
    }
    if (tid == 0 && hd == 0) __hip_atomic_store(flags + (size_t)(512 + b) * 16, tag, __ATOMIC_RELAXED, __HIP_MEMORY_SCOPE_AGENT);
  }
}

extern "C" void kernel_launch(void* const* d_in, const int* in_sizes, int n_in, void* d_out, int out_size, void* d_ws, size_t ws_size,
                              hipStream_t stream) {
  const __hip_bfloat16* x = (const __hip_bfloat16*)d_in[0];
  const __hip_bfloat16* kc = (const __hip_bfloat16*)d_in[1];
  const __hip_bfloat16* vc = (const __hip_bfloat16*)d_in[2];
  const __hip_bfloat16* wq = (const __hip_bfloat16*)d_in[3];
  const __hip_bfloat16* wo = (const __hip_bfloat16*)d_in[4];
  unsigned long long* diag = (unsigned long long*)((char*)d_ws + (64u << 20));
  mha_kernel<<<256, 768, 0, stream>>>(x, kc, vc, wq, wo, (__hip_bfloat16*)d_out, (char*)d_ws, diag);
}
```

```cpp
#define DIAG
#define ST_I -1
#define ST_J 1
#define ST_RANK 128
#include <hip/hip_runtime.h>
#include <hip/hip_bf16.h>
#include <type_traits>

typedef __bf16 bf16x8 __attribute__((ext_vector_type(8)));
typedef __bf16 bf16x4 __attribute__((ext_vector_type(4)));
typedef __bf16 bf16x2 __attribute__((ext_vector_type(2)));
typedef float f32x16 __attribute__((ext_vector_type(16)));
typedef float f32x4 __attribute__((ext_vector_type(4)));
typedef float f32x2 __attribute__((ext_vector_type(2)));
typedef unsigned u32x4 __attribute__((ext_vector_type(4)));
typedef unsigned u32x2 __attribute__((ext_vector_type(2)));
typedef __attribute__((address_space(3))) char lds_char;
typedef __attribute__((address_space(3))) bf16x4 lds_bf16x4;
typedef __attribute__((address_space(3))) bf16x8 lds_bf16x8;
typedef __attribute__((address_space(3))) f32x4 lds_f32x4;
typedef __attribute__((address_space(3))) f32x2 lds_f32x2;
typedef __attribute__((address_space(1))) unsigned long long gu64;

#define DI __device__ __forceinline__
#define MFMA32(a, b, c) __builtin_amdgcn_mfma_f32_32x32x16_bf16((a), (b), (c), 0, 0, 0)
#define MFMA16(a, b, c) __builtin_amdgcn_mfma_f32_16x16x32_bf16((a), (b), (c), 0, 0, 0)
#define SB() __builtin_amdgcn_sched_barrier(0)
template <int V> using IC = std::integral_constant<int, V>;
#define SG_MFMA(n) __builtin_amdgcn_sched_group_barrier(0x008, (n), 0)
#define SG_DSRD(n) __builtin_amdgcn_sched_group_barrier(0x100, (n), 0)

#define DIAG_LANE0() ({ unsigned _l; asm volatile("v_mbcnt_lo_u32_b32 %0, -1, 0\n\tv_mbcnt_hi_u32_b32 %0, -1, %0" : "=v"(_l)); _l == 0; })
#define STAMPW(wv, i) do { if (w == (wv)) { const unsigned long long _t = __builtin_amdgcn_s_memrealtime(); if (DIAG_LANE0()) diag[blockIdx.x * 32 + (i)] = _t; } } while (0)
#define STAMP(i) STAMPW(0, i)
#define WREC(i, v) do { if (w == 0) { const unsigned long long _t = (v); if (DIAG_LANE0()) diag[8192 + (blockIdx.x * 12) * 64 + (i)] = _t; } } while (0)
#define TNOW() __builtin_amdgcn_s_memtime()

constexpr int NB = 8, SQ = 192, DM = 576, NH = 32, SK = 512, DH = 128;
constexpr float QSCALE = 0.08838834764831845f * 1.4426950408889634f;
constexpr float THR = 8.0f;

constexpr unsigned CODE_BYTES = 26 * 1024;
constexpr size_t WS_O = 0;
constexpr size_t WS_PART = 16u << 20;
constexpr size_t WS_FLAG = 32u << 20;
constexpr unsigned PART_BLK = 24 * 144 * 2;
constexpr unsigned O_BYTES = NB * NH * 6 * 8 * 1024, PART_BYTES = 8 * 8 * 4 * 4 * PART_BLK;

constexpr unsigned QS_BYTES = 40960, QS_W = 24576;
constexpr unsigned SPARE = 155648, LDS_TOTAL = 163840, ML_OFF = 98304, VOTE_OFF = 101376;
constexpr float L_MIN = 9.313225746154785e-10f, L_MAX = 1048576.0f;
DI constexpr unsigned kv_slot(int s) { return (unsigned)((s + 3) & 3) * QS_BYTES; }
DI unsigned qblk(unsigned blk) { return blk < 40 ? 2 * QS_BYTES + blk * 1024 : SPARE + (blk - 40) * 1024; }
constexpr unsigned P2_OSLOT = 12288, P2_WSLOT = 18432, P2_WBASE = 53248, IMG_STRIDE = 592, IMG_BYTES = 96 * IMG_STRIDE;
DI unsigned p2_oslot(unsigned i) { return (i & 3) * P2_OSLOT; }
DI unsigned p2_wslot(unsigned i) { return P2_WBASE + ((i + 3) % 6) * P2_WSLOT; }

DI unsigned rfl(unsigned x) { return __builtin_amdgcn_readfirstlane(x); }
DI unsigned cvtpk(float lo, float hi) { f32x2 v = {lo, hi}; bf16x2 b = __builtin_convertvector(v, bf16x2); return __builtin_bit_cast(unsigned, b); }
DI bf16x8 pack8(const f32x16& x, int s) {
  u32x4 p = {cvtpk(x[8 * s], x[8 * s + 1]), cvtpk(x[8 * s + 2], x[8 * s + 3]), cvtpk(x[8 * s + 4], x[8 * s + 5]), cvtpk(x[8 * s + 6], x[8 * s + 7])};
  return __builtin_bit_cast(bf16x8, p);
}
DI bf16x8 lds_rd128(lds_char* base, unsigned off) { return *(lds_bf16x8*)(base + off); }
DI bf16x8 lds_tr2(lds_char* base, unsigned off0, unsigned off1) {
  bf16x4 lo = __builtin_amdgcn_ds_read_tr16_b64_v4bf16((lds_bf16x4*)(base + off0));
  bf16x4 hi = __builtin_amdgcn_ds_read_tr16_b64_v4bf16((lds_bf16x4*)(base + off1));
  return __builtin_shufflevector(lo, hi, 0, 1, 2, 3, 4, 5, 6, 7);
}
template <int POL> DI void dma16(__amdgpu_buffer_rsrc_t r, unsigned voff, unsigned soff, unsigned m0v) {
  if constexpr (POL == 0) asm volatile("s_mov_b32 m0, %3\n\ts_nop 0\n\tbuffer_load_dwordx4 %0, %1, %2 offen lds" ::"v"(voff), "s"(r), "s"(soff), "s"(m0v) : "memory");
  else if constexpr (POL == 1) asm volatile("s_mov_b32 m0, %3\n\ts_nop 0\n\tbuffer_load_dwordx4 %0, %1, %2 offen nt lds" ::"v"(voff), "s"(r), "s"(soff), "s"(m0v) : "memory");
  else asm volatile("s_mov_b32 m0, %3\n\ts_nop 0\n\tbuffer_load_dwordx4 %0, %1, %2 offen sc1 lds" ::"v"(voff), "s"(r), "s"(soff), "s"(m0v) : "memory");
}
template <int N> DI void wait_vm() { asm volatile("s_waitcnt vmcnt(%0)" ::"i"(N) : "memory"); }
DI void bar() { SB(); asm volatile("s_waitcnt lgkmcnt(0)\n\ts_barrier" ::: "memory"); SB(); }
DI float pl32_max(float v) {
  auto r = __builtin_amdgcn_permlane32_swap(__float_as_uint(v), __float_as_uint(v), false, false);
  return fmaxf(__uint_as_float(r[0]), __uint_as_float(r[1]));
}
DI float pl32_sum(float v) {
  auto r = __builtin_amdgcn_permlane32_swap(__float_as_uint(v), __float_as_uint(v), false, false);
  return __uint_as_float(r[0]) + __uint_as_float(r[1]);
}
DI unsigned lane_id() { unsigned l; asm volatile("v_mbcnt_lo_u32_b32 %0, -1, 0\n\tv_mbcnt_hi_u32_b32 %0, -1, %0" : "=v"(l)); return l; }
DI void poll_flags(gu64* flags, unsigned idx0, unsigned stride, unsigned n, unsigned skip, unsigned long long tag, unsigned lane) {
  const bool mine = lane < n && lane != skip;
  gu64* p = flags + (size_t)(idx0 + (mine ? lane : 0) * stride) * 16;
  for (unsigned spins = 0; spins < 400000u; ++spins) {
    const unsigned long long v = __hip_atomic_load(p, __ATOMIC_RELAXED, __HIP_MEMORY_SCOPE_AGENT);
    if (__all(!mine || v == tag)) break;
    __builtin_amdgcn_s_sleep(1);
  }
}

#define OUT_AUX 2
#define ROTMODE 2
#define POL_X 0
#define POL_WQ 0
#define O_ST_AUX 16
#define POL_O 2
#define POL_WO 0

__global__ __launch_bounds__(768, 3) void mha_kernel(const __hip_bfloat16* __restrict__ x, const __hip_bfloat16* __restrict__ kc,
                                                     const __hip_bfloat16* __restrict__ vc, const __hip_bfloat16* __restrict__ wq,
                                                     const __hip_bfloat16* __restrict__ wo, __hip_bfloat16* __restrict__ out,
                                                     char* __restrict__ ws, unsigned long long* diag) {
  __shared__ __attribute__((aligned(1024))) char smem[LDS_TOTAL];
  lds_char* lds = (lds_char*)smem;
  const unsigned ldsb = (unsigned)(size_t)smem;
  const unsigned tid = threadIdx.x, w = rfl(tid >> 6);
  STAMP(0);
  const unsigned xcd = blockIdx.x & 7, jj = blockIdx.x >> 3;
  const unsigned b = 4 * (xcd & 1) + (jj & 3), hd = 8 * (xcd >> 1) + (jj >> 2);
  gu64* flags = (gu64*)(ws + WS_FLAG);
  unsigned long long epoch;
  auto read_epoch = [&]() {
    asm volatile("s_load_dwordx2 %0, %1, 0x0\n\ts_waitcnt lgkmcnt(0)" : "=s"(epoch) : "s"((const char*)(ws + WS_FLAG) + (512 + b) * 128) : "memory");
  };

  const __amdgpu_buffer_rsrc_t rwo = __builtin_amdgcn_make_buffer_rsrc((void*)wo, (short)0, NH * DH * DM * 2, 0x00020000);
  bf16x8 qf[8];
  f32x16 oacc[4];
  float m, l;
  {
    const unsigned lane = tid & 63, r = lane & 31, h = lane >> 5;
    const unsigned t = w >> 1, j = w & 1;
    const unsigned g1 = (lane >> 4) & 1, qq = (lane & 15) >> 2, pp = lane & 3, sp = ((pp & 1) << 1) | (pp >> 1);
    const __amdgpu_buffer_rsrc_t rk = __builtin_amdgcn_make_buffer_rsrc((void*)kc, (short)0, NB * NH * SK * DH * 2, 0x00020000);
    const __amdgpu_buffer_rsrc_t rv = __builtin_amdgcn_make_buffer_rsrc((void*)vc, (short)0, NB * NH * SK * DH * 2, 0x00020000);
    const unsigned skv = (b * NH + hd) * (SK * DH * 2);
    const unsigned vk = (lane >> 4) * 256 + ((lane & 15) ^ (lane >> 4)) * 16;
    const unsigned vv = (lane >> 4) * 256 + ((lane & 15) ^ ((lane >> 4) << 2)) * 16;

    auto kv4 = [&](int s) {
      const unsigned lw = w - 4;
      const unsigned dst = ldsb + kv_slot(s) + lw * 4096u;
      const unsigned so = skv + ((lw >> 1) & 1) * 65536u + (lw & 1) * 4096u + (unsigned)s * 8192u;
#pragma unroll
      for (int p = 0; p < 4; ++p) {
        if (lw < 4) dma16<1>(rk, vk ^ (p << 6), so + p * 1024u, dst + p * 1024u);
        else dma16<1>(rv, vv, so + p * 1024u, dst + p * 1024u);
      }
    };
    const unsigned koff = 3 * (((jj >> 2) + (jj & 3)) % 3);
    if (w >= 4) {
      const unsigned lw = w - 4;
      const __amdgpu_buffer_rsrc_t rx = __builtin_amdgcn_make_buffer_rsrc((void*)x, (short)0, NB * SQ * DM * 2, 0x00020000);
      const __amdgpu_buffer_rsrc_t rw = __builtin_amdgcn_make_buffer_rsrc((void*)wq, (short)0, DM * NH * DH * 2, 0x00020000);
      const unsigned vx = (lane >> 3) * 1152 + (((lane & 7) ^ (lane >> 4) ^ ((lw & 1) << 2)) * 16);
      const unsigned sx = (b * SQ + 8 * lw) * 1152u;
      const unsigned vw = (lane >> 4) * 8192 + (((lane & 15) ^ ((lane >> 4) << 2)) * 16);
      const unsigned sw = 4 * lw * 8192u + hd * 256u;
      auto issue_q = [&](int i) {
        unsigned st = koff + i; st = st >= 9 ? st - 9 : st;
        const unsigned slot = ldsb + (unsigned)(i % 3) * QS_BYTES;
#pragma unroll
        for (int p = 0; p < 3; ++p) dma16<POL_X>(rx, vx, sx + (unsigned)(64 * p) * 1152u + st * 128u, slot + (lw + 8 * p) * 1024u);
#pragma unroll
        for (int p = 0; p < 2; ++p) dma16<POL_WQ>(rw, vw, sw + (st * 64 + 32 * p) * 8192u, slot + QS_W + (lw + 8 * p) * 1024u);
      };
      issue_q(0); issue_q(1);
      wait_vm<5>(); bar(); issue_q(2);
      wait_vm<5>(); bar(); issue_q(3);
      wait_vm<5>(); bar(); issue_q(4); kv4(0);
      wait_vm<9>(); bar(); issue_q(5);
      wait_vm<9>(); bar(); issue_q(6);
      wait_vm<5>(); bar(); issue_q(7);
      wait_vm<5>(); bar(); issue_q(8);
      wait_vm<5>(); bar(); kv4(1);
      wait_vm<4>(); bar(); kv4(2);
      read_epoch();
      bar();
      bar();
    } else {
      read_epoch();
      if (w < 2) {
        unsigned long long pc;
        asm volatile("s_getpc_b64 %0" : "=s"(pc));
        const unsigned off = (jj * 2 + w) * 1024u;
        if (off + 1024u <= CODE_BYTES) {
          u32x4 t;
          const char* p = (const char*)(pc & ~127ull) + off + lane * 16;
          asm volatile("global_load_dwordx4 %0, %1, off\n\ts_waitcnt vmcnt(0)" : "=v"(t) : "v"(p) : "memory");
        }
      }
      if (w == 2) {
        const unsigned hg_ = hd >> 3, hl_ = hd & 7, wg_ = b * NH + hd;
        const char* p = (const char*)ws + WS_FLAG;
        if (lane == 0) p = (const char*)kc + (size_t)wg_ * (SK * DH * 2);
        if (lane == 1) p = (const char*)vc + (size_t)wg_ * (SK * DH * 2);
        if (lane == 2) p = (const char*)ws + WS_O + (size_t)wg_ * 49152;
        if (lane == 3) p = (const char*)ws + WS_O + (size_t)(b * NH + 8 * hg_) * 49152;
        if (lane == 4) p = (const char*)ws + WS_O + (size_t)(b * NH + 8 * hg_ + 8) * 49152 - 64;
        if (lane >= 5 && lane < 9) p = (const char*)ws + WS_PART + (size_t)(((b * 8 + hl_) * 4 + (lane - 5)) * 4) * PART_BLK;
        if (lane == 9) p = (const char*)wo + (size_t)hg_ * (1024 * 1152);
        if (lane == 10) p = (const char*)wo + (size_t)(hg_ + 1) * (1024 * 1152) - 64;
        if (lane == 11) p = (const char*)out + (size_t)(b * SQ) * (DM * 2);
        if (lane == 12) p = (const char*)kc + (size_t)(wg_ + 1) * (SK * DH * 2) - 64;
        if (lane == 13) p = (const char*)vc + (size_t)(wg_ + 1) * (SK * DH * 2) - 64;
        unsigned t;
        asm volatile("global_load_dword %0, %1, off\n\ts_waitcnt vmcnt(0)" : "=v"(t) : "v"(p) : "memory");
      }
      const unsigned qh = w >> 1, dh = w & 1;
      const unsigned xa0 = (96 * qh + r) * 128 + ((h ^ ((r >> 1) & 7)) << 4);
      const unsigned wa0 = QS_W + (8 * h + qq) * 256 + ((4 * ((2 * dh) ^ qq) + 2 * g1 + (sp >> 1)) << 4) + (sp & 1) * 8;
      f32x16 qacc[3][2];
#pragma unroll
      for (int a = 0; a < 3; ++a)
#pragma unroll
        for (int i = 0; i < 16; ++i) { qacc[a][0][i] = 0.f; qacc[a][1][i] = 0.f; }
      bf16x8 bA[3], aA[2], bB[3], aB[2];
      unsigned xb, wb;
      auto rd = [&](bf16x8 (&bb)[3], bf16x8 (&aa)[2], int s) {
        const unsigned xs = xb ^ (s << 5), wb1 = wb ^ 64;
        aa[0] = lds_tr2(lds, wb + s * 4096, wb + s * 4096 + 1024);
        bb[0] = lds_rd128(lds, xs); bb[1] = lds_rd128(lds, xs + 4096); bb[2] = lds_rd128(lds, xs + 8192);
        aa[1] = lds_tr2(lds, wb1 + s * 4096, wb1 + s * 4096 + 1024);
      };
      auto mm = [&](bf16x8 (&bb)[3], bf16x8 (&aa)[2]) {
        qacc[0][0] = MFMA32(aa[0], bb[0], qacc[0][0]);
        qacc[1][0] = MFMA32(aa[0], bb[1], qacc[1][0]);
        qacc[2][0] = MFMA32(aa[0], bb[2], qacc[2][0]);
        qacc[0][1] = MFMA32(aa[1], bb[0], qacc[0][1]);
        qacc[1][1] = MFMA32(aa[1], bb[1], qacc[1][1]);
        qacc[2][1] = MFMA32(aa[1], bb[2], qacc[2][1]);
      };
      bar();
      STAMP(1);
      unsigned long long qc0 = TNOW(), qr0 = __builtin_amdgcn_s_memrealtime();
      xb = xa0; wb = wa0;
      asm volatile("" : "+v"(xb), "+v"(wb));
      rd(bA, aA, 0);
      auto mix = [&]() { SG_MFMA(1); SG_DSRD(2); SG_MFMA(1); SG_DSRD(2); SG_MFMA(1); SG_DSRD(1); SG_MFMA(1); SG_DSRD(1); SG_MFMA(1); SG_DSRD(1); SG_MFMA(1); SB(); };
#pragma unroll 1
      for (int st = 0; st < 8; ++st) {
        SB(); rd(bB, aB, 1); mm(bA, aA); mix();
        rd(bA, aA, 2); mm(bB, aB); mix();
        rd(bB, aB, 3); mm(bA, aA); mix();
        bar();
        const unsigned so = (unsigned)((st + 1) % 3) * QS_BYTES;
        xb = xa0 + so; wb = wa0 + so;
        asm volatile("" : "+v"(xb), "+v"(wb));
        rd(bA, aA, 0); mm(bB, aB); mix();
      }
      SB(); rd(bB, aB, 1); mm(bA, aA); mix();
      rd(bA, aA, 2); mm(bB, aB); mix();
      rd(bB, aB, 3); mm(bA, aA); mix();
      mm(bB, aB);
      WREC(32, TNOW() - qc0); WREC(33, __builtin_amdgcn_s_memrealtime() - qr0);
      bar();
      STAMP(4);
#pragma unroll
      for (int a = 0; a < 3; ++a)
#pragma unroll
        for (int d = 0; d < 2; ++d) {
#pragma unroll
          for (int i = 0; i < 16; ++i) qacc[a][d][i] *= QSCALE;
#pragma unroll
          for (int s2 = 0; s2 < 2; ++s2) {
            const unsigned blk = (3 * qh + a) * 8 + 4 * dh + 2 * d + s2;
            *(lds_bf16x8*)(lds + qblk(blk) + lane * 16) = pack8(qacc[a][d], s2);
          }
        }
      bar();
    }
#pragma unroll
    for (int i = 0; i < 8; ++i) qf[i] = lds_rd128(lds, qblk(t * 8 + i) + lane * 16);

    const unsigned grp = w >> 2;
    float l0, l1;
    bool safe = false;
    const unsigned ka0 = j * 8192 + r * 256 + ((h ^ (r & 15)) << 4);
    const unsigned va0 = (16384 + j * 8192 + (4 * h + qq) * 256 + ((4 * qq + 2 * g1 + (sp >> 1)) << 4) + (sp & 1) * 8) ^ (j << 7);
    const unsigned vkx = vk ^ ((w & 3) << 6);
    const unsigned sp0 = skv + ((w >> 3) & 1) * 65536u + (w & 7) * 1024u;
    const unsigned sp1 = skv + (((w + 12) >> 3) & 1) * 65536u + ((w + 12) & 7) * 1024u;
    const unsigned sp2 = skv + (((w + 24) >> 3) & 1) * 65536u + ((w + 24) & 7) * 1024u;

    bf16x8 kf[4];
    auto step = [&](auto G_, int s) {
      constexpr int G = decltype(G_)::value;
      constexpr int NP = G == 2 ? 2 : 3;
      unsigned vb = va0 + kv_slot(s), kc0 = ka0 + kv_slot(s), kb = ka0 + kv_slot(s + 1);
      asm volatile("" : "+v"(kb), "+v"(vb), "+v"(kc0));
      __builtin_amdgcn_s_setprio(1);
      f32x16 sc;
      bf16x8 vf[4][2];
      {
        bf16x8 kg[4];
#pragma unroll
        for (int ks = 0; ks < 4; ++ks) kg[ks] = lds_rd128(lds, kc0 ^ ((ks + 4) << 5));
#pragma unroll
        for (int i = 0; i < 16; ++i) sc[i] = 0.f;
#pragma unroll
        for (int ks = 0; ks < 4; ++ks) sc = MFMA32(kf[ks], qf[ks], sc);
#pragma unroll
        for (int d = 0; d < 4; ++d) { const unsigned va = vb ^ (d << 6); vf[d][0] = lds_tr2(lds, va, va + 2048); }
#pragma unroll
        for (int ks = 0; ks < 4; ++ks) sc = MFMA32(kg[ks], qf[ks + 4], sc);
        SG_MFMA(1); SG_DSRD(2); SG_MFMA(1); SG_DSRD(2); SG_MFMA(1); SG_DSRD(2); SG_MFMA(1); SG_DSRD(2);
        SG_MFMA(1); SG_DSRD(2); SG_MFMA(1); SG_DSRD(2); SG_MFMA(2);
        SB();
        asm volatile("" : "+v"(sc));
      }
      __builtin_amdgcn_s_setprio(0);
      if constexpr (G == 1) { if (s <= 5) wait_vm<NP>(); else if (s == 6) wait_vm<0>(); }
      bar();
#pragma unroll
      for (int d = 0; d < 4; ++d) { const unsigned va = vb ^ (d << 6); vf[d][1] = lds_tr2(lds, va + 4096, va + 4096 + 2048); }
      SB();
      if (safe) {
        const float t0 = fmaxf(fmaxf(sc[0], sc[1]), sc[2]), t1 = fmaxf(fmaxf(sc[3], sc[4]), sc[5]), t2 = fmaxf(fmaxf(sc[6], sc[7]), sc[8]);
        const float t3 = fmaxf(fmaxf(sc[9], sc[10]), sc[11]), t4 = fmaxf(fmaxf(sc[12], sc[13]), sc[14]);
        float tm = fmaxf(fmaxf(fmaxf(t0, t1), t2), fmaxf(fmaxf(t3, t4), sc[15]));
        tm = pl32_max(tm);
        if (__any(tm > m + THR)) {
          const float mn = fmaxf(m, tm);
          const float al = __builtin_amdgcn_exp2f(m - mn);
          l0 *= al; l1 *= al; m = mn;
#pragma unroll
          for (int d = 0; d < 4; ++d)
#pragma unroll
            for (int i = 0; i < 16; ++i) oacc[d][i] *= al;
        }
#pragma unroll
        for (int i = 0; i < 16; ++i) sc[i] -= m;
      }
      SB();
#pragma unroll
      for (int i = 0; i < 16; ++i) sc[i] = __builtin_amdgcn_exp2f(sc[i]);
      bf16x8 p0 = pack8(sc, 0), p1 = pack8(sc, 1);
#pragma unroll
      for (int i = 0; i < 16; i += 2) { l0 += sc[i]; l1 += sc[i + 1]; }
      asm volatile("" : "+v"(p0), "+v"(p1), "+v"(l0), "+v"(l1));
      if constexpr (G == 0) { if (s <= 5) wait_vm<NP>(); else if (s == 6) wait_vm<0>(); }
      bar();
      __builtin_amdgcn_s_setprio(1);
#pragma unroll
      for (int d = 0; d < 4; ++d) oacc[d] = MFMA32(vf[d][0], p0, oacc[d]);
#pragma unroll
      for (int ks = 0; ks < 4; ++ks) kf[ks] = lds_rd128(lds, kb ^ (ks << 5));
#pragma unroll
      for (int d = 0; d < 4; ++d) oacc[d] = MFMA32(vf[d][1], p1, oacc[d]);
      SG_MFMA(2); SG_DSRD(1); SG_MFMA(1); SG_DSRD(1); SG_MFMA(1); SG_DSRD(1); SG_MFMA(1); SG_DSRD(1); SG_MFMA(3);
      SB();
      asm volatile("" : "+v"(oacc[0]), "+v"(oacc[1]), "+v"(oacc[2]), "+v"(oacc[3]));
      __builtin_amdgcn_s_setprio(0);
      SB();
      if (s <= 4) {
        const unsigned dst = ldsb + kv_slot(s + 3) + w * 1024u, sofs = (unsigned)(s + 3) * 8192u;
        dma16<1>(rk, vkx, sp0 + sofs, dst);
        if constexpr (G == 0) dma16<1>(rk, vkx, sp1 + sofs, dst + 12288u); else dma16<1>(rv, vv, sp1 + sofs, dst + 12288u);
        if constexpr (G != 2) dma16<1>(rv, vv, sp2 + sofs, dst + 24576u);
      }
      if constexpr (G == 2) { if (s <= 4) wait_vm<NP>(); else if (s == 5) wait_vm<0>(); }
      if (s >= 6 && !safe) {
        const unsigned pc = ((b & 3) * 8 + (hd & 7)) * 36 + w * 3 + (s - 6) * 2;
        const unsigned so = (hd >> 3) * (1024u * 1152u) + pc * 1024u;
        dma16<POL_WO>(rwo, lane * 16, so, ldsb + SPARE + (w & 7) * 1024u);
        if (s == 6) dma16<POL_WO>(rwo, lane * 16, so + 1024u, ldsb + SPARE + (w & 7) * 1024u);
      }
      bar();
    };
    auto run = [&](auto G_) {
      constexpr int G = decltype(G_)::value;
      {
        unsigned kb0 = ka0 + kv_slot(0);
        asm volatile("" : "+v"(kb0));
#pragma unroll
        for (int ks = 0; ks < 4; ++ks) kf[ks] = lds_rd128(lds, kb0 ^ (ks << 5));
      }
      if constexpr (G >= 1) bar();
      if constexpr (G == 2) { wait_vm<4>(); bar(); }
#pragma unroll 1
      for (int s = 0; s < 8; ++s) step(G_, s);
      if constexpr (G == 0) bar();
      if constexpr (G <= 1) bar();
    };
    unsigned long long ca0 = 0, ra0 = 0;
#pragma unroll 1
    for (;;) {
#pragma unroll
      for (int d = 0; d < 4; ++d)
#pragma unroll
        for (int i = 0; i < 16; ++i) oacc[d][i] = 0.f;
      m = safe ? -__builtin_inff() : 0.f; l0 = 0.f; l1 = 0.f;
      bar(); STAMP(5);
      ca0 = TNOW(); ra0 = __builtin_amdgcn_s_memrealtime();
      if (grp == 0) run(IC<0>{});
      else if (grp == 1) run(IC<1>{});
      else run(IC<2>{});
      WREC(30, TNOW() - ca0); WREC(31, __builtin_amdgcn_s_memrealtime() - ra0);
      STAMP(13);
      const unsigned ln = lane_id();
      l = pl32_sum(l0 + l1);
      const unsigned xw = w * 4096 + ln * 16;
#pragma unroll
      for (int i = 0; i < 4; ++i) *(lds_bf16x8*)(lds + xw + i * 1024) = pack8(oacc[2 + (i >> 1)], i & 1);
      if (ln < 32) { f32x2 ml = {m, l}; *(lds_f32x2*)(lds + ML_OFF + w * 256 + ln * 8) = ml; }
      const bool bad = __any(!(l > L_MIN && l < L_MAX));
      if (ln == 0) *(__attribute__((address_space(3))) unsigned*)(lds + VOTE_OFF + w * 4) = bad ? 1u : 0u;
      bar();
      STAMP(6);
      if (safe) break;
      const unsigned vote = *(__attribute__((address_space(3))) unsigned*)(lds + VOTE_OFF + (ln < 12 ? ln : 0) * 4);
      if (!__any(vote != 0)) break;
      safe = true;
      bar();
      if (w >= 4) {
        const unsigned lw = rfl(w - 4), sk = rfl((b * NH + hd) * (SK * DH * 2));
        const unsigned vo = (ln >> 4) * 256 + (lw < 4 ? ((ln & 15) ^ (ln >> 4)) : ((ln & 15) ^ ((ln >> 4) << 2))) * 16;
        const __amdgpu_buffer_rsrc_t rr = __builtin_amdgcn_make_buffer_rsrc((void*)(lw < 4 ? kc : vc), (short)0, NB * NH * SK * DH * 2, 0x00020000);
#pragma unroll 1
        for (unsigned q = 0; q < 12; ++q) {
          const unsigned st = q >> 2, p = q & 3;
          dma16<1>(rr, lw < 4 ? vo ^ (p << 6) : vo, rfl(sk + ((lw >> 1) & 1) * 65536u + (lw & 1) * 4096u + st * 8192u + p * 1024u), rfl(ldsb + kv_slot(st) + lw * 4096u + p * 1024u));
        }
        wait_vm<0>();
      }
    }
  }

  const unsigned lane = lane_id();
  const unsigned hg = hd >> 3, hl = hd & 7, rh = hl & 1, cq = hl >> 1;
  const unsigned long long tag = epoch + 1;
  const unsigned wg = b * NH + hd;
  auto wo_voff = [&](int i) {
    const unsigned n = 64 * i + lane, p = n / 18, ch = n - 18 * p;
    const unsigned k = (p & 19) | ((p & 4) << 1) | ((p & 8) >> 1);
    return k * 1152 + ch * 16;
  };
  auto wo_issue = [&](const unsigned (&vo)[9], unsigned i) {
    const unsigned kh = w & 1, hh = (hl + (i >> 1)) & 7;
    const unsigned so = ((8 * hg + hh) * 128 + (2 * (i & 1) + kh) * 32) * 1152u + cq * 288u;
    const unsigned dst = ldsb + p2_wslot(i) + kh * 9216u;
#pragma unroll
    for (int p = 0; p < 9; ++p) dma16<POL_WO>(rwo, vo[p], so, dst + p * 1024u);
  };
  unsigned wvo[9];
  if (w == 8 || w == 9) {
#pragma unroll
    for (int p = 0; p < 9; ++p) wvo[p] = wo_voff(p);
  }

  {
    const unsigned r = lane & 31, h = lane >> 5, t = w >> 1, j = w & 1;
    f32x2 o = *(lds_f32x2*)(lds + ML_OFF + (w ^ 1) * 256 + r * 8);
    const float mn = fmaxf(m, o[0]);
    const float f = __builtin_amdgcn_exp2f(m - mn), fo = __builtin_amdgcn_exp2f(o[0] - mn);
    const float inv = 1.0f / (l * f + o[1] * fo);
    const float sa = f * inv, sb = fo * inv;
    const unsigned xr = (w ^ 1) * 4096 + lane * 16;
#pragma unroll
    for (int i = 0; i < 4; ++i) {
      const u32x4 v = __builtin_bit_cast(u32x4, lds_rd128(lds, xr + i * 1024));
#pragma unroll
      for (int e = 0; e < 4; ++e) {
        oacc[i >> 1][8 * (i & 1) + 2 * e] = oacc[i >> 1][8 * (i & 1) + 2 * e] * sa + __uint_as_float(v[e] << 16) * sb;
        oacc[i >> 1][8 * (i & 1) + 2 * e + 1] = oacc[i >> 1][8 * (i & 1) + 2 * e + 1] * sa + __uint_as_float(v[e] & 0xffff0000u) * sb;
      }
    }
    const __amdgpu_buffer_rsrc_t ro = __builtin_amdgcn_make_buffer_rsrc((void*)(ws + WS_O), (short)0, O_BYTES, 0x00020000);
    const unsigned so = ((wg * 6 + t) * 8 + 4 * j) * 1024u;
#pragma unroll
    for (int i = 0; i < 4; ++i)
      __builtin_amdgcn_raw_buffer_store_b128(__builtin_bit_cast(u32x4, pack8(oacc[i >> 1], i & 1)), ro, so + lane * 16 + i * 1024, 0, O_ST_AUX);
    STAMP(7);
    if (w == 8 || w == 9) { wo_issue(wvo, 0); wo_issue(wvo, 1); wait_vm<18>(); } else wait_vm<0>();
    STAMP(8);
    bar();
    if (tid == 0) __hip_atomic_store(flags + (size_t)wg * 16, tag, __ATOMIC_RELAXED, __HIP_MEMORY_SCOPE_AGENT);
  }
  STAMP(14);

  f32x4 acc[3][5];
#pragma unroll
  for (int a = 0; a < 3; ++a)
#pragma unroll
    for (int n = 0; n < 5; ++n) acc[a][n] = f32x4{0.f, 0.f, 0.f, 0.f};
  const unsigned nh = (w >> 2) & 1, kh = w & 1, qh = (w >> 1) & 1;
  if (w >= 10) {
    const __amdgpu_buffer_rsrc_t ro = __builtin_amdgcn_make_buffer_rsrc((void*)(ws + WS_O), (short)0, O_BYTES, 0x00020000);
    auto o_issue = [&](unsigned i) {
      const unsigned hh = (hl + (i >> 1)) & 7;
      const unsigned so = (((b * NH + 8 * hg + hh) * 6 + 3 * rh) * 8 + 2 * (2 * (i & 1) + kh)) * 1024u;
      const unsigned dst = ldsb + p2_oslot(i) + kh * 6144u;
#pragma unroll
      for (int q = 0; q < 3; ++q) { dma16<POL_O>(ro, lane * 16, so + q * 8192u, dst + q * 2048u); dma16<POL_O>(ro, lane * 16, so + q * 8192u + 1024u, dst + q * 2048u + 1024u); }
    };
    gu64* fp = flags + (size_t)(b * NH + 8 * hg + (lane & 7)) * 16;
    unsigned long long pv;
    auto poll_issue = [&]() { asm volatile("global_load_dwordx2 %0, %1, off sc1" : "=v"(pv) : "v"(fp) : "memory"); };
    unsigned rdy = 1u << hl;
    auto poll_take = [&]() {
      asm volatile("s_waitcnt vmcnt(0)" : "+v"(pv) :: "memory");
      rdy |= (unsigned)__ballot(pv == tag) & 0xffu;
    };
    o_issue(0); o_issue(1);
    poll_issue();
    STAMPW(10, 22);
    bool pending = true;
    unsigned spins = 0;
#pragma unroll 1
    for (unsigned i = 0; i < 16; ++i) {
      if (pending) wait_vm<7>(); else if (i <= 14) wait_vm<6>(); else wait_vm<0>();
      if (i == 0) STAMPW(10, 23);
      bar();
      if (i + 2 < 16) {
        const unsigned hh = (hl + ((i + 2) >> 1)) & 7;
        if (pending) { poll_take(); pending = false; }
        while (!((rdy >> hh) & 1) && spins < 400000u) { __builtin_amdgcn_s_sleep(1); poll_issue(); poll_take(); ++spins; }
        o_issue(i + 2);
      }
    }
  } else if (w >= 8) {
    wo_issue(wvo, 2); wo_issue(wvo, 3); wo_issue(wvo, 4);
#pragma unroll 1
    for (unsigned i = 0; i < 16; ++i) {
      if (i <= 11) wait_vm<36>(); else if (i == 12) wait_vm<27>(); else if (i == 13) wait_vm<18>(); else if (i == 14) wait_vm<9>(); else wait_vm<0>();
      bar();
      if (i + 5 < 16) wo_issue(wvo, i + 5);
    }
  } else {
    const unsigned c = lane & 15, g = lane >> 4;
    unsigned ba[3];
#pragma unroll
    for (int a = 0; a < 3; ++a) {
      const unsigned qi = 3 * qh + a;
      ba[a] = kh * 6144 + ((qi >> 1) * 2 + (g >> 1)) * 1024 + (16 * (qi & 1) + c + 32 * (g & 1)) * 16;
    }
    const unsigned aa = kh * 9216 + ((g >> 1) * 16 + (g & 1) * 4 + (c >> 2)) * 288 + 5 * nh * 32 + (c & 3) * 8;
    bf16x8 bX[3], aX[5], bY[3], aY[5];
    auto rd = [&](auto NT_, bf16x8 (&bb)[3], bf16x8 (&av)[5], unsigned i) {
      constexpr int NT = decltype(NT_)::value;
      unsigned ob = p2_oslot(i), wb = aa + p2_wslot(i);
      asm volatile("" : "+v"(wb));
#pragma unroll
      for (int a = 0; a < 3; ++a) bb[a] = lds_rd128(lds, ba[a] + ob);
#pragma unroll
      for (int n = 0; n < NT; ++n) av[n] = lds_tr2(lds, wb + n * 32, wb + n * 32 + 2304);
    };
    auto mm = [&](auto NT_, bf16x8 (&bb)[3], bf16x8 (&av)[5]) {
      constexpr int NT = decltype(NT_)::value;
#pragma unroll
      for (int n = 0; n < NT; ++n)
#pragma unroll
        for (int a = 0; a < 3; ++a) acc[a][n] = MFMA16(av[n], bb[a], acc[a][n]);
    };
    unsigned long long t0 = 0;
    auto mix = [&](auto NT_) {
      constexpr int NT = decltype(NT_)::value;
#pragma unroll
      for (int k = 0; k < 3 + 2 * NT - (NT == 4 ? 0 : 0); ++k) {
        __builtin_amdgcn_sched_group_barrier(0x008, 1, 0);
        __builtin_amdgcn_sched_group_barrier(0x100, 1, 0);
      }
      __builtin_amdgcn_sched_group_barrier(0x008, 3 * NT, 0);
    };
    auto gemm = [&](auto NT_) {
      bar();
      STAMP(15);
      t0 = TNOW();
      rd(NT_, bX, aX, 0);
#pragma unroll 1
      for (unsigned i = 1; i < 15; i += 2) {
        bar(); rd(NT_, bY, aY, i); mm(NT_, bX, aX); mix(NT_);
        bar(); rd(NT_, bX, aX, i + 1); mm(NT_, bY, aY); mix(NT_);
      }
      bar(); rd(NT_, bY, aY, 15); mm(NT_, bX, aX); mix(NT_);
      SB(); mm(NT_, bY, aY);
    };
    if (nh == 0) gemm(IC<5>{}); else gemm(IC<4>{});
    WREC(44, TNOW() - t0);
  }
  STAMP(16);
  bar();
  if (w < 8) {
    const unsigned c = lane & 15, g = lane >> 4;
    const unsigned base = kh * IMG_BYTES + (48 * qh + c) * IMG_STRIDE + (80 * nh + 4 * g) * 4;
#pragma unroll
    for (int a = 0; a < 3; ++a)
#pragma unroll
      for (int n = 0; n < 5; ++n)
        if (n < 4 || nh == 0) *(lds_f32x4*)(lds + base + a * 16 * IMG_STRIDE + n * 64) = acc[a][n];
  }
  bar();
  {
    const __amdgpu_buffer_rsrc_t rp = __builtin_amdgcn_make_buffer_rsrc((void*)(ws + WS_PART), (short)0, PART_BYTES, 0x00020000);
    const unsigned pbase = ((b * 8 + hl) * 4 + hg) * 4 * PART_BLK;
#pragma unroll 1
    for (unsigned n = tid; n < 72 * 18; n += 768) {
      const unsigned rr = n / 18, ch = n - rr * 18;
      unsigned dst = rr / 24; const unsigned row = rr - dst * 24;
      dst += dst >= hg ? 1 : 0;
      const unsigned a0 = (24 * dst + row) * IMG_STRIDE + ch * 32;
      const f32x4 v0 = *(lds_f32x4*)(lds + a0) + *(lds_f32x4*)(lds + IMG_BYTES + a0);
      const f32x4 v1 = *(lds_f32x4*)(lds + a0 + 16) + *(lds_f32x4*)(lds + IMG_BYTES + a0 + 16);
      const u32x4 pk = {cvtpk(v0[0], v0[1]), cvtpk(v0[2], v0[3]), cvtpk(v1[0], v1[1]), cvtpk(v1[2], v1[3])};
      __builtin_amdgcn_raw_buffer_store_b128(pk, rp, dst * PART_BLK + row * 288 + ch * 16 + pbase, 0, 16);
    }
    wait_vm<0>();
    bar();
    if (tid == 0) __hip_atomic_store(flags + (size_t)(256 + wg) * 16, tag, __ATOMIC_RELAXED, __HIP_MEMORY_SCOPE_AGENT);
    STAMP(17);
    if (w == 0) poll_flags(flags, 256 + b * NH + hl, 8, 4, hg, tag, lane);
    bar();
    STAMP(18);
    if (tid < 24 * 18) {
      const unsigned row = tid / 18, ch = tid - row * 18;
      u32x4 u[3];
#pragma unroll
      for (int k = 0; k < 3; ++k) {
        const unsigned src = k + (k >= (int)hg ? 1 : 0);
        u[k] = __builtin_amdgcn_raw_buffer_load_b128(rp, (((b * 8 + hl) * 4 + src) * 4 + hg) * PART_BLK + row * 288 + ch * 16, 0, 16);
      }
      const unsigned a0 = (24 * hg + row) * IMG_STRIDE + ch * 32;
      f32x4 v0 = *(lds_f32x4*)(lds + a0) + *(lds_f32x4*)(lds + IMG_BYTES + a0);
      f32x4 v1 = *(lds_f32x4*)(lds + a0 + 16) + *(lds_f32x4*)(lds + IMG_BYTES + a0 + 16);
#pragma unroll
      for (int k = 0; k < 3; ++k) {
        v0[0] += __uint_as_float(u[k][0] << 16); v0[1] += __uint_as_float(u[k][0] & 0xffff0000u);
        v0[2] += __uint_as_float(u[k][1] << 16); v0[3] += __uint_as_float(u[k][1] & 0xffff0000u);
        v1[0] += __uint_as_float(u[k][2] << 16); v1[1] += __uint_as_float(u[k][2] & 0xffff0000u);
        v1[2] += __uint_as_float(u[k][3] << 16); v1[3] += __uint_as_float(u[k][3] & 0xffff0000u);
      }
      const u32x4 o = {cvtpk(v0[0], v0[1]), cvtpk(v0[2], v0[3]), cvtpk(v1[0], v1[1]), cvtpk(v1[2], v1[3])};
      const __amdgpu_buffer_rsrc_t rout = __builtin_amdgcn_make_buffer_rsrc((void*)out, (short)0, NB * SQ * DM * 2, 0x00020000);
      __builtin_amdgcn_raw_buffer_store_b128(o, rout, ((b * SQ + 96 * rh + 24 * hg + row) * DM + 144 * cq + 8 * ch) * 2, 0, OUT_AUX);
    }
    if (tid == 0 && hd == 0) __hip_atomic_store(flags + (size_t)(512 + b) * 16, tag, __ATOMIC_RELAXED, __HIP_MEMORY_SCOPE_AGENT);
  }
  asm volatile("s_waitcnt vmcnt(0)" ::: "memory");
  STAMP(19);
}

__global__ void spin_kernel(const unsigned long long* diag) {
  __shared__ long long v[256];
  const unsigned t = threadIdx.x;
  unsigned long long mn = ~0ull;
  __shared__ unsigned long long s0[256];
  s0[t] = diag[t * 32 + 0];
  __syncthreads();
  for (int i = 0; i < 256; ++i) mn = s0[i] < mn ? s0[i] : mn;
  v[t] = (long long)(diag[t * 32 + ST_J] - mn);
  __syncthreads();
  if (t == 0) {
    long long med = 0;
    for (int i = 0; i < 256; ++i) { int c = 0; for (int k = 0; k < 256; ++k) c += v[k] < v[i] || (v[k] == v[i] && k < i); if (c == ST_RANK) med = v[i]; }
    const unsigned long long dur = (unsigned long long)med * 20;
    const unsigned long long t0 = __builtin_amdgcn_s_memrealtime();
    while (__builtin_amdgcn_s_memrealtime() - t0 < dur) __builtin_amdgcn_s_sleep(8);
  }
}

extern "C" void kernel_launch(void* const* d_in, const int* in_sizes, int n_in, void* d_out, int out_size, void* d_ws, size_t ws_size,
                              hipStream_t stream) {
  const __hip_bfloat16* x = (const __hip_bfloat16*)d_in[0];
  const __hip_bfloat16* kc = (const __hip_bfloat16*)d_in[1];
  const __hip_bfloat16* vc = (const __hip_bfloat16*)d_in[2];
  const __hip_bfloat16* wq = (const __hip_bfloat16*)d_in[3];
  const __hip_bfloat16* wo = (const __hip_bfloat16*)d_in[4];
  unsigned long long* diag = (unsigned long long*)((char*)d_ws + (64u << 20));
  mha_kernel<<<256, 768, 0, stream>>>(x, kc, vc, wq, wo, (__hip_bfloat16*)d_out, (char*)d_ws, diag);
  spin_kernel<<<1, 256, 0, stream>>>(diag);
}
```

```cpp
#define DIAG
#define ST_I -1
#define ST_J 1
#define ST_RANK 128
#include <hip/hip_runtime.h>
#include <hip/hip_bf16.h>
#include <type_traits>

typedef __bf16 bf16x8 __attribute__((ext_vector_type(8)));
typedef __bf16 bf16x4 __attribute__((ext_vector_type(4)));
typedef __bf16 bf16x2 __attribute__((ext_vector_type(2)));
typedef float f32x16 __attribute__((ext_vector_type(16)));
typedef float f32x4 __attribute__((ext_vector_type(4)));
typedef float f32x2 __attribute__((ext_vector_type(2)));
typedef unsigned u32x4 __attribute__((ext_vector_type(4)));
typedef unsigned u32x2 __attribute__((ext_vector_type(2)));
typedef __attribute__((address_space(3))) char lds_char;
typedef __attribute__((address_space(3))) bf16x4 lds_bf16x4;
typedef __attribute__((address_space(3))) bf16x8 lds_bf16x8;
typedef __attribute__((address_space(3))) f32x4 lds_f32x4;
typedef __attribute__((address_space(3))) f32x2 lds_f32x2;
typedef __attribute__((address_space(1))) unsigned long long gu64;

#define DI __device__ __forceinline__
#define MFMA32(a, b, c) __builtin_amdgcn_mfma_f32_32x32x16_bf16((a), (b), (c), 0, 0, 0)
#define MFMA16(a, b, c) __builtin_amdgcn_mfma_f32_16x16x32_bf16((a), (b), (c), 0, 0, 0)
#define SB() __builtin_amdgcn_sched_barrier(0)
template <int V> using IC = std::integral_constant<int, V>;
#define SG_MFMA(n) __builtin_amdgcn_sched_group_barrier(0x008, (n), 0)
#define SG_DSRD(n) __builtin_amdgcn_sched_group_barrier(0x100, (n), 0)

#define DIAG_LANE0() ({ unsigned _l; asm volatile("v_mbcnt_lo_u32_b32 %0, -1, 0\n\tv_mbcnt_hi_u32_b32 %0, -1, %0" : "=v"(_l)); _l == 0; })
#define STAMPW(wv, i) do { if (w == (wv)) { const unsigned long long _t = __builtin_amdgcn_s_memrealtime(); if (DIAG_LANE0()) diag[blockIdx.x * 32 + (i)] = _t; } } while (0)
#define STAMP(i) STAMPW(0, i)
#define WREC(i, v) do { if (w == 0) { const unsigned long long _t = (v); if (DIAG_LANE0()) diag[8192 + (blockIdx.x * 12) * 64 + (i)] = _t; } } while (0)
#define TNOW() __builtin_amdgcn_s_memtime()

constexpr int NB = 8, SQ = 192, DM = 576, NH = 32, SK = 512, DH = 128;
constexpr float QSCALE = 0.08838834764831845f * 1.4426950408889634f;
constexpr float THR = 8.0f;

constexpr unsigned CODE_BYTES = 26 * 1024;
constexpr size_t WS_O = 0;
constexpr size_t WS_PART = 16u << 20;
constexpr size_t WS_FLAG = 32u << 20;
constexpr unsigned PART_BLK = 24 * 144 * 2;
constexpr unsigned O_BYTES = NB * NH * 6 * 8 * 1024, PART_BYTES = 8 * 8 * 4 * 4 * PART_BLK;

constexpr unsigned QS_BYTES = 40960, QS_W = 24576;
constexpr unsigned SPARE = 155648, LDS_TOTAL = 163840, ML_OFF = 98304, VOTE_OFF = 101376;
constexpr float L_MIN = 9.313225746154785e-10f, L_MAX = 1048576.0f;
DI constexpr unsigned kv_slot(int s) { return (unsigned)((s + 3) & 3) * QS_BYTES; }
DI unsigned qblk(unsigned blk) { return blk < 40 ? 2 * QS_BYTES + blk * 1024 : SPARE + (blk - 40) * 1024; }
constexpr unsigned P2_OSLOT = 12288, P2_WSLOT = 18432, P2_WBASE = 53248, IMG_STRIDE = 592, IMG_BYTES = 96 * IMG_STRIDE;
DI unsigned p2_oslot(unsigned i) { return (i & 3) * P2_OSLOT; }
DI unsigned p2_wslot(unsigned i) { return P2_WBASE + ((i + 3) % 6) * P2_WSLOT; }

DI unsigned rfl(unsigned x) { return __builtin_amdgcn_readfirstlane(x); }
DI unsigned cvtpk(float lo, float hi) { f32x2 v = {lo, hi}; bf16x2 b = __builtin_convertvector(v, bf16x2); return __builtin_bit_cast(unsigned, b); }
DI bf16x8 pack8(const f32x16& x, int s) {
  u32x4 p = {cvtpk(x[8 * s], x[8 * s + 1]), cvtpk(x[8 * s + 2], x[8 * s + 3]), cvtpk(x[8 * s + 4], x[8 * s + 5]), cvtpk(x[8 * s + 6], x[8 * s + 7])};
  return __builtin_bit_cast(bf16x8, p);
}
DI bf16x8 lds_rd128(lds_char* base, unsigned off) { return *(lds_bf16x8*)(base + off); }
DI bf16x8 lds_tr2(lds_char* base, unsigned off0, unsigned off1) {
  bf16x4 lo = __builtin_amdgcn_ds_read_tr16_b64_v4bf16((lds_bf16x4*)(base + off0));
  bf16x4 hi = __builtin_amdgcn_ds_read_tr16_b64_v4bf16((lds_bf16x4*)(base + off1));
  return __builtin_shufflevector(lo, hi, 0, 1, 2, 3, 4, 5, 6, 7);
}
template <int POL> DI void dma16(__amdgpu_buffer_rsrc_t r, unsigned voff, unsigned soff, unsigned m0v) {
  if constexpr (POL == 0) asm volatile("s_mov_b32 m0, %3\n\ts_nop 0\n\tbuffer_load_dwordx4 %0, %1, %2 offen lds" ::"v"(voff), "s"(r), "s"(soff), "s"(m0v) : "memory");
  else if constexpr (POL == 1) asm volatile("s_mov_b32 m0, %3\n\ts_nop 0\n\tbuffer_load_dwordx4 %0, %1, %2 offen nt lds" ::"v"(voff), "s"(r), "s"(soff), "s"(m0v) : "memory");
  else asm volatile("s_mov_b32 m0, %3\n\ts_nop 0\n\tbuffer_load_dwordx4 %0, %1, %2 offen sc1 lds" ::"v"(voff), "s"(r), "s"(soff), "s"(m0v) : "memory");
}
template <int N> DI void wait_vm() { asm volatile("s_waitcnt vmcnt(%0)" ::"i"(N) : "memory"); }
DI void bar() { SB(); asm volatile("s_waitcnt lgkmcnt(0)\n\ts_barrier" ::: "memory"); SB(); }
DI float pl32_max(float v) {
  auto r = __builtin_amdgcn_permlane32_swap(__float_as_uint(v), __float_as_uint(v), false, false);
  return fmaxf(__uint_as_float(r[0]), __uint_as_float(r[1]));
}
DI float pl32_sum(float v) {
  auto r = __builtin_amdgcn_permlane32_swap(__float_as_uint(v), __float_as_uint(v), false, false);
  return __uint_as_float(r[0]) + __uint_as_float(r[1]);
}
DI unsigned lane_id() { unsigned l; asm volatile("v_mbcnt_lo_u32_b32 %0, -1, 0\n\tv_mbcnt_hi_u32_b32 %0, -1, %0" : "=v"(l)); return l; }
DI void poll_flags(gu64* flags, unsigned idx0, unsigned stride, unsigned n, unsigned skip, unsigned long long tag, unsigned lane) {
  const bool mine = lane < n && lane != skip;
  gu64* p = flags + (size_t)(idx0 + (mine ? lane : 0) * stride) * 16;
  for (unsigned spins = 0; spins < 400000u; ++spins) {
    const unsigned long long v = __hip_atomic_load(p, __ATOMIC_RELAXED, __HIP_MEMORY_SCOPE_AGENT);
    if (__all(!mine || v == tag)) break;
    __builtin_amdgcn_s_sleep(1);
  }
}

#define OUT_AUX 2
#define ROTMODE 2
#define POL_X 0
#define POL_WQ 0
#define O_ST_AUX 16
#define POL_O 2
#define POL_WO 0

__global__ __launch_bounds__(768, 3) void mha_kernel(const __hip_bfloat16* __restrict__ x, const __hip_bfloat16* __restrict__ kc,
                                                     const __hip_bfloat16* __restrict__ vc, const __hip_bfloat16* __restrict__ wq,
                                                     const __hip_bfloat16* __restrict__ wo, __hip_bfloat16* __restrict__ out,
                                                     char* __restrict__ ws, unsigned long long* diag) {
  __shared__ __attribute__((aligned(1024))) char smem[LDS_TOTAL];
  lds_char* lds = (lds_char*)smem;
  const unsigned ldsb = (unsigned)(size_t)smem;
  const unsigned tid = threadIdx.x, w = rfl(tid >> 6);
  STAMP(0);
  const unsigned xcd = blockIdx.x & 7, jj = blockIdx.x >> 3;
  const unsigned b = 4 * (xcd & 1) + (jj & 3), hd = 8 * (xcd >> 1) + (jj >> 2);
  gu64* flags = (gu64*)(ws + WS_FLAG);
  unsigned long long epoch;
  auto read_epoch = [&]() {
    asm volatile("s_load_dwordx2 %0, %1, 0x0\n\ts_waitcnt lgkmcnt(0)" : "=s"(epoch) : "s"((const char*)(ws + WS_FLAG) + (512 + b) * 128) : "memory");
  };

  const __amdgpu_buffer_rsrc_t rwo = __builtin_amdgcn_make_buffer_rsrc((void*)wo, (short)0, NH * DH * DM * 2, 0x00020000);
  bf16x8 qf[8];
  f32x16 oacc[4];
  float m, l;
  {
    const unsigned lane = tid & 63, r = lane & 31, h = lane >> 5;
    const unsigned t = w >> 1, j = w & 1;
    const unsigned g1 = (lane >> 4) & 1, qq = (lane & 15) >> 2, pp = lane & 3, sp = ((pp & 1) << 1) | (pp >> 1);
    const __amdgpu_buffer_rsrc_t rk = __builtin_amdgcn_make_buffer_rsrc((void*)kc, (short)0, NB * NH * SK * DH * 2, 0x00020000);
    const __amdgpu_buffer_rsrc_t rv = __builtin_amdgcn_make_buffer_rsrc((void*)vc, (short)0, NB * NH * SK * DH * 2, 0x00020000);
    const unsigned skv = (b * NH + hd) * (SK * DH * 2);
    const unsigned vk = (lane >> 4) * 256 + ((lane & 15) ^ (lane >> 4)) * 16;
    const unsigned vv = (lane >> 4) * 256 + ((lane & 15) ^ ((lane >> 4) << 2)) * 16;

    auto kv4 = [&](int s) {
      const unsigned lw = w - 4;
      const unsigned dst = ldsb + kv_slot(s) + lw * 4096u;
      const unsigned so = skv + ((lw >> 1) & 1) * 65536u + (lw & 1) * 4096u + (unsigned)s * 8192u;
#pragma unroll
      for (int p = 0; p < 4; ++p) {
        if (lw < 4) dma16<1>(rk, vk ^ (p << 6), so + p * 1024u, dst + p * 1024u);
        else dma16<1>(rv, vv, so + p * 1024u, dst + p * 1024u);
      }
    };
    const unsigned koff = 3 * (((jj >> 2) + (jj & 3)) % 3);
    if (w >= 4) {
      const unsigned lw = w - 4;
      const __amdgpu_buffer_rsrc_t rx = __builtin_amdgcn_make_buffer_rsrc((void*)x, (short)0, NB * SQ * DM * 2, 0x00020000);
      const __amdgpu_buffer_rsrc_t rw = __builtin_amdgcn_make_buffer_rsrc((void*)wq, (short)0, DM * NH * DH * 2, 0x00020000);
      const unsigned vx = (lane >> 3) * 1152 + (((lane & 7) ^ (lane >> 4) ^ ((lw & 1) << 2)) * 16);
      const unsigned sx = (b * SQ + 8 * lw) * 1152u;
      const unsigned vw = (lane >> 4) * 8192 + (((lane & 15) ^ ((lane >> 4) << 2)) * 16);
      const unsigned sw = 4 * lw * 8192u + hd * 256u;
      auto issue_q = [&](int i) {
        unsigned st = koff + i; st = st >= 9 ? st - 9 : st;
        const unsigned slot = ldsb + (unsigned)(i % 3) * QS_BYTES;
#pragma unroll
        for (int p = 0; p < 3; ++p) dma16<POL_X>(rx, vx, sx + (unsigned)(64 * p) * 1152u + st * 128u, slot + (lw + 8 * p) * 1024u);
#pragma unroll
        for (int p = 0; p < 2; ++p) dma16<POL_WQ>(rw, vw, sw + (st * 64 + 32 * p) * 8192u, slot + QS_W + (lw + 8 * p) * 1024u);
      };
      issue_q(0); issue_q(1);
      wait_vm<5>(); bar(); issue_q(2);
      wait_vm<5>(); bar(); issue_q(3);
      wait_vm<5>(); bar(); issue_q(4); kv4(0);
      wait_vm<9>(); bar(); issue_q(5);
      wait_vm<9>(); bar(); issue_q(6);
      wait_vm<5>(); bar(); issue_q(7);
      wait_vm<5>(); bar(); issue_q(8);
      wait_vm<5>(); bar(); kv4(1);
      wait_vm<4>(); bar(); kv4(2);
      read_epoch();
      bar();
      bar();
    } else {
      read_epoch();
      if (w < 2) {
        unsigned long long pc;
        asm volatile("s_getpc_b64 %0" : "=s"(pc));
        const unsigned off = (jj * 2 + w) * 1024u;
        if (off + 1024u <= CODE_BYTES) {
          u32x4 t;
          const char* p = (const char*)(pc & ~127ull) + off + lane * 16;
          asm volatile("global_load_dwordx4 %0, %1, off\n\ts_waitcnt vmcnt(0)" : "=v"(t) : "v"(p) : "memory");
        }
      }
      if (w == 2) {
        const unsigned hg_ = hd >> 3, hl_ = hd & 7, wg_ = b * NH + hd;
        const char* p = (const char*)ws + WS_FLAG;
        if (lane == 0) p = (const char*)kc + (size_t)wg_ * (SK * DH * 2);
        if (lane == 1) p = (const char*)vc + (size_t)wg_ * (SK * DH * 2);
        if (lane == 2) p = (const char*)ws + WS_O + (size_t)wg_ * 49152;
        if (lane == 3) p = (const char*)ws + WS_O + (size_t)(b * NH + 8 * hg_) * 49152;
        if (lane == 4) p = (const char*)ws + WS_O + (size_t)(b * NH + 8 * hg_ + 8) * 49152 - 64;
        if (lane >= 5 && lane < 9) p = (const char*)ws + WS_PART + (size_t)(((b * 8 + hl_) * 4 + (lane - 5)) * 4) * PART_BLK;
        if (lane == 9) p = (const char*)wo + (size_t)hg_ * (1024 * 1152);
        if (lane == 10) p = (const char*)wo + (size_t)(hg_ + 1) * (1024 * 1152) - 64;
        if (lane == 11) p = (const char*)out + (size_t)(b * SQ) * (DM * 2);
        if (lane == 12) p = (const char*)kc + (size_t)(wg_ + 1) * (SK * DH * 2) - 64;
        if (lane == 13) p = (const char*)vc + (size_t)(wg_ + 1) * (SK * DH * 2) - 64;
        unsigned t;
        asm volatile("global_load_dword %0, %1, off\n\ts_waitcnt vmcnt(0)" : "=v"(t) : "v"(p) : "memory");
      }
      const unsigned qh = w >> 1, dh = w & 1;
      const unsigned xa0 = (96 * qh + r) * 128 + ((h ^ ((r >> 1) & 7)) << 4);
      const unsigned wa0 = QS_W + (8 * h + qq) * 256 + ((4 * ((2 * dh) ^ qq) + 2 * g1 + (sp >> 1)) << 4) + (sp & 1) * 8;
      f32x16 qacc[3][2];
#pragma unroll
      for (int a = 0; a < 3; ++a)
#pragma unroll
        for (int i = 0; i < 16; ++i) { qacc[a][0][i] = 0.f; qacc[a][1][i] = 0.f; }
      bf16x8 bA[3], aA[2], bB[3], aB[2];
      unsigned xb, wb;
      auto rd = [&](bf16x8 (&bb)[3], bf16x8 (&aa)[2], int s) {
        const unsigned xs = xb ^ (s << 5), wb1 = wb ^ 64;
        aa[0] = lds_tr2(lds, wb + s * 4096, wb + s * 4096 + 1024);
        bb[0] = lds_rd128(lds, xs); bb[1] = lds_rd128(lds, xs + 4096); bb[2] = lds_rd128(lds, xs + 8192);
        aa[1] = lds_tr2(lds, wb1 + s * 4096, wb1 + s * 4096 + 1024);
      };
      auto mm = [&](bf16x8 (&bb)[3], bf16x8 (&aa)[2]) {
        qacc[0][0] = MFMA32(aa[0], bb[0], qacc[0][0]);
        qacc[1][0] = MFMA32(aa[0], bb[1], qacc[1][0]);
        qacc[2][0] = MFMA32(aa[0], bb[2], qacc[2][0]);
        qacc[0][1] = MFMA32(aa[1], bb[0], qacc[0][1]);
        qacc[1][1] = MFMA32(aa[1], bb[1], qacc[1][1]);
        qacc[2][1] = MFMA32(aa[1], bb[2], qacc[2][1]);
      };
      bar();
      STAMP(1);
      unsigned long long qc0 = TNOW(), qr0 = __builtin_amdgcn_s_memrealtime();
      xb = xa0; wb = wa0;
      asm volatile("" : "+v"(xb), "+v"(wb));
      rd(bA, aA, 0);
      auto mix = [&]() { SG_MFMA(1); SG_DSRD(2); SG_MFMA(1); SG_DSRD(2); SG_MFMA(1); SG_DSRD(1); SG_MFMA(1); SG_DSRD(1); SG_MFMA(1); SG_DSRD(1); SG_MFMA(1); SB(); };
#pragma unroll 1
      for (int st = 0; st < 8; ++st) {
        SB(); rd(bB, aB, 1); mm(bA, aA); mix();
        rd(bA, aA, 2); mm(bB, aB); mix();
        rd(bB, aB, 3); mm(bA, aA); mix();
        bar();
        const unsigned so = (unsigned)((st + 1) % 3) * QS_BYTES;
        xb = xa0 + so; wb = wa0 + so;
        asm volatile("" : "+v"(xb), "+v"(wb));
        rd(bA, aA, 0); mm(bB, aB); mix();
      }
      SB(); rd(bB, aB, 1); mm(bA, aA); mix();
      rd(bA, aA, 2); mm(bB, aB); mix();
      rd(bB, aB, 3); mm(bA, aA); mix();
      mm(bB, aB);
      WREC(32, TNOW() - qc0); WREC(33, __builtin_amdgcn_s_memrealtime() - qr0);
      bar();
      STAMP(4);
#pragma unroll
      for (int a = 0; a < 3; ++a)
#pragma unroll
        for (int d = 0; d < 2; ++d) {
#pragma unroll
          for (int i = 0; i < 16; ++i) qacc[a][d][i] *= QSCALE;
#pragma unroll
          for (int s2 = 0; s2 < 2; ++s2) {
            const unsigned blk = (3 * qh + a) * 8 + 4 * dh + 2 * d + s2;
            *(lds_bf16x8*)(lds + qblk(blk) + lane * 16) = pack8(qacc[a][d], s2);
          }
        }
      bar();
    }
#pragma unroll
    for (int i = 0; i < 8; ++i) qf[i] = lds_rd128(lds, qblk(t * 8 + i) + lane * 16);

    const unsigned grp = w >> 2;
    float l0, l1;
    bool safe = false;
    const unsigned ka0 = j * 8192 + r * 256 + ((h ^ (r & 15)) << 4);
    const unsigned va0 = (16384 + j * 8192 + (4 * h + qq) * 256 + ((4 * qq + 2 * g1 + (sp >> 1)) << 4) + (sp & 1) * 8) ^ (j << 7);
    const unsigned vkx = vk ^ ((w & 3) << 6);
    const unsigned sp0 = skv + ((w >> 3) & 1) * 65536u + (w & 7) * 1024u;
    const unsigned sp1 = skv + (((w + 12) >> 3) & 1) * 65536u + ((w + 12) & 7) * 1024u;
    const unsigned sp2 = skv + (((w + 24) >> 3) & 1) * 65536u + ((w + 24) & 7) * 1024u;

    bf16x8 kf[4];
    auto step = [&](auto G_, int s) {
      constexpr int G = decltype(G_)::value;
      constexpr int NP = G == 2 ? 2 : 3;
      unsigned vb = va0 + kv_slot(s), kc0 = ka0 + kv_slot(s), kb = ka0 + kv_slot(s + 1);
      asm volatile("" : "+v"(kb), "+v"(vb), "+v"(kc0));
      __builtin_amdgcn_s_setprio(1);
      f32x16 sc;
      bf16x8 vf[4][2];
      {
        bf16x8 kg[4];
#pragma unroll
        for (int ks = 0; ks < 4; ++ks) kg[ks] = lds_rd128(lds, kc0 ^ ((ks + 4) << 5));
#pragma unroll
        for (int i = 0; i < 16; ++i) sc[i] = 0.f;
#pragma unroll
        for (int ks = 0; ks < 4; ++ks) sc = MFMA32(kf[ks], qf[ks], sc);
#pragma unroll
        for (int d = 0; d < 4; ++d) { const unsigned va = vb ^ (d << 6); vf[d][0] = lds_tr2(lds, va, va + 2048); }
#pragma unroll
        for (int ks = 0; ks < 4; ++ks) sc = MFMA32(kg[ks], qf[ks + 4], sc);
        SG_MFMA(1); SG_DSRD(2); SG_MFMA(1); SG_DSRD(2); SG_MFMA(1); SG_DSRD(2); SG_MFMA(1); SG_DSRD(2);
        SG_MFMA(1); SG_DSRD(2); SG_MFMA(1); SG_DSRD(2); SG_MFMA(2);
        SB();
        asm volatile("" : "+v"(sc));
      }
      __builtin_amdgcn_s_setprio(0);
      if constexpr (G == 1) { if (s <= 5) wait_vm<NP>(); else if (s == 6) wait_vm<0>(); }
      bar();
#pragma unroll
      for (int d = 0; d < 4; ++d) { const unsigned va = vb ^ (d << 6); vf[d][1] = lds_tr2(lds, va + 4096, va + 4096 + 2048); }
      SB();
      if (safe) {
        const float t0 = fmaxf(fmaxf(sc[0], sc[1]), sc[2]), t1 = fmaxf(fmaxf(sc[3], sc[4]), sc[5]), t2 = fmaxf(fmaxf(sc[6], sc[7]), sc[8]);
        const float t3 = fmaxf(fmaxf(sc[9], sc[10]), sc[11]), t4 = fmaxf(fmaxf(sc[12], sc[13]), sc[14]);
        float tm = fmaxf(fmaxf(fmaxf(t0, t1), t2), fmaxf(fmaxf(t3, t4), sc[15]));
        tm = pl32_max(tm);
        if (__any(tm > m + THR)) {
          const float mn = fmaxf(m, tm);
          const float al = __builtin_amdgcn_exp2f(m - mn);
          l0 *= al; l1 *= al; m = mn;
#pragma unroll
          for (int d = 0; d < 4; ++d)
#pragma unroll
            for (int i = 0; i < 16; ++i) oacc[d][i] *= al;
        }
#pragma unroll
        for (int i = 0; i < 16; ++i) sc[i] -= m;
      }
      SB();
#pragma unroll
      for (int i = 0; i < 16; ++i) sc[i] = __builtin_amdgcn_exp2f(sc[i]);
      bf16x8 p0 = pack8(sc, 0), p1 = pack8(sc, 1);
#pragma unroll
      for (int i = 0; i < 16; i += 2) { l0 += sc[i]; l1 += sc[i + 1]; }
      asm volatile("" : "+v"(p0), "+v"(p1), "+v"(l0), "+v"(l1));
      if constexpr (G == 0) { if (s <= 5) wait_vm<NP>(); else if (s == 6) wait_vm<0>(); }
      bar();
      __builtin_amdgcn_s_setprio(1);
#pragma unroll
      for (int d = 0; d < 4; ++d) oacc[d] = MFMA32(vf[d][0], p0, oacc[d]);
#pragma unroll
      for (int ks = 0; ks < 4; ++ks) kf[ks] = lds_rd128(lds, kb ^ (ks << 5));
#pragma unroll
      for (int d = 0; d < 4; ++d) oacc[d] = MFMA32(vf[d][1], p1, oacc[d]);
      SG_MFMA(2); SG_DSRD(1); SG_MFMA(1); SG_DSRD(1); SG_MFMA(1); SG_DSRD(1); SG_MFMA(1); SG_DSRD(1); SG_MFMA(3);
      SB();
      asm volatile("" : "+v"(oacc[0]), "+v"(oacc[1]), "+v"(oacc[2]), "+v"(oacc[3]));
      __builtin_amdgcn_s_setprio(0);
      SB();
      if (s <= 4) {
        const unsigned dst = ldsb + kv_slot(s + 3) + w * 1024u, sofs = (unsigned)(s + 3) * 8192u;
        dma16<1>(rk, vkx, sp0 + sofs, dst);
        if constexpr (G == 0) dma16<1>(rk, vkx, sp1 + sofs, dst + 12288u); else dma16<1>(rv, vv, sp1 + sofs, dst + 12288u);
        if constexpr (G != 2) dma16<1>(rv, vv, sp2 + sofs, dst + 24576u);
      }
      if constexpr (G == 2) { if (s <= 4) wait_vm<NP>(); else if (s == 5) wait_vm<0>(); }
      if (s >= 6 && !safe) {
        const unsigned pc = ((b & 3) * 8 + (hd & 7)) * 36 + w * 3 + (s - 6) * 2;
        const unsigned so = (hd >> 3) * (1024u * 1152u) + pc * 1024u;
        dma16<POL_WO>(rwo, lane * 16, so, ldsb + SPARE + (w & 7) * 1024u);
        if (s == 6) dma16<POL_WO>(rwo, lane * 16, so + 1024u, ldsb + SPARE + (w & 7) * 1024u);
      }
      bar();
    };
    auto run = [&](auto G_) {
      constexpr int G = decltype(G_)::value;
      {
        unsigned kb0 = ka0 + kv_slot(0);
        asm volatile("" : "+v"(kb0));
#pragma unroll
        for (int ks = 0; ks < 4; ++ks) kf[ks] = lds_rd128(lds, kb0 ^ (ks << 5));
      }
      if constexpr (G >= 1) bar();
      if constexpr (G == 2) { wait_vm<4>(); bar(); }
#pragma unroll 1
      for (int s = 0; s < 8; ++s) step(G_, s);
      if constexpr (G == 0) bar();
      if constexpr (G <= 1) bar();
    };
    unsigned long long ca0 = 0, ra0 = 0;
#pragma unroll 1
    for (;;) {
#pragma unroll
      for (int d = 0; d < 4; ++d)
#pragma unroll
        for (int i = 0; i < 16; ++i) oacc[d][i] = 0.f;
      m = safe ? -__builtin_inff() : 0.f; l0 = 0.f; l1 = 0.f;
      bar(); STAMP(5);
      ca0 = TNOW(); ra0 = __builtin_amdgcn_s_memrealtime();
      if (grp == 0) run(IC<0>{});
      else if (grp == 1) run(IC<1>{});
      else run(IC<2>{});
      WREC(30, TNOW() - ca0); WREC(31, __builtin_amdgcn_s_memrealtime() - ra0);
      STAMP(13);
      const unsigned ln = lane_id();
      l = pl32_sum(l0 + l1);
      const unsigned xw = w * 4096 + ln * 16;
#pragma unroll
      for (int i = 0; i < 4; ++i) *(lds_bf16x8*)(lds + xw + i * 1024) = pack8(oacc[2 + (i >> 1)], i & 1);
      if (ln < 32) { f32x2 ml = {m, l}; *(lds_f32x2*)(lds + ML_OFF + w * 256 + ln * 8) = ml; }
      const bool bad = __any(!(l > L_MIN && l < L_MAX));
      if (ln == 0) *(__attribute__((address_space(3))) unsigned*)(lds + VOTE_OFF + w * 4) = bad ? 1u : 0u;
      bar();
      STAMP(6);
      if (safe) break;
      const unsigned vote = *(__attribute__((address_space(3))) unsigned*)(lds + VOTE_OFF + (ln < 12 ? ln : 0) * 4);
      if (!__any(vote != 0)) break;
      safe = true;
      bar();
      if (w >= 4) {
        const unsigned lw = rfl(w - 4), sk = rfl((b * NH + hd) * (SK * DH * 2));
        const unsigned vo = (ln >> 4) * 256 + (lw < 4 ? ((ln & 15) ^ (ln >> 4)) : ((ln & 15) ^ ((ln >> 4) << 2))) * 16;
        const __amdgpu_buffer_rsrc_t rr = __builtin_amdgcn_make_buffer_rsrc((void*)(lw < 4 ? kc : vc), (short)0, NB * NH * SK * DH * 2, 0x00020000);
#pragma unroll 1
        for (unsigned q = 0; q < 12; ++q) {
          const unsigned st = q >> 2, p = q & 3;
          dma16<1>(rr, lw < 4 ? vo ^ (p << 6) : vo, rfl(sk + ((lw >> 1) & 1) * 65536u + (lw & 1) * 4096u + st * 8192u + p * 1024u), rfl(ldsb + kv_slot(st) + lw * 4096u + p * 1024u));
        }
        wait_vm<0>();
      }
    }
  }

  const unsigned lane = lane_id();
  const unsigned hg = hd >> 3, hl = hd & 7, rh = hl & 1, cq = hl >> 1;
  const unsigned long long tag = epoch + 1;
  const unsigned wg = b * NH + hd;
  auto wo_voff = [&](int i) {
    const unsigned n = 64 * i + lane, p = n / 18, ch = n - 18 * p;
    const unsigned k = (p & 19) | ((p & 4) << 1) | ((p & 8) >> 1);
    return k * 1152 + ch * 16;
  };
  auto wo_issue = [&](const unsigned (&vo)[9], unsigned i) {
    const unsigned kh = w & 1, hh = (hl + (i >> 1)) & 7;
    const unsigned so = ((8 * hg + hh) * 128 + (2 * (i & 1) + kh) * 32) * 1152u + cq * 288u;
    const unsigned dst = ldsb + p2_wslot(i) + kh * 9216u;
#pragma unroll
    for (int p = 0; p < 9; ++p) dma16<POL_WO>(rwo, vo[p], so, dst + p * 1024u);
  };
  unsigned wvo[9];
  if (w == 8 || w == 9) {
#pragma unroll
    for (int p = 0; p < 9; ++p) wvo[p] = wo_voff(p);
  }

  {
    const unsigned r = lane & 31, h = lane >> 5, t = w >> 1, j = w & 1;
    f32x2 o = *(lds_f32x2*)(lds + ML_OFF + (w ^ 1) * 256 + r * 8);
    const float mn = fmaxf(m, o[0]);
    const float f = __builtin_amdgcn_exp2f(m - mn), fo = __builtin_amdgcn_exp2f(o[0] - mn);
    const float inv = 1.0f / (l * f + o[1] * fo);
    const float sa = f * inv, sb = fo * inv;
    const unsigned xr = (w ^ 1) * 4096 + lane * 16;
#pragma unroll
    for (int i = 0; i < 4; ++i) {
      const u32x4 v = __builtin_bit_cast(u32x4, lds_rd128(lds, xr + i * 1024));
#pragma unroll
      for (int e = 0; e < 4; ++e) {
        oacc[i >> 1][8 * (i & 1) + 2 * e] = oacc[i >> 1][8 * (i & 1) + 2 * e] * sa + __uint_as_float(v[e] << 16) * sb;
        oacc[i >> 1][8 * (i & 1) + 2 * e + 1] = oacc[i >> 1][8 * (i & 1) + 2 * e + 1] * sa + __uint_as_float(v[e] & 0xffff0000u) * sb;
      }
    }
    const __amdgpu_buffer_rsrc_t ro = __builtin_amdgcn_make_buffer_rsrc((void*)(ws + WS_O), (short)0, O_BYTES, 0x00020000);
    const unsigned so = ((wg * 6 + t) * 8 + 4 * j) * 1024u;
#pragma unroll
    for (int i = 0; i < 4; ++i)
      __builtin_amdgcn_raw_buffer_store_b128(__builtin_bit_cast(u32x4, pack8(oacc[i >> 1], i & 1)), ro, so + lane * 16 + i * 1024, 0, O_ST_AUX);
    STAMP(7);
    if (w == 8 || w == 9) { wo_issue(wvo, 0); wo_issue(wvo, 1); wait_vm<18>(); } else wait_vm<0>();
    STAMP(8);
    bar();
    if (tid == 0) __hip_atomic_store(flags + (size_t)wg * 16, tag, __ATOMIC_RELAXED, __HIP_MEMORY_SCOPE_AGENT);
  }
  STAMP(14);

  f32x4 acc[3][5];
#pragma unroll
  for (int a = 0; a < 3; ++a)
#pragma unroll
    for (int n = 0; n < 5; ++n) acc[a][n] = f32x4{0.f, 0.f, 0.f, 0.f};
  const unsigned nh = (w >> 2) & 1, kh = w & 1, qh = (w >> 1) & 1;
  if (w >= 10) {
    const __amdgpu_buffer_rsrc_t ro = __builtin_amdgcn_make_buffer_rsrc((void*)(ws + WS_O), (short)0, O_BYTES, 0x00020000);
    auto o_issue = [&](unsigned i) {
      const unsigned hh = (hl + (i >> 1)) & 7;
      const unsigned so = (((b * NH + 8 * hg + hh) * 6 + 3 * rh) * 8 + 2 * (2 * (i & 1) + kh)) * 1024u;
      const unsigned dst = ldsb + p2_oslot(i) + kh * 6144u;
#pragma unroll
      for (int q = 0; q < 3; ++q) { dma16<POL_O>(ro, lane * 16, so + q * 8192u, dst + q * 2048u); dma16<POL_O>(ro, lane * 16, so + q * 8192u + 1024u, dst + q * 2048u + 1024u); }
    };
    gu64* fp = flags + (size_t)(b * NH + 8 * hg + (lane & 7)) * 16;
    unsigned long long pv;
    auto poll_issue = [&]() { asm volatile("global_load_dwordx2 %0, %1, off sc1" : "=v"(pv) : "v"(fp) : "memory"); };
    unsigned rdy = 1u << hl;
    auto poll_take = [&]() {
      asm volatile("s_waitcnt vmcnt(0)" : "+v"(pv) :: "memory");
      rdy |= (unsigned)__ballot(pv == tag) & 0xffu;
    };
    o_issue(0); o_issue(1);
    poll_issue();
    STAMPW(10, 22);
    bool pending = true;
    unsigned spins = 0;
#pragma unroll 1
    for (unsigned i = 0; i < 16; ++i) {
      if (pending) wait_vm<7>(); else if (i <= 14) wait_vm<6>(); else wait_vm<0>();
      if (i == 0) STAMPW(10, 23);
      bar();
      if (i + 2 < 16) {
        const unsigned hh = (hl + ((i + 2) >> 1)) & 7;
        if (pending) { poll_take(); pending = false; }
        while (!((rdy >> hh) & 1) && spins < 400000u) { __builtin_amdgcn_s_sleep(1); poll_issue(); poll_take(); ++spins; }
        o_issue(i + 2);
      }
    }
  } else if (w >= 8) {
    wo_issue(wvo, 2); wo_issue(wvo, 3); wo_issue(wvo, 4);
#pragma unroll 1
    for (unsigned i = 0; i < 16; ++i) {
      if (i <= 11) wait_vm<36>(); else if (i == 12) wait_vm<27>(); else if (i == 13) wait_vm<18>(); else if (i == 14) wait_vm<9>(); else wait_vm<0>();
      bar();
      if (i + 5 < 16) wo_issue(wvo, i + 5);
    }
  } else {
    const unsigned c = lane & 15, g = lane >> 4;
    unsigned ba[3];
#pragma unroll
    for (int a = 0; a < 3; ++a) {
      const unsigned qi = 3 * qh + a;
      ba[a] = kh * 6144 + ((qi >> 1) * 2 + (g >> 1)) * 1024 + (16 * (qi & 1) + c + 32 * (g & 1)) * 16;
    }
    const unsigned aa = kh * 9216 + ((g >> 1) * 16 + (g & 1) * 4 + (c >> 2)) * 288 + 5 * nh * 32 + (c & 3) * 8;
    bf16x8 bX[3], aX[5], bY[3], aY[5];
    auto rd = [&](auto NT_, bf16x8 (&bb)[3], bf16x8 (&av)[5], unsigned i) {
      constexpr int NT = decltype(NT_)::value;
      unsigned ob = p2_oslot(i), wb = aa + p2_wslot(i);
      asm volatile("" : "+v"(wb));
#pragma unroll
      for (int a = 0; a < 3; ++a) bb[a] = lds_rd128(lds, ba[a] + ob);
#pragma unroll
      for (int n = 0; n < NT; ++n) av[n] = lds_tr2(lds, wb + n * 32, wb + n * 32 + 2304);
    };
    auto mm = [&](auto NT_, bf16x8 (&bb)[3], bf16x8 (&av)[5]) {
      constexpr int NT = decltype(NT_)::value;
#pragma unroll
      for (int n = 0; n < NT; ++n)
#pragma unroll
        for (int a = 0; a < 3; ++a) acc[a][n] = MFMA16(av[n], bb[a], acc[a][n]);
    };
    unsigned long long t0 = 0;
    auto mix = [&](auto NT_) {
      constexpr int NT = decltype(NT_)::value;
#pragma unroll
      for (int k = 0; k < 3 + 2 * NT - (NT == 4 ? 0 : 0); ++k) {
        __builtin_amdgcn_sched_group_barrier(0x008, 1, 0);
        __builtin_amdgcn_sched_group_barrier(0x100, 1, 0);
      }
      __builtin_amdgcn_sched_group_barrier(0x008, 3 * NT, 0);
    };
    auto gemm = [&](auto NT_) {
      bar();
      STAMP(15);
      t0 = TNOW();
      rd(NT_, bX, aX, 0);
#pragma unroll 1
      for (unsigned i = 1; i < 15; i += 2) {
        bar(); rd(NT_, bY, aY, i); mm(NT_, bX, aX); mix(NT_);
        bar(); rd(NT_, bX, aX, i + 1); mm(NT_, bY, aY); mix(NT_);
      }
      bar(); rd(NT_, bY, aY, 15); mm(NT_, bX, aX); mix(NT_);
      SB(); mm(NT_, bY, aY);
    };
    if (nh == 0) gemm(IC<5>{}); else gemm(IC<4>{});
    WREC(44, TNOW() - t0);
  }
  STAMP(16);
  bar();
  if (w < 8) {
    const unsigned c = lane & 15, g = lane >> 4;
    const unsigned base = kh * IMG_BYTES + (48 * qh + c) * IMG_STRIDE + (80 * nh + 4 * g) * 4;
#pragma unroll
    for (int a = 0; a < 3; ++a)
#pragma unroll
      for (int n = 0; n < 5; ++n)
        if (n < 4 || nh == 0) *(lds_f32x4*)(lds + base + a * 16 * IMG_STRIDE + n * 64) = acc[a][n];
  }
  bar();
  {
    const __amdgpu_buffer_rsrc_t rp = __builtin_amdgcn_make_buffer_rsrc((void*)(ws + WS_PART), (short)0, PART_BYTES, 0x00020000);
    const unsigned pbase = ((b * 8 + hl) * 4 + hg) * 4 * PART_BLK;
#pragma unroll 1
    for (unsigned n = tid; n < 72 * 18; n += 768) {
      const unsigned rr = n / 18, ch = n - rr * 18;
      unsigned dst = rr / 24; const unsigned row = rr - dst * 24;
      dst += dst >= hg ? 1 : 0;
      const unsigned a0 = (24 * dst + row) * IMG_STRIDE + ch * 32;
      const f32x4 v0 = *(lds_f32x4*)(lds + a0) + *(lds_f32x4*)(lds + IMG_BYTES + a0);
      const f32x4 v1 = *(lds_f32x4*)(lds + a0 + 16) + *(lds_f32x4*)(lds + IMG_BYTES + a0 + 16);
      const u32x4 pk = {cvtpk(v0[0], v0[1]), cvtpk(v0[2], v0[3]), cvtpk(v1[0], v1[1]), cvtpk(v1[2], v1[3])};
      __builtin_amdgcn_raw_buffer_store_b128(pk, rp, dst * PART_BLK + row * 288 + ch * 16 + pbase, 0, 16);
    }
    wait_vm<0>();
    bar();
    if (tid == 0) __hip_atomic_store(flags + (size_t)(256 + wg) * 16, tag, __ATOMIC_RELAXED, __HIP_MEMORY_SCOPE_AGENT);
    STAMP(17);
    if (w == 0) poll_flags(flags, 256 + b * NH + hl, 8, 4, hg, tag, lane);
    bar();
    STAMP(18);
    if (tid < 24 * 18) {
      const unsigned row = tid / 18, ch = tid - row * 18;
      u32x4 u[3];
#pragma unroll
      for (int k = 0; k < 3; ++k) {
        const unsigned src = k + (k >= (int)hg ? 1 : 0);
        u[k] = __builtin_amdgcn_raw_buffer_load_b128(rp, (((b * 8 + hl) * 4 + src) * 4 + hg) * PART_BLK + row * 288 + ch * 16, 0, 16);
      }
      const unsigned a0 = (24 * hg + row) * IMG_STRIDE + ch * 32;
      f32x4 v0 = *(lds_f32x4*)(lds + a0) + *(lds_f32x4*)(lds + IMG_BYTES + a0);
      f32x4 v1 = *(lds_f32x4*)(lds + a0 + 16) + *(lds_f32x4*)(lds + IMG_BYTES + a0 + 16);
#pragma unroll
      for (int k = 0; k < 3; ++k) {
        v0[0] += __uint_as_float(u[k][0] << 16); v0[1] += __uint_as_float(u[k][0] & 0xffff0000u);
        v0[2] += __uint_as_float(u[k][1] << 16); v0[3] += __uint_as_float(u[k][1] & 0xffff0000u);
        v1[0] += __uint_as_float(u[k][2] << 16); v1[1] += __uint_as_float(u[k][2] & 0xffff0000u);
        v1[2] += __uint_as_float(u[k][3] << 16); v1[3] += __uint_as_float(u[k][3] & 0xffff0000u);
      }
      const u32x4 o = {cvtpk(v0[0], v0[1]), cvtpk(v0[2], v0[3]), cvtpk(v1[0], v1[1]), cvtpk(v1[2], v1[3])};
      const __amdgpu_buffer_rsrc_t rout = __builtin_amdgcn_make_buffer_rsrc((void*)out, (short)0, NB * SQ * DM * 2, 0x00020000);
      __builtin_amdgcn_raw_buffer_store_b128(o, rout, ((b * SQ + 96 * rh + 24 * hg + row) * DM + 144 * cq + 8 * ch) * 2, 0, OUT_AUX);
    }
    if (tid == 0 && hd == 0) __hip_atomic_store(flags + (size_t)(512 + b) * 16, tag, __ATOMIC_RELAXED, __HIP_MEMORY_SCOPE_AGENT);
  }
  asm volatile("s_waitcnt vmcnt(0)" ::: "memory");
  STAMP(19);
}

__global__ void spin_kernel(const unsigned long long* diag) {
  __shared__ long long v[256];
  const unsigned t = threadIdx.x;
  unsigned long long mn = ~0ull;
  __shared__ unsigned long long s0[256];
  s0[t] = diag[t * 32 + 0];
  __syncthreads();
  for (int i = 0; i < 256; ++i) mn = s0[i] < mn ? s0[i] : mn;
  v[t] = (long long)(diag[t * 32 + ST_J] - mn);
  __syncthreads();
  __shared__ long long smed;
  { int c = 0; for (int k = 0; k < 256; ++k) c += v[k] < v[t] || (v[k] == v[t] && k < (int)t); if (c == ST_RANK) smed = v[t]; }
  __syncthreads();
  if (t == 0) {
    const long long med = smed;
    const unsigned long long dur = (unsigned long long)med * 40;
    const unsigned long long t0 = __builtin_amdgcn_s_memrealtime();
    while (__builtin_amdgcn_s_memrealtime() - t0 < dur) __builtin_amdgcn_s_sleep(8);
  }
}

extern "C" void kernel_launch(void* const* d_in, const int* in_sizes, int n_in, void* d_out, int out_size, void* d_ws, size_t ws_size,
                              hipStream_t stream) {
  const __hip_bfloat16* x = (const __hip_bfloat16*)d_in[0];
  const __hip_bfloat16* kc = (const __hip_bfloat16*)d_in[1];
  const __hip_bfloat16* vc = (const __hip_bfloat16*)d_in[2];
  const __hip_bfloat16* wq = (const __hip_bfloat16*)d_in[3];
  const __hip_bfloat16* wo = (const __hip_bfloat16*)d_in[4];
  unsigned long long* diag = (unsigned long long*)((char*)d_ws + (64u << 20));
  mha_kernel<<<256, 768, 0, stream>>>(x, kc, vc, wq, wo, (__hip_bfloat16*)d_out, (char*)d_ws, diag);
  spin_kernel<<<1, 256, 0, stream>>>(diag);
}
```

```cpp
#include <hip/hip_runtime.h>
#include <hip/hip_bf16.h>
#include <type_traits>

typedef __bf16 bf16x8 __attribute__((ext_vector_type(8)));
typedef __bf16 bf16x4 __attribute__((ext_vector_type(4)));
typedef __bf16 bf16x2 __attribute__((ext_vector_type(2)));
typedef float f32x16 __attribute__((ext_vector_type(16)));
typedef float f32x4 __attribute__((ext_vector_type(4)));
typedef float f32x2 __attribute__((ext_vector_type(2)));
typedef unsigned u32x4 __attribute__((ext_vector_type(4)));
typedef unsigned u32x2 __attribute__((ext_vector_type(2)));
typedef __attribute__((address_space(3))) char lds_char;
typedef __attribute__((address_space(3))) bf16x4 lds_bf16x4;
typedef __attribute__((address_space(3))) bf16x8 lds_bf16x8;
typedef __attribute__((address_space(3))) f32x4 lds_f32x4;
typedef __attribute__((address_space(3))) f32x2 lds_f32x2;
typedef __attribute__((address_space(1))) unsigned long long gu64;

#define DI __device__ __forceinline__
#define MFMA32(a, b, c) __builtin_amdgcn_mfma_f32_32x32x16_bf16((a), (b), (c), 0, 0, 0)
#define MFMA16(a, b, c) __builtin_amdgcn_mfma_f32_16x16x32_bf16((a), (b), (c), 0, 0, 0)
#define SB() __builtin_amdgcn_sched_barrier(0)
template <int V> using IC = std::integral_constant<int, V>;
#define SG_MFMA(n) __builtin_amdgcn_sched_group_barrier(0x008, (n), 0)
#define SG_DSRD(n) __builtin_amdgcn_sched_group_barrier(0x100, (n), 0)

#define STAMPW(wv, i) do {} while (0)
#define STAMP(i) do {} while (0)
#define WREC(i, v) do {} while (0)
#define TNOW() 0ull

constexpr int NB = 8, SQ = 192, DM = 576, NH = 32, SK = 512, DH = 128;
constexpr float QSCALE = 0.08838834764831845f * 1.4426950408889634f;
constexpr float THR = 8.0f;

constexpr unsigned CODE_BYTES = 26 * 1024;
constexpr size_t WS_O = 0;
constexpr size_t WS_PART = 16u << 20;
constexpr size_t WS_FLAG = 32u << 20;
constexpr unsigned PART_BLK = 24 * 144 * 2;
constexpr unsigned O_BYTES = NB * NH * 6 * 8 * 1024, PART_BYTES = 8 * 8 * 4 * 4 * PART_BLK;

constexpr unsigned QS_BYTES = 40960, QS_W = 24576;
constexpr unsigned SPARE = 155648, LDS_TOTAL = 163840, ML_OFF = 98304, VOTE_OFF = 101376;
constexpr float L_MIN = 9.313225746154785e-10f, L_MAX = 1048576.0f;
DI constexpr unsigned kv_slot(int s) { return (unsigned)((s + 3) & 3) * QS_BYTES; }
DI unsigned qblk(unsigned blk) { return blk < 40 ? 2 * QS_BYTES + blk * 1024 : SPARE + (blk - 40) * 1024; }
constexpr unsigned P2_OSLOT = 12288, P2_WSLOT = 18432, P2_WBASE = 53248, IMG_STRIDE = 592, IMG_BYTES = 96 * IMG_STRIDE;
DI unsigned p2_oslot(unsigned i) { return (i & 3) * P2_OSLOT; }
DI unsigned p2_wslot(unsigned i) { return P2_WBASE + ((i + 3) % 6) * P2_WSLOT; }

DI unsigned rfl(unsigned x) { return __builtin_amdgcn_readfirstlane(x); }
DI unsigned cvtpk(float lo, float hi) { f32x2 v = {lo, hi}; bf16x2 b = __builtin_convertvector(v, bf16x2); return __builtin_bit_cast(unsigned, b); }
DI bf16x8 pack8(const f32x16& x, int s) {
  u32x4 p = {cvtpk(x[8 * s], x[8 * s + 1]), cvtpk(x[8 * s + 2], x[8 * s + 3]), cvtpk(x[8 * s + 4], x[8 * s + 5]), cvtpk(x[8 * s + 6], x[8 * s + 7])};
  return __builtin_bit_cast(bf16x8, p);
}
DI bf16x8 lds_rd128(lds_char* base, unsigned off) { return *(lds_bf16x8*)(base + off); }
DI bf16x8 lds_tr2(lds_char* base, unsigned off0, unsigned off1) {
  bf16x4 lo = __builtin_amdgcn_ds_read_tr16_b64_v4bf16((lds_bf16x4*)(base + off0));
  bf16x4 hi = __builtin_amdgcn_ds_read_tr16_b64_v4bf16((lds_bf16x4*)(base + off1));
  return __builtin_shufflevector(lo, hi, 0, 1, 2, 3, 4, 5, 6, 7);
}
template <int POL> DI void dma16(__amdgpu_buffer_rsrc_t r, unsigned voff, unsigned soff, unsigned m0v) {
  if constexpr (POL == 0) asm volatile("s_mov_b32 m0, %3\n\ts_nop 0\n\tbuffer_load_dwordx4 %0, %1, %2 offen lds" ::"v"(voff), "s"(r), "s"(soff), "s"(m0v) : "memory");
  else if constexpr (POL == 1) asm volatile("s_mov_b32 m0, %3\n\ts_nop 0\n\tbuffer_load_dwordx4 %0, %1, %2 offen nt lds" ::"v"(voff), "s"(r), "s"(soff), "s"(m0v) : "memory");
  else asm volatile("s_mov_b32 m0, %3\n\ts_nop 0\n\tbuffer_load_dwordx4 %0, %1, %2 offen sc1 lds" ::"v"(voff), "s"(r), "s"(soff), "s"(m0v) : "memory");
}
template <int N> DI void wait_vm() { asm volatile("s_waitcnt vmcnt(%0)" ::"i"(N) : "memory"); }
DI void bar() { SB(); asm volatile("s_waitcnt lgkmcnt(0)\n\ts_barrier" ::: "memory"); SB(); }
DI float pl32_max(float v) {
  auto r = __builtin_amdgcn_permlane32_swap(__float_as_uint(v), __float_as_uint(v), false, false);
  return fmaxf(__uint_as_float(r[0]), __uint_as_float(r[1]));
}
DI float pl32_sum(float v) {
  auto r = __builtin_amdgcn_permlane32_swap(__float_as_uint(v), __float_as_uint(v), false, false);
  return __uint_as_float(r[0]) + __uint_as_float(r[1]);
}
DI unsigned lane_id() { unsigned l; asm volatile("v_mbcnt_lo_u32_b32 %0, -1, 0\n\tv_mbcnt_hi_u32_b32 %0, -1, %0" : "=v"(l)); return l; }
DI void poll_flags(gu64* flags, unsigned idx0, unsigned stride, unsigned n, unsigned skip, unsigned long long tag, unsigned lane) {
  const bool mine = lane < n && lane != skip;
  gu64* p = flags + (size_t)(idx0 + (mine ? lane : 0) * stride) * 16;
  for (unsigned spins = 0; spins < 400000u; ++spins) {
    const unsigned long long v = __hip_atomic_load(p, __ATOMIC_RELAXED, __HIP_MEMORY_SCOPE_AGENT);
    if (__all(!mine || v == tag)) break;
    __builtin_amdgcn_s_sleep(1);
  }
}

#define OUT_AUX 2
#define ROTMODE 2
#define POL_X 0
#define POL_WQ 0
#define O_ST_AUX 16
#define POL_O 2
#define POL_WO 0

__global__ __launch_bounds__(768, 3) void mha_kernel(const __hip_bfloat16* __restrict__ x, const __hip_bfloat16* __restrict__ kc,
                                                     const __hip_bfloat16* __restrict__ vc, const __hip_bfloat16* __restrict__ wq,
                                                     const __hip_bfloat16* __restrict__ wo, __hip_bfloat16* __restrict__ out,
                                                     char* __restrict__ ws, unsigned long long* diag) {
  __shared__ __attribute__((aligned(1024))) char smem[LDS_TOTAL];
  lds_char* lds = (lds_char*)smem;
  const unsigned ldsb = (unsigned)(size_t)smem;
  const unsigned tid = threadIdx.x, w = rfl(tid >> 6);
  STAMP(0);
  const unsigned xcd = blockIdx.x & 7, jj = blockIdx.x >> 3;
  const unsigned b = 4 * (xcd & 1) + (jj & 3), hd = 8 * (xcd >> 1) + (jj >> 2);
  gu64* flags = (gu64*)(ws + WS_FLAG);
  unsigned long long epoch;
  auto read_epoch = [&]() {
    asm volatile("s_load_dwordx2 %0, %1, 0x0\n\ts_waitcnt lgkmcnt(0)" : "=s"(epoch) : "s"((const char*)(ws + WS_FLAG) + (512 + b) * 128) : "memory");
  };

  const __amdgpu_buffer_rsrc_t rwo = __builtin_amdgcn_make_buffer_rsrc((void*)wo, (short)0, NH * DH * DM * 2, 0x00020000);
  bf16x8 qf[8];
  f32x16 oacc[4];
  float m, l;
  {
    const unsigned lane = tid & 63, r = lane & 31, h = lane >> 5;
    const unsigned t = w >> 1, j = w & 1;
    const unsigned g1 = (lane >> 4) & 1, qq = (lane & 15) >> 2, pp = lane & 3, sp = ((pp & 1) << 1) | (pp >> 1);
    const __amdgpu_buffer_rsrc_t rk = __builtin_amdgcn_make_buffer_rsrc((void*)kc, (short)0, NB * NH * SK * DH * 2, 0x00020000);
    const __amdgpu_buffer_rsrc_t rv = __builtin_amdgcn_make_buffer_rsrc((void*)vc, (short)0, NB * NH * SK * DH * 2, 0x00020000);
    const unsigned skv = (b * NH + hd) * (SK * DH * 2);
    const unsigned vk = (lane >> 4) * 256 + ((lane & 15) ^ (lane >> 4)) * 16;
    const unsigned vv = (lane >> 4) * 256 + ((lane & 15) ^ ((lane >> 4) << 2)) * 16;

    auto kv4 = [&](int s) {
      const unsigned lw = w - 4;
      const unsigned dst = ldsb + kv_slot(s) + lw * 4096u;
      const unsigned so = skv + ((lw >> 1) & 1) * 65536u + (lw & 1) * 4096u + (unsigned)s * 8192u;
#pragma unroll
      for (int p = 0; p < 4; ++p) {
        if (lw < 4) dma16<1>(rk, vk ^ (p << 6), so + p * 1024u, dst + p * 1024u);
        else dma16<1>(rv, vv, so + p * 1024u, dst + p * 1024u);
      }
    };
    const unsigned koff = 3 * (((jj >> 2) + (jj & 3)) % 3);
    if (w >= 4) {
      const unsigned lw = w - 4;
      const __amdgpu_buffer_rsrc_t rx = __builtin_amdgcn_make_buffer_rsrc((void*)x, (short)0, NB * SQ * DM * 2, 0x00020000);
      const __amdgpu_buffer_rsrc_t rw = __builtin_amdgcn_make_buffer_rsrc((void*)wq, (short)0, DM * NH * DH * 2, 0x00020000);
      const unsigned vx = (lane >> 3) * 1152 + (((lane & 7) ^ (lane >> 4) ^ ((lw & 1) << 2)) * 16);
      const unsigned sx = (b * SQ + 8 * lw) * 1152u;
      const unsigned vw = (lane >> 4) * 8192 + (((lane & 15) ^ ((lane >> 4) << 2)) * 16);
      const unsigned sw = 4 * lw * 8192u + hd * 256u;
      auto issue_q = [&](int i) {
        unsigned st = koff + i; st = st >= 9 ? st - 9 : st;
        const unsigned slot = ldsb + (unsigned)(i % 3) * QS_BYTES;
#pragma unroll
        for (int p = 0; p < 3; ++p) dma16<POL_X>(rx, vx, sx + (unsigned)(64 * p) * 1152u + st * 128u, slot + (lw + 8 * p) * 1024u);
#pragma unroll
        for (int p = 0; p < 2; ++p) dma16<POL_WQ>(rw, vw, sw + (st * 64 + 32 * p) * 8192u, slot + QS_W + (lw + 8 * p) * 1024u);
      };
      issue_q(0); issue_q(1);
      wait_vm<5>(); bar(); issue_q(2);
      wait_vm<5>(); bar(); issue_q(3);
      wait_vm<5>(); bar(); issue_q(4); kv4(0);
      wait_vm<9>(); bar(); issue_q(5);
      wait_vm<9>(); bar(); issue_q(6);
      wait_vm<5>(); bar(); issue_q(7);
      wait_vm<5>(); bar(); issue_q(8);
      wait_vm<5>(); bar(); kv4(1);
      wait_vm<4>(); bar(); kv4(2);
      read_epoch();
      bar();
      bar();
    } else {
      read_epoch();
      if (w < 2) {
        unsigned long long pc;
        asm volatile("s_getpc_b64 %0" : "=s"(pc));
        const unsigned off = (jj * 2 + w) * 1024u;
        if (off + 1024u <= CODE_BYTES) {
          u32x4 t;
          const char* p = (const char*)(pc & ~127ull) + off + lane * 16;
          asm volatile("global_load_dwordx4 %0, %1, off\n\ts_waitcnt vmcnt(0)" : "=v"(t) : "v"(p) : "memory");
        }
      }
      if (w == 2) {
        const unsigned hg_ = hd >> 3, hl_ = hd & 7, wg_ = b * NH + hd;
        const char* p = (const char*)ws + WS_FLAG;
        if (lane == 0) p = (const char*)kc + (size_t)wg_ * (SK * DH * 2);
        if (lane == 1) p = (const char*)vc + (size_t)wg_ * (SK * DH * 2);
        if (lane == 2) p = (const char*)ws + WS_O + (size_t)wg_ * 49152;
        if (lane == 3) p = (const char*)ws + WS_O + (size_t)(b * NH + 8 * hg_) * 49152;
        if (lane == 4) p = (const char*)ws + WS_O + (size_t)(b * NH + 8 * hg_ + 8) * 49152 - 64;
        if (lane >= 5 && lane < 9) p = (const char*)ws + WS_PART + (size_t)(((b * 8 + hl_) * 4 + (lane - 5)) * 4) * PART_BLK;
        if (lane == 9) p = (const char*)wo + (size_t)hg_ * (1024 * 1152);
        if (lane == 10) p = (const char*)wo + (size_t)(hg_ + 1) * (1024 * 1152) - 64;
        if (lane == 11) p = (const char*)out + (size_t)(b * SQ) * (DM * 2);
        if (lane == 12) p = (const char*)kc + (size_t)(wg_ + 1) * (SK * DH * 2) - 64;
        if (lane == 13) p = (const char*)vc + (size_t)(wg_ + 1) * (SK * DH * 2) - 64;
        unsigned t;
        asm volatile("global_load_dword %0, %1, off\n\ts_waitcnt vmcnt(0)" : "=v"(t) : "v"(p) : "memory");
      }
      const unsigned qh = w >> 1, dh = w & 1;
      const unsigned xa0 = (96 * qh + r) * 128 + ((h ^ ((r >> 1) & 7)) << 4);
      const unsigned wa0 = QS_W + (8 * h + qq) * 256 + ((4 * ((2 * dh) ^ qq) + 2 * g1 + (sp >> 1)) << 4) + (sp & 1) * 8;
      f32x16 qacc[3][2];
#pragma unroll
      for (int a = 0; a < 3; ++a)
#pragma unroll
        for (int i = 0; i < 16; ++i) { qacc[a][0][i] = 0.f; qacc[a][1][i] = 0.f; }
      bf16x8 bA[3], aA[2], bB[3], aB[2];
      unsigned xb, wb;
      auto rd = [&](bf16x8 (&bb)[3], bf16x8 (&aa)[2], int s) {
        const unsigned xs = xb ^ (s << 5), wb1 = wb ^ 64;
        aa[0] = lds_tr2(lds, wb + s * 4096, wb + s * 4096 + 1024);
        bb[0] = lds_rd128(lds, xs); bb[1] = lds_rd128(lds, xs + 4096); bb[2] = lds_rd128(lds, xs + 8192);
        aa[1] = lds_tr2(lds, wb1 + s * 4096, wb1 + s * 4096 + 1024);
      };
      auto mm = [&](bf16x8 (&bb)[3], bf16x8 (&aa)[2]) {
        qacc[0][0] = MFMA32(aa[0], bb[0], qacc[0][0]);
        qacc[1][0] = MFMA32(aa[0], bb[1], qacc[1][0]);
        qacc[2][0] = MFMA32(aa[0], bb[2], qacc[2][0]);
        qacc[0][1] = MFMA32(aa[1], bb[0], qacc[0][1]);
        qacc[1][1] = MFMA32(aa[1], bb[1], qacc[1][1]);
        qacc[2][1] = MFMA32(aa[1], bb[2], qacc[2][1]);
      };
      bar();
      STAMP(1);
      xb = xa0; wb = wa0;
      asm volatile("" : "+v"(xb), "+v"(wb));
      rd(bA, aA, 0);
      auto mix = [&]() { SG_MFMA(1); SG_DSRD(2); SG_MFMA(1); SG_DSRD(2); SG_MFMA(1); SG_DSRD(1); SG_MFMA(1); SG_DSRD(1); SG_MFMA(1); SG_DSRD(1); SG_MFMA(1); SB(); };
#pragma unroll 1
      for (int st = 0; st < 8; ++st) {
        SB(); rd(bB, aB, 1); mm(bA, aA); mix();
        rd(bA, aA, 2); mm(bB, aB); mix();
        rd(bB, aB, 3); mm(bA, aA); mix();
        bar();
        const unsigned so = (unsigned)((st + 1) % 3) * QS_BYTES;
        xb = xa0 + so; wb = wa0 + so;
        asm volatile("" : "+v"(xb), "+v"(wb));
        rd(bA, aA, 0); mm(bB, aB); mix();
      }
      SB(); rd(bB, aB, 1); mm(bA, aA); mix();
      rd(bA, aA, 2); mm(bB, aB); mix();
      rd(bB, aB, 3); mm(bA, aA); mix();
      mm(bB, aB);
      bar();
      STAMP(4);
#pragma unroll
      for (int a = 0; a < 3; ++a)
#pragma unroll
        for (int d = 0; d < 2; ++d) {
#pragma unroll
          for (int i = 0; i < 16; ++i) qacc[a][d][i] *= QSCALE;
#pragma unroll
          for (int s2 = 0; s2 < 2; ++s2) {
            const unsigned blk = (3 * qh + a) * 8 + 4 * dh + 2 * d + s2;
            *(lds_bf16x8*)(lds + qblk(blk) + lane * 16) = pack8(qacc[a][d], s2);
          }
        }
      bar();
    }
#pragma unroll
    for (int i = 0; i < 8; ++i) qf[i] = lds_rd128(lds, qblk(t * 8 + i) + lane * 16);

    const unsigned grp = w >> 2;
    float l0, l1;
    bool safe = false;
    const unsigned ka0 = j * 8192 + r * 256 + ((h ^ (r & 15)) << 4);
    const unsigned va0 = (16384 + j * 8192 + (4 * h + qq) * 256 + ((4 * qq + 2 * g1 + (sp >> 1)) << 4) + (sp & 1) * 8) ^ (j << 7);
    const unsigned vkx = vk ^ ((w & 3) << 6);
    const unsigned sp0 = skv + ((w >> 3) & 1) * 65536u + (w & 7) * 1024u;
    const unsigned sp1 = skv + (((w + 12) >> 3) & 1) * 65536u + ((w + 12) & 7) * 1024u;
    const unsigned sp2 = skv + (((w + 24) >> 3) & 1) * 65536u + ((w + 24) & 7) * 1024u;

    bf16x8 kf[4];
    auto step = [&](auto G_, int s) {
      constexpr int G = decltype(G_)::value;
      constexpr int NP = G == 2 ? 2 : 3;
      unsigned vb = va0 + kv_slot(s), kc0 = ka0 + kv_slot(s), kb = ka0 + kv_slot(s + 1);
      asm volatile("" : "+v"(kb), "+v"(vb), "+v"(kc0));
      __builtin_amdgcn_s_setprio(1);
      f32x16 sc;
      bf16x8 vf[4][2];
      {
        bf16x8 kg[4];
#pragma unroll
        for (int ks = 0; ks < 4; ++ks) kg[ks] = lds_rd128(lds, kc0 ^ ((ks + 4) << 5));
#pragma unroll
        for (int i = 0; i < 16; ++i) sc[i] = 0.f;
#pragma unroll
        for (int ks = 0; ks < 4; ++ks) sc = MFMA32(kf[ks], qf[ks], sc);
#pragma unroll
        for (int d = 0; d < 4; ++d) { const unsigned va = vb ^ (d << 6); vf[d][0] = lds_tr2(lds, va, va + 2048); }
#pragma unroll
        for (int ks = 0; ks < 4; ++ks) sc = MFMA32(kg[ks], qf[ks + 4], sc);
        SG_MFMA(1); SG_DSRD(2); SG_MFMA(1); SG_DSRD(2); SG_MFMA(1); SG_DSRD(2); SG_MFMA(1); SG_DSRD(2);
        SG_MFMA(1); SG_DSRD(2); SG_MFMA(1); SG_DSRD(2); SG_MFMA(2);
        SB();
        asm volatile("" : "+v"(sc));
      }
      __builtin_amdgcn_s_setprio(0);
      if constexpr (G == 1) wait_vm<NP>();
      bar();
#pragma unroll
      for (int d = 0; d < 4; ++d) { const unsigned va = vb ^ (d << 6); vf[d][1] = lds_tr2(lds, va + 4096, va + 4096 + 2048); }
      SB();
      if (safe) {
        const float t0 = fmaxf(fmaxf(sc[0], sc[1]), sc[2]), t1 = fmaxf(fmaxf(sc[3], sc[4]), sc[5]), t2 = fmaxf(fmaxf(sc[6], sc[7]), sc[8]);
        const float t3 = fmaxf(fmaxf(sc[9], sc[10]), sc[11]), t4 = fmaxf(fmaxf(sc[12], sc[13]), sc[14]);
        float tm = fmaxf(fmaxf(fmaxf(t0, t1), t2), fmaxf(fmaxf(t3, t4), sc[15]));
        tm = pl32_max(tm);
        if (__any(tm > m + THR)) {
          const float mn = fmaxf(m, tm);
          const float al = __builtin_amdgcn_exp2f(m - mn);
          l0 *= al; l1 *= al; m = mn;
#pragma unroll
          for (int d = 0; d < 4; ++d)
#pragma unroll
            for (int i = 0; i < 16; ++i) oacc[d][i] *= al;
        }
#pragma unroll
        for (int i = 0; i < 16; ++i) sc[i] -= m;
      }
      SB();
#pragma unroll
      for (int i = 0; i < 16; ++i) sc[i] = __builtin_amdgcn_exp2f(sc[i]);
      bf16x8 p0 = pack8(sc, 0), p1 = pack8(sc, 1);
#pragma unroll
      for (int i = 0; i < 16; i += 2) { l0 += sc[i]; l1 += sc[i + 1]; }
      asm volatile("" : "+v"(p0), "+v"(p1), "+v"(l0), "+v"(l1));
      if constexpr (G == 0) wait_vm<NP>();
      bar();
      __builtin_amdgcn_s_setprio(1);
#pragma unroll
      for (int d = 0; d < 4; ++d) oacc[d] = MFMA32(vf[d][0], p0, oacc[d]);
#pragma unroll
      for (int ks = 0; ks < 4; ++ks) kf[ks] = lds_rd128(lds, kb ^ (ks << 5));
      SG_MFMA(2); SG_DSRD(2); SG_MFMA(1); SG_DSRD(1); SG_MFMA(1); SG_DSRD(1);
      SB();
      if (s <= 4) {
        const unsigned dst = ldsb + kv_slot(s + 3) + w * 1024u, sofs = (unsigned)(s + 3) * 8192u;
        dma16<1>(rk, vkx, sp0 + sofs, dst);
        if constexpr (G == 0) dma16<1>(rk, vkx, sp1 + sofs, dst + 12288u); else dma16<1>(rv, vv, sp1 + sofs, dst + 12288u);
        if constexpr (G != 2) dma16<1>(rv, vv, sp2 + sofs, dst + 24576u);
      } else if (s == 5) {
        const unsigned pc = ((b & 3) * 8 + (hd & 7)) * 36 + (G == 2 ? 24 + 2 * (w - 8) : 3 * w);
        const unsigned so = (hd >> 3) * (1024u * 1152u) + pc * 1024u, vo = lane_id() * 16;
#pragma unroll
        for (int p = 0; p < NP; ++p) dma16<POL_WO>(rwo, vo, so + p * 1024u, ldsb + SPARE + (w & 7) * 1024u);
      }
      SB();
#pragma unroll
      for (int d = 0; d < 4; ++d) oacc[d] = MFMA32(vf[d][1], p1, oacc[d]);
      SB();
      asm volatile("" : "+v"(oacc[0]), "+v"(oacc[1]), "+v"(oacc[2]), "+v"(oacc[3]));
      __builtin_amdgcn_s_setprio(0);
      if constexpr (G == 2) wait_vm<NP>();
      bar();
    };
    auto run = [&](auto G_) {
      constexpr int G = decltype(G_)::value;
      {
        unsigned kb0 = ka0 + kv_slot(0);
        asm volatile("" : "+v"(kb0));
#pragma unroll
        for (int ks = 0; ks < 4; ++ks) kf[ks] = lds_rd128(lds, kb0 ^ (ks << 5));
      }
      if constexpr (G >= 1) bar();
      if constexpr (G == 2) { wait_vm<4>(); bar(); }
#pragma unroll 1
      for (int s = 0; s < 8; ++s) step(G_, s);
      if constexpr (G == 0) {
        const unsigned pc = ((b & 3) * 8 + (hd & 7)) * 36 + 32 + w;
        dma16<POL_WO>(rwo, lane_id() * 16, (hd >> 3) * (1024u * 1152u) + pc * 1024u, ldsb + SPARE + w * 1024u);
      }
      if constexpr (G == 0) bar();
      if constexpr (G <= 1) bar();
    };
#pragma unroll 1
    for (;;) {
#pragma unroll
      for (int d = 0; d < 4; ++d)
#pragma unroll
        for (int i = 0; i < 16; ++i) oacc[d][i] = 0.f;
      m = safe ? -__builtin_inff() : 0.f; l0 = 0.f; l1 = 0.f;
      bar(); STAMP(5);
      if (grp == 0) run(IC<0>{});
      else if (grp == 1) run(IC<1>{});
      else run(IC<2>{});
      STAMP(13);
      const unsigned ln = lane_id();
      l = pl32_sum(l0 + l1);
      const unsigned xw = w * 4096 + ln * 16;
#pragma unroll
      for (int i = 0; i < 4; ++i) *(lds_bf16x8*)(lds + xw + i * 1024) = pack8(oacc[2 + (i >> 1)], i & 1);
      if (ln < 32) { f32x2 ml = {m, l}; *(lds_f32x2*)(lds + ML_OFF + w * 256 + ln * 8) = ml; }
      const bool bad = __any(!(l > L_MIN && l < L_MAX));
      if (ln == 0) *(__attribute__((address_space(3))) unsigned*)(lds + VOTE_OFF + w * 4) = bad ? 1u : 0u;
      bar();
      STAMP(6);
      if (safe) break;
      const unsigned vote = *(__attribute__((address_space(3))) unsigned*)(lds + VOTE_OFF + (ln < 12 ? ln : 0) * 4);
      if (!__any(vote != 0)) break;
      safe = true;
      bar();
      if (w >= 4) {
        const unsigned lw = rfl(w - 4), sk = rfl((b * NH + hd) * (SK * DH * 2));
        const unsigned vo = (ln >> 4) * 256 + (lw < 4 ? ((ln & 15) ^ (ln >> 4)) : ((ln & 15) ^ ((ln >> 4) << 2))) * 16;
        const __amdgpu_buffer_rsrc_t rr = __builtin_amdgcn_make_buffer_rsrc((void*)(lw < 4 ? kc : vc), (short)0, NB * NH * SK * DH * 2, 0x00020000);
#pragma unroll 1
        for (unsigned q = 0; q < 12; ++q) {
          const unsigned st = q >> 2, p = q & 3;
          dma16<1>(rr, lw < 4 ? vo ^ (p << 6) : vo, rfl(sk + ((lw >> 1) & 1) * 65536u + (lw & 1) * 4096u + st * 8192u + p * 1024u), rfl(ldsb + kv_slot(st) + lw * 4096u + p * 1024u));
        }
        wait_vm<0>();
      }
    }
  }

  const unsigned lane = lane_id();
  const unsigned hg = hd >> 3, hl = hd & 7, rh = hl & 1, cq = hl >> 1;
  const unsigned long long tag = epoch + 1;
  const unsigned wg = b * NH + hd;
  auto wo_voff = [&](int i) {
    const unsigned n = 64 * i + lane, p = n / 18, ch = n - 18 * p;
    const unsigned k = (p & 19) | ((p & 4) << 1) | ((p & 8) >> 1);
    return k * 1152 + ch * 16;
  };
  auto wo_issue = [&](const unsigned (&vo)[9], unsigned i) {
    const unsigned kh = w & 1, hh = (hl + (i >> 1)) & 7;
    const unsigned so = ((8 * hg + hh) * 128 + (2 * (i & 1) + kh) * 32) * 1152u + cq * 288u;
    const unsigned dst = ldsb + p2_wslot(i) + kh * 9216u;
#pragma unroll
    for (int p = 0; p < 9; ++p) dma16<POL_WO>(rwo, vo[p], so, dst + p * 1024u);
  };
  unsigned wvo[9];
  if (w == 8 || w == 9) {
#pragma unroll
    for (int p = 0; p < 9; ++p) wvo[p] = wo_voff(p);
  }

  {
    const unsigned r = lane & 31, h = lane >> 5, t = w >> 1, j = w & 1;
    f32x2 o = *(lds_f32x2*)(lds + ML_OFF + (w ^ 1) * 256 + r * 8);
    const float mn = fmaxf(m, o[0]);
    const float f = __builtin_amdgcn_exp2f(m - mn), fo = __builtin_amdgcn_exp2f(o[0] - mn);
    const float inv = 1.0f / (l * f + o[1] * fo);
    const float sa = f * inv, sb = fo * inv;
    const unsigned xr = (w ^ 1) * 4096 + lane * 16;
#pragma unroll
    for (int i = 0; i < 4; ++i) {
      const u32x4 v = __builtin_bit_cast(u32x4, lds_rd128(lds, xr + i * 1024));
#pragma unroll
      for (int e = 0; e < 4; ++e) {
        oacc[i >> 1][8 * (i & 1) + 2 * e] = oacc[i >> 1][8 * (i & 1) + 2 * e] * sa + __uint_as_float(v[e] << 16) * sb;
        oacc[i >> 1][8 * (i & 1) + 2 * e + 1] = oacc[i >> 1][8 * (i & 1) + 2 * e + 1] * sa + __uint_as_float(v[e] & 0xffff0000u) * sb;
      }
    }
    const __amdgpu_buffer_rsrc_t ro = __builtin_amdgcn_make_buffer_rsrc((void*)(ws + WS_O), (short)0, O_BYTES, 0x00020000);
    const unsigned so = ((wg * 6 + t) * 8 + 4 * j) * 1024u;
#pragma unroll
    for (int i = 0; i < 4; ++i)
      __builtin_amdgcn_raw_buffer_store_b128(__builtin_bit_cast(u32x4, pack8(oacc[i >> 1], i & 1)), ro, so + lane * 16 + i * 1024, 0, O_ST_AUX);
    STAMP(7);
    if (w == 8 || w == 9) { wo_issue(wvo, 0); wo_issue(wvo, 1); wait_vm<18>(); } else wait_vm<0>();
    STAMP(8);
    bar();
    if (tid == 0) __hip_atomic_store(flags + (size_t)wg * 16, tag, __ATOMIC_RELAXED, __HIP_MEMORY_SCOPE_AGENT);
  }
  STAMP(14);

  f32x4 acc[3][5];
#pragma unroll
  for (int a = 0; a < 3; ++a)
#pragma unroll
    for (int n = 0; n < 5; ++n) acc[a][n] = f32x4{0.f, 0.f, 0.f, 0.f};
  const unsigned nh = (w >> 2) & 1, kh = w & 1, qh = (w >> 1) & 1;
  if (w >= 10) {
    const __amdgpu_buffer_rsrc_t ro = __builtin_amdgcn_make_buffer_rsrc((void*)(ws + WS_O), (short)0, O_BYTES, 0x00020000);
    auto o_issue = [&](unsigned i) {
      const unsigned hh = (hl + (i >> 1)) & 7;
      const unsigned so = (((b * NH + 8 * hg + hh) * 6 + 3 * rh) * 8 + 2 * (2 * (i & 1) + kh)) * 1024u;
      const unsigned dst = ldsb + p2_oslot(i) + kh * 6144u;
#pragma unroll
      for (int q = 0; q < 3; ++q) { dma16<POL_O>(ro, lane * 16, so + q * 8192u, dst + q * 2048u); dma16<POL_O>(ro, lane * 16, so + q * 8192u + 1024u, dst + q * 2048u + 1024u); }
    };
    gu64* fp = flags + (size_t)(b * NH + 8 * hg + (lane & 7)) * 16;
    unsigned long long pv;
    auto poll_issue = [&]() { asm volatile("global_load_dwordx2 %0, %1, off sc1" : "=v"(pv) : "v"(fp) : "memory"); };
    unsigned rdy = 1u << hl;
    auto poll_take = [&]() {
      asm volatile("s_waitcnt vmcnt(0)" : "+v"(pv) :: "memory");
      rdy |= (unsigned)__ballot(pv == tag) & 0xffu;
    };
    o_issue(0); o_issue(1);
    poll_issue();
    STAMPW(10, 22);
    bool pending = true;
    unsigned spins = 0;
#pragma unroll 1
    for (unsigned i = 0; i < 16; ++i) {
      if (pending) wait_vm<7>(); else if (i <= 14) wait_vm<6>(); else wait_vm<0>();
      if (i == 0) STAMPW(10, 23);
      bar();
      if (i + 2 < 16) {
        const unsigned hh = (hl + ((i + 2) >> 1)) & 7;
        if (pending) { poll_take(); pending = false; }
        while (!((rdy >> hh) & 1) && spins < 400000u) { __builtin_amdgcn_s_sleep(1); poll_issue(); poll_take(); ++spins; }
        o_issue(i + 2);
      }
    }
  } else if (w >= 8) {
    wo_issue(wvo, 2); wo_issue(wvo, 3); wo_issue(wvo, 4);
#pragma unroll 1
    for (unsigned i = 0; i < 16; ++i) {
      if (i <= 11) wait_vm<36>(); else if (i == 12) wait_vm<27>(); else if (i == 13) wait_vm<18>(); else if (i == 14) wait_vm<9>(); else wait_vm<0>();
      bar();
      if (i + 5 < 16) wo_issue(wvo, i + 5);
    }
  } else {
    const unsigned c = lane & 15, g = lane >> 4;
    unsigned ba[3];
#pragma unroll
    for (int a = 0; a < 3; ++a) {
      const unsigned qi = 3 * qh + a;
      ba[a] = kh * 6144 + ((qi >> 1) * 2 + (g >> 1)) * 1024 + (16 * (qi & 1) + c + 32 * (g & 1)) * 16;
    }
    const unsigned aa = kh * 9216 + ((g >> 1) * 16 + (g & 1) * 4 + (c >> 2)) * 288 + 5 * nh * 32 + (c & 3) * 8;
    bf16x8 bX[3], aX[5], bY[3], aY[5];
    auto rd = [&](auto NT_, bf16x8 (&bb)[3], bf16x8 (&av)[5], unsigned i) {
      constexpr int NT = decltype(NT_)::value;
      unsigned ob = p2_oslot(i), wb = aa + p2_wslot(i);
      asm volatile("" : "+v"(wb));
#pragma unroll
      for (int a = 0; a < 3; ++a) bb[a] = lds_rd128(lds, ba[a] + ob);
#pragma unroll
      for (int n = 0; n < NT; ++n) av[n] = lds_tr2(lds, wb + n * 32, wb + n * 32 + 2304);
    };
    auto mm = [&](auto NT_, bf16x8 (&bb)[3], bf16x8 (&av)[5]) {
      constexpr int NT = decltype(NT_)::value;
#pragma unroll
      for (int n = 0; n < NT; ++n)
#pragma unroll
        for (int a = 0; a < 3; ++a) acc[a][n] = MFMA16(av[n], bb[a], acc[a][n]);
    };
    unsigned long long t0 = 0;
    auto mix = [&](auto NT_) {
      constexpr int NT = decltype(NT_)::value;
#pragma unroll
      for (int k = 0; k < 3 + 2 * NT - (NT == 4 ? 0 : 0); ++k) {
        __builtin_amdgcn_sched_group_barrier(0x008, 1, 0);
        __builtin_amdgcn_sched_group_barrier(0x100, 1, 0);
      }
      __builtin_amdgcn_sched_group_barrier(0x008, 3 * NT, 0);
    };
    auto gemm = [&](auto NT_) {
      bar();
      STAMP(15);
      rd(NT_, bX, aX, 0);
#pragma unroll 1
      for (unsigned i = 1; i < 15; i += 2) {
        bar(); rd(NT_, bY, aY, i); mm(NT_, bX, aX); mix(NT_);
        bar(); rd(NT_, bX, aX, i + 1); mm(NT_, bY, aY); mix(NT_);
      }
      bar(); rd(NT_, bY, aY, 15); mm(NT_, bX, aX); mix(NT_);
      SB(); mm(NT_, bY, aY);
    };
    if (nh == 0) gemm(IC<5>{}); else gemm(IC<4>{});
    WREC(44, TNOW() - t0);
  }
  STAMP(16);
  bar();
  if (w < 8) {
    const unsigned c = lane & 15, g = lane >> 4;
    const unsigned base = kh * IMG_BYTES + (48 * qh + c) * IMG_STRIDE + (80 * nh + 4 * g) * 4;
#pragma unroll
    for (int a = 0; a < 3; ++a)
#pragma unroll
      for (int n = 0; n < 5; ++n)
        if (n < 4 || nh == 0) *(lds_f32x4*)(lds + base + a * 16 * IMG_STRIDE + n * 64) = acc[a][n];
  }
  bar();
  {
    const __amdgpu_buffer_rsrc_t rp = __builtin_amdgcn_make_buffer_rsrc((void*)(ws + WS_PART), (short)0, PART_BYTES, 0x00020000);
    const unsigned pbase = ((b * 8 + hl) * 4 + hg) * 4 * PART_BLK;
#pragma unroll 1
    for (unsigned n = tid; n < 72 * 18; n += 768) {
      const unsigned rr = n / 18, ch = n - rr * 18;
      unsigned dst = rr / 24; const unsigned row = rr - dst * 24;
      dst += dst >= hg ? 1 : 0;
      const unsigned a0 = (24 * dst + row) * IMG_STRIDE + ch * 32;
      const f32x4 v0 = *(lds_f32x4*)(lds + a0) + *(lds_f32x4*)(lds + IMG_BYTES + a0);
      const f32x4 v1 = *(lds_f32x4*)(lds + a0 + 16) + *(lds_f32x4*)(lds + IMG_BYTES + a0 + 16);
      const u32x4 pk = {cvtpk(v0[0], v0[1]), cvtpk(v0[2], v0[3]), cvtpk(v1[0], v1[1]), cvtpk(v1[2], v1[3])};
      __builtin_amdgcn_raw_buffer_store_b128(pk, rp, dst * PART_BLK + row * 288 + ch * 16 + pbase, 0, 16);
    }
    wait_vm<0>();
    bar();
    if (tid == 0) __hip_atomic_store(flags + (size_t)(256 + wg) * 16, tag, __ATOMIC_RELAXED, __HIP_MEMORY_SCOPE_AGENT);
    STAMP(17);
    if (w == 0) poll_flags(flags, 256 + b * NH + hl, 8, 4, hg, tag, lane);
    bar();
    STAMP(18);
    if (tid < 24 * 18) {
      const unsigned row = tid / 18, ch = tid - row * 18;
      u32x4 u[3];
#pragma unroll
      for (int k = 0; k < 3; ++k) {
        const unsigned src = k + (k >= (int)hg ? 1 : 0);
        u[k] = __builtin_amdgcn_raw_buffer_load_b128(rp, (((b * 8 + hl) * 4 + src) * 4 + hg) * PART_BLK + row * 288 + ch * 16, 0, 16);
      }
      const unsigned a0 = (24 * hg + row) * IMG_STRIDE + ch * 32;
      f32x4 v0 = *(lds_f32x4*)(lds + a0) + *(lds_f32x4*)(lds + IMG_BYTES + a0);
      f32x4 v1 = *(lds_f32x4*)(lds + a0 + 16) + *(lds_f32x4*)(lds + IMG_BYTES + a0 + 16);
#pragma unroll
      for (int k = 0; k < 3; ++k) {
        v0[0] += __uint_as_float(u[k][0] << 16); v0[1] += __uint_as_float(u[k][0] & 0xffff0000u);
        v0[2] += __uint_as_float(u[k][1] << 16); v0[3] += __uint_as_float(u[k][1] & 0xffff0000u);
        v1[0] += __uint_as_float(u[k][2] << 16); v1[1] += __uint_as_float(u[k][2] & 0xffff0000u);
        v1[2] += __uint_as_float(u[k][3] << 16); v1[3] += __uint_as_float(u[k][3] & 0xffff0000u);
      }
      const u32x4 o = {cvtpk(v0[0], v0[1]), cvtpk(v0[2], v0[3]), cvtpk(v1[0], v1[1]), cvtpk(v1[2], v1[3])};
      const __amdgpu_buffer_rsrc_t rout = __builtin_amdgcn_make_buffer_rsrc((void*)out, (short)0, NB * SQ * DM * 2, 0x00020000);
      __builtin_amdgcn_raw_buffer_store_b128(o, rout, ((b * SQ + 96 * rh + 24 * hg + row) * DM + 144 * cq + 8 * ch) * 2, 0, OUT_AUX);
    }
    if (tid == 0 && hd == 0) __hip_atomic_store(flags + (size_t)(512 + b) * 16, tag, __ATOMIC_RELAXED, __HIP_MEMORY_SCOPE_AGENT);
  }
}

extern "C" void kernel_launch(void* const* d_in, const int* in_sizes, int n_in, void* d_out, int out_size, void* d_ws, size_t ws_size,
                              hipStream_t stream) {
  const __hip_bfloat16* x = (const __hip_bfloat16*)d_in[0];
  const __hip_bfloat16* kc = (const __hip_bfloat16*)d_in[1];
  const __hip_bfloat16* vc = (const __hip_bfloat16*)d_in[2];
  const __hip_bfloat16* wq = (const __hip_bfloat16*)d_in[3];
  const __hip_bfloat16* wo = (const __hip_bfloat16*)d_in[4];
  unsigned long long* diag = (unsigned long long*)((char*)d_ws + (64u << 20));
  mha_kernel<<<256, 768, 0, stream>>>(x, kc, vc, wq, wo, (__hip_bfloat16*)d_out, (char*)d_ws, diag);
}
```

```cpp
#include <hip/hip_runtime.h>
#include <hip/hip_bf16.h>
#include <type_traits>

typedef __bf16 bf16x8 __attribute__((ext_vector_type(8)));
typedef __bf16 bf16x4 __attribute__((ext_vector_type(4)));
typedef __bf16 bf16x2 __attribute__((ext_vector_type(2)));
typedef float f32x16 __attribute__((ext_vector_type(16)));
typedef float f32x4 __attribute__((ext_vector_type(4)));
typedef float f32x2 __attribute__((ext_vector_type(2)));
typedef unsigned u32x4 __attribute__((ext_vector_type(4)));
typedef unsigned u32x2 __attribute__((ext_vector_type(2)));
typedef __attribute__((address_space(3))) char lds_char;
typedef __attribute__((address_space(3))) bf16x4 lds_bf16x4;
typedef __attribute__((address_space(3))) bf16x8 lds_bf16x8;
typedef __attribute__((address_space(3))) f32x4 lds_f32x4;
typedef __attribute__((address_space(3))) f32x2 lds_f32x2;
typedef __attribute__((address_space(1))) unsigned long long gu64;

#define DI __device__ __forceinline__
#define MFMA32(a, b, c) __builtin_amdgcn_mfma_f32_32x32x16_bf16((a), (b), (c), 0, 0, 0)
#define MFMA16(a, b, c) __builtin_amdgcn_mfma_f32_16x16x32_bf16((a), (b), (c), 0, 0, 0)
#define SB() __builtin_amdgcn_sched_barrier(0)
template <int V> using IC = std::integral_constant<int, V>;
#define SG_MFMA(n) __builtin_amdgcn_sched_group_barrier(0x008, (n), 0)
#define SG_DSRD(n) __builtin_amdgcn_sched_group_barrier(0x100, (n), 0)

#define STAMPW(wv, i) do {} while (0)
#define STAMP(i) do {} while (0)
#define WREC(i, v) do {} while (0)
#define TNOW() 0ull

constexpr int NB = 8, SQ = 192, DM = 576, NH = 32, SK = 512, DH = 128;
constexpr float QSCALE = 0.08838834764831845f * 1.4426950408889634f;
constexpr float THR = 8.0f;

constexpr unsigned CODE_BYTES = 26 * 1024;
constexpr size_t WS_O = 0;
constexpr size_t WS_PART = 16u << 20;
constexpr size_t WS_FLAG = 32u << 20;
constexpr unsigned PART_BLK = 24 * 144 * 2;
constexpr unsigned O_BYTES = NB * NH * 6 * 8 * 1024, PART_BYTES = 8 * 8 * 4 * 4 * PART_BLK;

constexpr unsigned QS_BYTES = 40960, QS_W = 24576;
constexpr unsigned SPARE = 155648, LDS_TOTAL = 163840, ML_OFF = 98304, VOTE_OFF = 101376;
constexpr float L_MIN = 9.313225746154785e-10f, L_MAX = 1048576.0f;
DI constexpr unsigned kv_slot(int s) { return (unsigned)((s + 3) & 3) * QS_BYTES; }
DI unsigned qblk(unsigned blk) { return blk < 40 ? 2 * QS_BYTES + blk * 1024 : SPARE + (blk - 40) * 1024; }
constexpr unsigned P2_OSLOT = 12288, P2_WSLOT = 18432, P2_WBASE = 53248, IMG_STRIDE = 304, IMG_BYTES = 96 * IMG_STRIDE;
DI unsigned p2_oslot(unsigned i) { return (i & 3) * P2_OSLOT; }
DI unsigned p2_wslot(unsigned i) { return P2_WBASE + ((i + 3) % 6) * P2_WSLOT; }

DI unsigned rfl(unsigned x) { return __builtin_amdgcn_readfirstlane(x); }
DI unsigned cvtpk(float lo, float hi) { f32x2 v = {lo, hi}; bf16x2 b = __builtin_convertvector(v, bf16x2); return __builtin_bit_cast(unsigned, b); }
DI bf16x8 pack8(const f32x16& x, int s) {
  u32x4 p = {cvtpk(x[8 * s], x[8 * s + 1]), cvtpk(x[8 * s + 2], x[8 * s + 3]), cvtpk(x[8 * s + 4], x[8 * s + 5]), cvtpk(x[8 * s + 6], x[8 * s + 7])};
  return __builtin_bit_cast(bf16x8, p);
}
DI bf16x8 lds_rd128(lds_char* base, unsigned off) { return *(lds_bf16x8*)(base + off); }
DI bf16x8 lds_tr2(lds_char* base, unsigned off0, unsigned off1) {
  bf16x4 lo = __builtin_amdgcn_ds_read_tr16_b64_v4bf16((lds_bf16x4*)(base + off0));
  bf16x4 hi = __builtin_amdgcn_ds_read_tr16_b64_v4bf16((lds_bf16x4*)(base + off1));
  return __builtin_shufflevector(lo, hi, 0, 1, 2, 3, 4, 5, 6, 7);
}
template <int POL> DI void dma16(__amdgpu_buffer_rsrc_t r, unsigned voff, unsigned soff, unsigned m0v) {
  if constexpr (POL == 0) asm volatile("s_mov_b32 m0, %3\n\ts_nop 0\n\tbuffer_load_dwordx4 %0, %1, %2 offen lds" ::"v"(voff), "s"(r), "s"(soff), "s"(m0v) : "memory");
  else if constexpr (POL == 1) asm volatile("s_mov_b32 m0, %3\n\ts_nop 0\n\tbuffer_load_dwordx4 %0, %1, %2 offen nt lds" ::"v"(voff), "s"(r), "s"(soff), "s"(m0v) : "memory");
  else asm volatile("s_mov_b32 m0, %3\n\ts_nop 0\n\tbuffer_load_dwordx4 %0, %1, %2 offen sc1 lds" ::"v"(voff), "s"(r), "s"(soff), "s"(m0v) : "memory");
}
template <int N> DI void wait_vm() { asm volatile("s_waitcnt vmcnt(%0)" ::"i"(N) : "memory"); }
DI void bar() { SB(); asm volatile("s_waitcnt lgkmcnt(0)\n\ts_barrier" ::: "memory"); SB(); }
DI float pl32_max(float v) {
  auto r = __builtin_amdgcn_permlane32_swap(__float_as_uint(v), __float_as_uint(v), false, false);
  return fmaxf(__uint_as_float(r[0]), __uint_as_float(r[1]));
}
DI float pl32_sum(float v) {
  auto r = __builtin_amdgcn_permlane32_swap(__float_as_uint(v), __float_as_uint(v), false, false);
  return __uint_as_float(r[0]) + __uint_as_float(r[1]);
}
DI void add8(float (&v)[8], const u32x4& u) {
#pragma unroll
  for (int e = 0; e < 4; ++e) { v[2 * e] += __uint_as_float(u[e] << 16); v[2 * e + 1] += __uint_as_float(u[e] & 0xffff0000u); }
}
DI unsigned lane_id() { unsigned l; asm volatile("v_mbcnt_lo_u32_b32 %0, -1, 0\n\tv_mbcnt_hi_u32_b32 %0, -1, %0" : "=v"(l)); return l; }
DI void poll_flags(gu64* flags, unsigned idx0, unsigned stride, unsigned n, unsigned skip, unsigned long long tag, unsigned lane) {
  const bool mine = lane < n && lane != skip;
  gu64* p = flags + (size_t)(idx0 + (mine ? lane : 0) * stride) * 16;
  for (unsigned spins = 0; spins < 400000u; ++spins) {
    const unsigned long long v = __hip_atomic_load(p, __ATOMIC_RELAXED, __HIP_MEMORY_SCOPE_AGENT);
    if (__all(!mine || v == tag)) break;
    __builtin_amdgcn_s_sleep(1);
  }
}

#define OUT_AUX 2
#define ROTMODE 2
#define POL_X 0
#define POL_WQ 0
#define O_ST_AUX 16
#define POL_O 2
#define POL_WO 0

__global__ __launch_bounds__(768, 3) void mha_kernel(const __hip_bfloat16* __restrict__ x, const __hip_bfloat16* __restrict__ kc,
                                                     const __hip_bfloat16* __restrict__ vc, const __hip_bfloat16* __restrict__ wq,
                                                     const __hip_bfloat16* __restrict__ wo, __hip_bfloat16* __restrict__ out,
                                                     char* __restrict__ ws, unsigned long long* diag) {
  __shared__ __attribute__((aligned(1024))) char smem[LDS_TOTAL];
  lds_char* lds = (lds_char*)smem;
  const unsigned ldsb = (unsigned)(size_t)smem;
  const unsigned tid = threadIdx.x, w = rfl(tid >> 6);
  STAMP(0);
  const unsigned xcd = blockIdx.x & 7, jj = blockIdx.x >> 3;
  const unsigned b = 4 * (xcd & 1) + (jj & 3), hd = 8 * (xcd >> 1) + (jj >> 2);
  gu64* flags = (gu64*)(ws + WS_FLAG);
  unsigned long long epoch;
  auto read_epoch = [&]() {
    asm volatile("s_load_dwordx2 %0, %1, 0x0\n\ts_waitcnt lgkmcnt(0)" : "=s"(epoch) : "s"((const char*)(ws + WS_FLAG) + (512 + b) * 128) : "memory");
  };

  const __amdgpu_buffer_rsrc_t rwo = __builtin_amdgcn_make_buffer_rsrc((void*)wo, (short)0, NH * DH * DM * 2, 0x00020000);
  bf16x8 qf[8];
  f32x16 oacc[4];
  float m, l;
  {
    const unsigned lane = tid & 63, r = lane & 31, h = lane >> 5;
    const unsigned t = w >> 1, j = w & 1;
    const unsigned g1 = (lane >> 4) & 1, qq = (lane & 15) >> 2, pp = lane & 3, sp = ((pp & 1) << 1) | (pp >> 1);
    const __amdgpu_buffer_rsrc_t rk = __builtin_amdgcn_make_buffer_rsrc((void*)kc, (short)0, NB * NH * SK * DH * 2, 0x00020000);
    const __amdgpu_buffer_rsrc_t rv = __builtin_amdgcn_make_buffer_rsrc((void*)vc, (short)0, NB * NH * SK * DH * 2, 0x00020000);
    const unsigned skv = (b * NH + hd) * (SK * DH * 2);
    const unsigned vk = (lane >> 4) * 256 + ((lane & 15) ^ (lane >> 4)) * 16;
    const unsigned vv = (lane >> 4) * 256 + ((lane & 15) ^ ((lane >> 4) << 2)) * 16;

    auto kv4 = [&](int s) {
      const unsigned lw = w - 4;
      const unsigned dst = ldsb + kv_slot(s) + lw * 4096u;
      const unsigned so = skv + ((lw >> 1) & 1) * 65536u + (lw & 1) * 4096u + (unsigned)s * 8192u;
#pragma unroll
      for (int p = 0; p < 4; ++p) {
        if (lw < 4) dma16<1>(rk, vk ^ (p << 6), so + p * 1024u, dst + p * 1024u);
        else dma16<1>(rv, vv, so + p * 1024u, dst + p * 1024u);
      }
    };
    const unsigned koff = 3 * (((jj >> 2) + (jj & 3)) % 3);
    if (w >= 4) {
      const unsigned lw = w - 4;
      const __amdgpu_buffer_rsrc_t rx = __builtin_amdgcn_make_buffer_rsrc((void*)x, (short)0, NB * SQ * DM * 2, 0x00020000);
      const __amdgpu_buffer_rsrc_t rw = __builtin_amdgcn_make_buffer_rsrc((void*)wq, (short)0, DM * NH * DH * 2, 0x00020000);
      const unsigned vx = (lane >> 3) * 1152 + (((lane & 7) ^ (lane >> 4) ^ ((lw & 1) << 2)) * 16);
      const unsigned sx = (b * SQ + 8 * lw) * 1152u;
      const unsigned vw = (lane >> 4) * 8192 + (((lane & 15) ^ ((lane >> 4) << 2)) * 16);
      const unsigned sw = 4 * lw * 8192u + hd * 256u;
      auto issue_q = [&](int i) {
        unsigned st = koff + i; st = st >= 9 ? st - 9 : st;
        const unsigned slot = ldsb + (unsigned)(i % 3) * QS_BYTES;
#pragma unroll
        for (int p = 0; p < 3; ++p) dma16<POL_X>(rx, vx, sx + (unsigned)(64 * p) * 1152u + st * 128u, slot + (lw + 8 * p) * 1024u);
#pragma unroll
        for (int p = 0; p < 2; ++p) dma16<POL_WQ>(rw, vw, sw + (st * 64 + 32 * p) * 8192u, slot + QS_W + (lw + 8 * p) * 1024u);
      };
      issue_q(0); issue_q(1);
      wait_vm<5>(); bar(); issue_q(2);
      wait_vm<5>(); bar(); issue_q(3);
      wait_vm<5>(); bar(); issue_q(4); kv4(0);
      wait_vm<9>(); bar(); issue_q(5);
      wait_vm<9>(); bar(); issue_q(6);
      wait_vm<5>(); bar(); issue_q(7);
      wait_vm<5>(); bar(); issue_q(8);
      wait_vm<5>(); bar(); kv4(1);
      wait_vm<4>(); bar(); kv4(2);
      read_epoch();
      bar();
      bar();
    } else {
      read_epoch();
      if (w < 2) {
        unsigned long long pc;
        asm volatile("s_getpc_b64 %0" : "=s"(pc));
        const unsigned off = (jj * 2 + w) * 1024u;
        if (off + 1024u <= CODE_BYTES) {
          u32x4 t;
          const char* p = (const char*)(pc & ~127ull) + off + lane * 16;
          asm volatile("global_load_dwordx4 %0, %1, off\n\ts_waitcnt vmcnt(0)" : "=v"(t) : "v"(p) : "memory");
        }
      }
      if (w == 2) {
        const unsigned hg_ = hd >> 3, hl_ = hd & 7, wg_ = b * NH + hd;
        const char* p = (const char*)ws + WS_FLAG;
        if (lane == 0) p = (const char*)kc + (size_t)wg_ * (SK * DH * 2);
        if (lane == 1) p = (const char*)vc + (size_t)wg_ * (SK * DH * 2);
        if (lane == 2) p = (const char*)ws + WS_O + (size_t)wg_ * 49152;
        if (lane == 3) p = (const char*)ws + WS_O + (size_t)(b * NH + 8 * hg_) * 49152;
        if (lane == 4) p = (const char*)ws + WS_O + (size_t)(b * NH + 8 * hg_ + 8) * 49152 - 64;
        if (lane >= 5 && lane < 9) p = (const char*)ws + WS_PART + (size_t)(((b * 8 + hl_) * 4 + (lane - 5)) * 4) * PART_BLK;
        if (lane == 9) p = (const char*)wo + (size_t)hg_ * (1024 * 1152);
        if (lane == 10) p = (const char*)wo + (size_t)(hg_ + 1) * (1024 * 1152) - 64;
        if (lane == 11) p = (const char*)out + (size_t)(b * SQ) * (DM * 2);
        if (lane == 12) p = (const char*)kc + (size_t)(wg_ + 1) * (SK * DH * 2) - 64;
        if (lane == 13) p = (const char*)vc + (size_t)(wg_ + 1) * (SK * DH * 2) - 64;
        unsigned t;
        asm volatile("global_load_dword %0, %1, off\n\ts_waitcnt vmcnt(0)" : "=v"(t) : "v"(p) : "memory");
      }
      const unsigned qh = w >> 1, dh = w & 1;
      const unsigned xa0 = (96 * qh + r) * 128 + ((h ^ ((r >> 1) & 7)) << 4);
      const unsigned wa0 = QS_W + (8 * h + qq) * 256 + ((4 * ((2 * dh) ^ qq) + 2 * g1 + (sp >> 1)) << 4) + (sp & 1) * 8;
      f32x16 qacc[3][2];
#pragma unroll
      for (int a = 0; a < 3; ++a)
#pragma unroll
        for (int i = 0; i < 16; ++i) { qacc[a][0][i] = 0.f; qacc[a][1][i] = 0.f; }
      bf16x8 bA[3], aA[2], bB[3], aB[2];
      unsigned xb, wb;
      auto rd = [&](bf16x8 (&bb)[3], bf16x8 (&aa)[2], int s) {
        const unsigned xs = xb ^ (s << 5), wb1 = wb ^ 64;
        aa[0] = lds_tr2(lds, wb + s * 4096, wb + s * 4096 + 1024);
        bb[0] = lds_rd128(lds, xs); bb[1] = lds_rd128(lds, xs + 4096); bb[2] = lds_rd128(lds, xs + 8192);
        aa[1] = lds_tr2(lds, wb1 + s * 4096, wb1 + s * 4096 + 1024);
      };
      auto mm = [&](bf16x8 (&bb)[3], bf16x8 (&aa)[2]) {
        qacc[0][0] = MFMA32(aa[0], bb[0], qacc[0][0]);
        qacc[1][0] = MFMA32(aa[0], bb[1], qacc[1][0]);
        qacc[2][0] = MFMA32(aa[0], bb[2], qacc[2][0]);
        qacc[0][1] = MFMA32(aa[1], bb[0], qacc[0][1]);
        qacc[1][1] = MFMA32(aa[1], bb[1], qacc[1][1]);
        qacc[2][1] = MFMA32(aa[1], bb[2], qacc[2][1]);
      };
      bar();
      STAMP(1);
      xb = xa0; wb = wa0;
      asm volatile("" : "+v"(xb), "+v"(wb));
      rd(bA, aA, 0);
      auto mix = [&]() { SG_MFMA(1); SG_DSRD(2); SG_MFMA(1); SG_DSRD(2); SG_MFMA(1); SG_DSRD(1); SG_MFMA(1); SG_DSRD(1); SG_MFMA(1); SG_DSRD(1); SG_MFMA(1); SB(); };
#pragma unroll 1
      for (int st = 0; st < 8; ++st) {
        SB(); rd(bB, aB, 1); mm(bA, aA); mix();
        rd(bA, aA, 2); mm(bB, aB); mix();
        rd(bB, aB, 3); mm(bA, aA); mix();
        bar();
        const unsigned so = (unsigned)((st + 1) % 3) * QS_BYTES;
        xb = xa0 + so; wb = wa0 + so;
        asm volatile("" : "+v"(xb), "+v"(wb));
        rd(bA, aA, 0); mm(bB, aB); mix();
      }
      SB(); rd(bB, aB, 1); mm(bA, aA); mix();
      rd(bA, aA, 2); mm(bB, aB); mix();
      rd(bB, aB, 3); mm(bA, aA); mix();
      mm(bB, aB);
      bar();
      STAMP(4);
#pragma unroll
      for (int a = 0; a < 3; ++a)
#pragma unroll
        for (int d = 0; d < 2; ++d) {
#pragma unroll
          for (int i = 0; i < 16; ++i) qacc[a][d][i] *= QSCALE;
#pragma unroll
          for (int s2 = 0; s2 < 2; ++s2) {
            const unsigned blk = (3 * qh + a) * 8 + 4 * dh + 2 * d + s2;
            *(lds_bf16x8*)(lds + qblk(blk) + lane * 16) = pack8(qacc[a][d], s2);
          }
        }
      bar();
    }
#pragma unroll
    for (int i = 0; i < 8; ++i) qf[i] = lds_rd128(lds, qblk(t * 8 + i) + lane * 16);

    const unsigned grp = w >> 2;
    float l0, l1;
    bool safe = false;
    const unsigned ka0 = j * 8192 + r * 256 + ((h ^ (r & 15)) << 4);
    const unsigned va0 = (16384 + j * 8192 + (4 * h + qq) * 256 + ((4 * qq + 2 * g1 + (sp >> 1)) << 4) + (sp & 1) * 8) ^ (j << 7);
    const unsigned vkx = vk ^ ((w & 3) << 6);
    const unsigned sp0 = skv + ((w >> 3) & 1) * 65536u + (w & 7) * 1024u;
    const unsigned sp1 = skv + (((w + 12) >> 3) & 1) * 65536u + ((w + 12) & 7) * 1024u;
    const unsigned sp2 = skv + (((w + 24) >> 3) & 1) * 65536u + ((w + 24) & 7) * 1024u;

    bf16x8 kf[4];
    auto step = [&](auto G_, int s) {
      constexpr int G = decltype(G_)::value;
      constexpr int WGP = 2 - G;
      constexpr int NP = WGP == 2 ? 2 : 3;
      unsigned vb = va0 + kv_slot(s), kc0 = ka0 + kv_slot(s), kb = ka0 + kv_slot(s + 1);
      asm volatile("" : "+v"(kb), "+v"(vb), "+v"(kc0));
      __builtin_amdgcn_s_setprio(1);
      f32x16 sc;
      bf16x8 vf[4][2];
      {
        bf16x8 kg[4];
#pragma unroll
        for (int ks = 0; ks < 4; ++ks) kg[ks] = lds_rd128(lds, kc0 ^ ((ks + 4) << 5));
#pragma unroll
        for (int i = 0; i < 16; ++i) sc[i] = 0.f;
#pragma unroll
        for (int ks = 0; ks < 4; ++ks) sc = MFMA32(kf[ks], qf[ks], sc);
#pragma unroll
        for (int d = 0; d < 4; ++d) { const unsigned va = vb ^ (d << 6); vf[d][0] = lds_tr2(lds, va, va + 2048); }
#pragma unroll
        for (int ks = 0; ks < 4; ++ks) sc = MFMA32(kg[ks], qf[ks + 4], sc);
        SG_MFMA(1); SG_DSRD(2); SG_MFMA(1); SG_DSRD(2); SG_MFMA(1); SG_DSRD(2); SG_MFMA(1); SG_DSRD(2);
        SG_MFMA(1); SG_DSRD(2); SG_MFMA(1); SG_DSRD(2); SG_MFMA(2);
        SB();
        asm volatile("" : "+v"(sc));
      }
      __builtin_amdgcn_s_setprio(0);
      if constexpr (G == 1) wait_vm<NP>();
      bar();
#pragma unroll
      for (int d = 0; d < 4; ++d) { const unsigned va = vb ^ (d << 6); vf[d][1] = lds_tr2(lds, va + 4096, va + 4096 + 2048); }
      SB();
      if (safe) {
        const float t0 = fmaxf(fmaxf(sc[0], sc[1]), sc[2]), t1 = fmaxf(fmaxf(sc[3], sc[4]), sc[5]), t2 = fmaxf(fmaxf(sc[6], sc[7]), sc[8]);
        const float t3 = fmaxf(fmaxf(sc[9], sc[10]), sc[11]), t4 = fmaxf(fmaxf(sc[12], sc[13]), sc[14]);
        float tm = fmaxf(fmaxf(fmaxf(t0, t1), t2), fmaxf(fmaxf(t3, t4), sc[15]));
        tm = pl32_max(tm);
        if (__any(tm > m + THR)) {
          const float mn = fmaxf(m, tm);
          const float al = __builtin_amdgcn_exp2f(m - mn);
          l0 *= al; l1 *= al; m = mn;
#pragma unroll
          for (int d = 0; d < 4; ++d)
#pragma unroll
            for (int i = 0; i < 16; ++i) oacc[d][i] *= al;
        }
#pragma unroll
        for (int i = 0; i < 16; ++i) sc[i] -= m;
      }
      SB();
#pragma unroll
      for (int i = 0; i < 16; ++i) sc[i] = __builtin_amdgcn_exp2f(sc[i]);
      bf16x8 p0 = pack8(sc, 0), p1 = pack8(sc, 1);
#pragma unroll
      for (int i = 0; i < 16; i += 2) { l0 += sc[i]; l1 += sc[i + 1]; }
      asm volatile("" : "+v"(p0), "+v"(p1), "+v"(l0), "+v"(l1));
      if constexpr (G == 0) wait_vm<NP>();
      bar();
      __builtin_amdgcn_s_setprio(1);
#pragma unroll
      for (int d = 0; d < 4; ++d) oacc[d] = MFMA32(vf[d][0], p0, oacc[d]);
#pragma unroll
      for (int ks = 0; ks < 4; ++ks) kf[ks] = lds_rd128(lds, kb ^ (ks << 5));
      SG_MFMA(2); SG_DSRD(2); SG_MFMA(1); SG_DSRD(1); SG_MFMA(1); SG_DSRD(1);
      SB();
      if (s <= 4) {
        const unsigned dst = ldsb + kv_slot(s + 3) + w * 1024u, sofs = (unsigned)(s + 3) * 8192u;
        dma16<1>(rk, vkx, sp0 + sofs, dst);
        if constexpr (WGP == 0) dma16<1>(rk, vkx, sp1 + sofs, dst + 12288u); else dma16<1>(rv, vv, sp1 + sofs, dst + 12288u);
        if constexpr (WGP != 2) dma16<1>(rv, vv, sp2 + sofs, dst + 24576u);
      } else if (s == 5) {
        const unsigned pc = ((b & 3) * 8 + (hd & 7)) * 36 + (WGP == 2 ? 24 + 2 * (w - 8) : 3 * w);
        const unsigned so = (hd >> 3) * (1024u * 1152u) + pc * 1024u, vo = lane_id() * 16;
#pragma unroll
        for (int p = 0; p < NP; ++p) dma16<POL_WO>(rwo, vo, so + p * 1024u, ldsb + SPARE + (w & 7) * 1024u);
      }
      SB();
#pragma unroll
      for (int d = 0; d < 4; ++d) oacc[d] = MFMA32(vf[d][1], p1, oacc[d]);
      SB();
      asm volatile("" : "+v"(oacc[0]), "+v"(oacc[1]), "+v"(oacc[2]), "+v"(oacc[3]));
      __builtin_amdgcn_s_setprio(0);
      if constexpr (G == 2) wait_vm<NP>();
      bar();
    };
    auto run = [&](auto G_) {
      constexpr int G = decltype(G_)::value;
      {
        unsigned kb0 = ka0 + kv_slot(0);
        asm volatile("" : "+v"(kb0));
#pragma unroll
        for (int ks = 0; ks < 4; ++ks) kf[ks] = lds_rd128(lds, kb0 ^ (ks << 5));
      }
      if constexpr (G >= 1) bar();
      if constexpr (G == 2) bar();
#pragma unroll 1
      for (int s = 0; s < 8; ++s) step(G_, s);
      if constexpr (G == 0) {
        const unsigned pc = ((b & 3) * 8 + (hd & 7)) * 36 + 32 + (w & 3);
        dma16<POL_WO>(rwo, lane_id() * 16, (hd >> 3) * (1024u * 1152u) + pc * 1024u, ldsb + SPARE + (w & 3) * 1024u);
      }
      if constexpr (G == 0) bar();
      if constexpr (G <= 1) bar();
    };
#pragma unroll 1
    for (;;) {
#pragma unroll
      for (int d = 0; d < 4; ++d)
#pragma unroll
        for (int i = 0; i < 16; ++i) oacc[d][i] = 0.f;
      m = safe ? -__builtin_inff() : 0.f; l0 = 0.f; l1 = 0.f;
      bar(); STAMP(5);
      if (grp == 2) run(IC<0>{});
      else if (grp == 1) run(IC<1>{});
      else run(IC<2>{});
      STAMP(13);
      const unsigned ln = lane_id();
      l = pl32_sum(l0 + l1);
      const unsigned xw = w * 4096 + ln * 16;
#pragma unroll
      for (int i = 0; i < 4; ++i) *(lds_bf16x8*)(lds + xw + i * 1024) = pack8(oacc[2 + (i >> 1)], i & 1);
      if (ln < 32) { f32x2 ml = {m, l}; *(lds_f32x2*)(lds + ML_OFF + w * 256 + ln * 8) = ml; }
      const bool bad = __any(!(l > L_MIN && l < L_MAX));
      if (ln == 0) *(__attribute__((address_space(3))) unsigned*)(lds + VOTE_OFF + w * 4) = bad ? 1u : 0u;
      bar();
      STAMP(6);
      if (safe) break;
      const unsigned vote = *(__attribute__((address_space(3))) unsigned*)(lds + VOTE_OFF + (ln < 12 ? ln : 0) * 4);
      if (!__any(vote != 0)) break;
      safe = true;
      bar();
      if (w >= 4) {
        const unsigned lw = rfl(w - 4), sk = rfl((b * NH + hd) * (SK * DH * 2));
        const unsigned vo = (ln >> 4) * 256 + (lw < 4 ? ((ln & 15) ^ (ln >> 4)) : ((ln & 15) ^ ((ln >> 4) << 2))) * 16;
        const __amdgpu_buffer_rsrc_t rr = __builtin_amdgcn_make_buffer_rsrc((void*)(lw < 4 ? kc : vc), (short)0, NB * NH * SK * DH * 2, 0x00020000);
#pragma unroll 1
        for (unsigned q = 0; q < 12; ++q) {
          const unsigned st = q >> 2, p = q & 3;
          dma16<1>(rr, lw < 4 ? vo ^ (p << 6) : vo, rfl(sk + ((lw >> 1) & 1) * 65536u + (lw & 1) * 4096u + st * 8192u + p * 1024u), rfl(ldsb + kv_slot(st) + lw * 4096u + p * 1024u));
        }
        wait_vm<0>();
      }
    }
  }

  const unsigned lane = lane_id();
  const unsigned hg = hd >> 3, hl = hd & 7, rh = hl & 1, cq = hl >> 1;
  const unsigned long long tag = epoch + 1;
  const unsigned wg = b * NH + hd;
  auto wo_voff = [&](int i) {
    const unsigned n = 64 * i + lane, p = n / 18, ch = n - 18 * p;
    const unsigned k = (p & 19) | ((p & 4) << 1) | ((p & 8) >> 1);
    return k * 1152 + ch * 16;
  };
  auto wo_issue = [&](const unsigned (&vo)[9], unsigned i) {
    const unsigned kh = w & 1, hh = (hl + (i >> 1)) & 7;
    const unsigned so = ((8 * hg + hh) * 128 + (2 * (i & 1) + kh) * 32) * 1152u + cq * 288u;
    const unsigned dst = ldsb + p2_wslot(i) + kh * 9216u;
#pragma unroll
    for (int p = 0; p < 9; ++p) dma16<POL_WO>(rwo, vo[p], so, dst + p * 1024u);
  };
  unsigned wvo[9];
  if (w == 8 || w == 9) {
#pragma unroll
    for (int p = 0; p < 9; ++p) wvo[p] = wo_voff(p);
  }

  {
    const unsigned r = lane & 31, h = lane >> 5, t = w >> 1, j = w & 1;
    f32x2 o = *(lds_f32x2*)(lds + ML_OFF + (w ^ 1) * 256 + r * 8);
    const float mn = fmaxf(m, o[0]);
    const float f = __builtin_amdgcn_exp2f(m - mn), fo = __builtin_amdgcn_exp2f(o[0] - mn);
    const float inv = 1.0f / (l * f + o[1] * fo);
    const float sa = f * inv, sb = fo * inv;
    const unsigned xr = (w ^ 1) * 4096 + lane * 16;
#pragma unroll
    for (int i = 0; i < 4; ++i) {
      const u32x4 v = __builtin_bit_cast(u32x4, lds_rd128(lds, xr + i * 1024));
#pragma unroll
      for (int e = 0; e < 4; ++e) {
        oacc[i >> 1][8 * (i & 1) + 2 * e] = oacc[i >> 1][8 * (i & 1) + 2 * e] * sa + __uint_as_float(v[e] << 16) * sb;
        oacc[i >> 1][8 * (i & 1) + 2 * e + 1] = oacc[i >> 1][8 * (i & 1) + 2 * e + 1] * sa + __uint_as_float(v[e] & 0xffff0000u) * sb;
      }
    }
    const __amdgpu_buffer_rsrc_t ro = __builtin_amdgcn_make_buffer_rsrc((void*)(ws + WS_O), (short)0, O_BYTES, 0x00020000);
    const unsigned so = ((wg * 6 + t) * 8 + 4 * j) * 1024u;
#pragma unroll
    for (int i = 0; i < 4; ++i)
      __builtin_amdgcn_raw_buffer_store_b128(__builtin_bit_cast(u32x4, pack8(oacc[i >> 1], i & 1)), ro, so + lane * 16 + i * 1024, 0, O_ST_AUX);
    STAMP(7);
    if (w == 8 || w == 9) { wo_issue(wvo, 0); wo_issue(wvo, 1); wait_vm<18>(); } else wait_vm<0>();
    STAMP(8);
    bar();
    if (tid == 0) __hip_atomic_store(flags + (size_t)wg * 16, tag, __ATOMIC_RELAXED, __HIP_MEMORY_SCOPE_AGENT);
  }
  STAMP(14);

  f32x4 acc[3][5];
#pragma unroll
  for (int a = 0; a < 3; ++a)
#pragma unroll
    for (int n = 0; n < 5; ++n) acc[a][n] = f32x4{0.f, 0.f, 0.f, 0.f};
  const unsigned nh = (w >> 2) & 1, kh = w & 1, qh = (w >> 1) & 1;
  if (w >= 10) {
    const __amdgpu_buffer_rsrc_t ro = __builtin_amdgcn_make_buffer_rsrc((void*)(ws + WS_O), (short)0, O_BYTES, 0x00020000);
    auto o_issue = [&](unsigned i) {
      const unsigned hh = (hl + (i >> 1)) & 7;
      const unsigned so = (((b * NH + 8 * hg + hh) * 6 + 3 * rh) * 8 + 2 * (2 * (i & 1) + kh)) * 1024u;
      const unsigned dst = ldsb + p2_oslot(i) + kh * 6144u;
#pragma unroll
      for (int q = 0; q < 3; ++q) { dma16<POL_O>(ro, lane * 16, so + q * 8192u, dst + q * 2048u); dma16<POL_O>(ro, lane * 16, so + q * 8192u + 1024u, dst + q * 2048u + 1024u); }
    };
    gu64* fp = flags + (size_t)(b * NH + 8 * hg + (lane & 7)) * 16;
    unsigned long long pv;
    auto poll_issue = [&]() { asm volatile("global_load_dwordx2 %0, %1, off sc1" : "=v"(pv) : "v"(fp) : "memory"); };
    unsigned rdy = 1u << hl;
    auto poll_take = [&]() {
      asm volatile("s_waitcnt vmcnt(0)" : "+v"(pv) :: "memory");
      rdy |= (unsigned)__ballot(pv == tag) & 0xffu;
    };
    o_issue(0); o_issue(1);
    poll_issue();
    STAMPW(10, 22);
    bool pending = true;
    unsigned spins = 0;
#pragma unroll 1
    for (unsigned i = 0; i < 16; ++i) {
      if (pending) wait_vm<7>(); else if (i <= 14) wait_vm<6>(); else wait_vm<0>();
      if (i == 0) STAMPW(10, 23);
      bar();
      if (i + 2 < 16) {
        const unsigned hh = (hl + ((i + 2) >> 1)) & 7;
        if (pending) { poll_take(); pending = false; }
        while (!((rdy >> hh) & 1) && spins < 400000u) { __builtin_amdgcn_s_sleep(1); poll_issue(); poll_take(); ++spins; }
        o_issue(i + 2);
      }
    }
  } else if (w >= 8) {
    wo_issue(wvo, 2); wo_issue(wvo, 3); wo_issue(wvo, 4);
#pragma unroll 1
    for (unsigned i = 0; i < 16; ++i) {
      if (i <= 11) wait_vm<36>(); else if (i == 12) wait_vm<27>(); else if (i == 13) wait_vm<18>(); else if (i == 14) wait_vm<9>(); else wait_vm<0>();
      bar();
      if (i + 5 < 16) wo_issue(wvo, i + 5);
    }
  } else {
    const unsigned c = lane & 15, g = lane >> 4;
    unsigned ba[3];
#pragma unroll
    for (int a = 0; a < 3; ++a) {
      const unsigned qi = 3 * qh + a;
      ba[a] = kh * 6144 + ((qi >> 1) * 2 + (g >> 1)) * 1024 + (16 * (qi & 1) + c + 32 * (g & 1)) * 16;
    }
    const unsigned aa = kh * 9216 + ((g >> 1) * 16 + (g & 1) * 4 + (c >> 2)) * 288 + 5 * nh * 32 + (c & 3) * 8;
    bf16x8 bX[3], aX[5], bY[3], aY[5];
    auto rd = [&](auto NT_, bf16x8 (&bb)[3], bf16x8 (&av)[5], unsigned i) {
      constexpr int NT = decltype(NT_)::value;
      unsigned ob = p2_oslot(i), wb = aa + p2_wslot(i);
      asm volatile("" : "+v"(wb));
#pragma unroll
      for (int a = 0; a < 3; ++a) bb[a] = lds_rd128(lds, ba[a] + ob);
#pragma unroll
      for (int n = 0; n < NT; ++n) av[n] = lds_tr2(lds, wb + n * 32, wb + n * 32 + 2304);
    };
    auto mm = [&](auto NT_, bf16x8 (&bb)[3], bf16x8 (&av)[5]) {
      constexpr int NT = decltype(NT_)::value;
#pragma unroll
      for (int n = 0; n < NT; ++n)
#pragma unroll
        for (int a = 0; a < 3; ++a) acc[a][n] = MFMA16(av[n], bb[a], acc[a][n]);
    };
    unsigned long long t0 = 0;
    auto mix = [&](auto NT_) {
      constexpr int NT = decltype(NT_)::value;
#pragma unroll
      for (int k = 0; k < 3 + 2 * NT - (NT == 4 ? 0 : 0); ++k) {
        __builtin_amdgcn_sched_group_barrier(0x008, 1, 0);
        __builtin_amdgcn_sched_group_barrier(0x100, 1, 0);
      }
      __builtin_amdgcn_sched_group_barrier(0x008, 3 * NT, 0);
    };
    auto gemm = [&](auto NT_) {
      bar();
      STAMP(15);
      rd(NT_, bX, aX, 0);
#pragma unroll 1
      for (unsigned i = 1; i < 15; i += 2) {
        bar(); rd(NT_, bY, aY, i); mm(NT_, bX, aX); mix(NT_);
        bar(); rd(NT_, bX, aX, i + 1); mm(NT_, bY, aY); mix(NT_);
      }
      bar(); rd(NT_, bY, aY, 15); mm(NT_, bX, aX); mix(NT_);
      SB(); mm(NT_, bY, aY);
    };
    if (nh == 0) gemm(IC<5>{}); else gemm(IC<4>{});
    WREC(44, TNOW() - t0);
  }
  STAMP(16);
  bar();
  if (w < 8) {
    const unsigned c = lane & 15, g = lane >> 4;
    const unsigned base = kh * IMG_BYTES + (48 * qh + c) * IMG_STRIDE + (80 * nh + 4 * g) * 2;
#pragma unroll
    for (int a = 0; a < 3; ++a)
#pragma unroll
      for (int n = 0; n < 5; ++n)
        if (n < 4 || nh == 0) {
          const u32x2 pk = {cvtpk(acc[a][n][0], acc[a][n][1]), cvtpk(acc[a][n][2], acc[a][n][3])};
          *(__attribute__((address_space(3))) u32x2*)(lds + base + a * 16 * IMG_STRIDE + n * 32) = pk;
        }
  }
  bar();
  {
    const __amdgpu_buffer_rsrc_t rp = __builtin_amdgcn_make_buffer_rsrc((void*)(ws + WS_PART), (short)0, PART_BYTES, 0x00020000);
    const unsigned pbase = ((b * 8 + hl) * 4 + hg) * 4 * PART_BLK;
#pragma unroll 1
    for (unsigned n = tid; n < 72 * 18; n += 768) {
      const unsigned rr = n / 18, ch = n - rr * 18;
      unsigned dst = rr / 24; const unsigned row = rr - dst * 24;
      dst += dst >= hg ? 1 : 0;
      const unsigned a0 = (24 * dst + row) * IMG_STRIDE + ch * 16;
      float v[8] = {0.f, 0.f, 0.f, 0.f, 0.f, 0.f, 0.f, 0.f};
      add8(v, __builtin_bit_cast(u32x4, lds_rd128(lds, a0))); add8(v, __builtin_bit_cast(u32x4, lds_rd128(lds, IMG_BYTES + a0)));
      const u32x4 pk = {cvtpk(v[0], v[1]), cvtpk(v[2], v[3]), cvtpk(v[4], v[5]), cvtpk(v[6], v[7])};
      __builtin_amdgcn_raw_buffer_store_b128(pk, rp, dst * PART_BLK + row * 288 + ch * 16 + pbase, 0, 16);
    }
    wait_vm<0>();
    bar();
    if (tid == 0) __hip_atomic_store(flags + (size_t)(256 + wg) * 16, tag, __ATOMIC_RELAXED, __HIP_MEMORY_SCOPE_AGENT);
    STAMP(17);
    if (w == 0) poll_flags(flags, 256 + b * NH + hl, 8, 4, hg, tag, lane);
    bar();
    STAMP(18);
    if (tid < 24 * 18) {
      const unsigned row = tid / 18, ch = tid - row * 18;
      u32x4 u[3];
#pragma unroll
      for (int k = 0; k < 3; ++k) {
        const unsigned src = k + (k >= (int)hg ? 1 : 0);
        u[k] = __builtin_amdgcn_raw_buffer_load_b128(rp, (((b * 8 + hl) * 4 + src) * 4 + hg) * PART_BLK + row * 288 + ch * 16, 0, 16);
      }
      const unsigned a0 = (24 * hg + row) * IMG_STRIDE + ch * 16;
      float v[8] = {0.f, 0.f, 0.f, 0.f, 0.f, 0.f, 0.f, 0.f};
      add8(v, __builtin_bit_cast(u32x4, lds_rd128(lds, a0))); add8(v, __builtin_bit_cast(u32x4, lds_rd128(lds, IMG_BYTES + a0)));
#pragma unroll
      for (int k = 0; k < 3; ++k) add8(v, u[k]);
      const u32x4 o = {cvtpk(v[0], v[1]), cvtpk(v[2], v[3]), cvtpk(v[4], v[5]), cvtpk(v[6], v[7])};
      const __amdgpu_buffer_rsrc_t rout = __builtin_amdgcn_make_buffer_rsrc((void*)out, (short)0, NB * SQ * DM * 2, 0x00020000);
      __builtin_amdgcn_raw_buffer_store_b128(o, rout, ((b * SQ + 96 * rh + 24 * hg + row) * DM + 144 * cq + 8 * ch) * 2, 0, OUT_AUX);
    }
    if (tid == 0 && hd == 0) __hip_atomic_store(flags + (size_t)(512 + b) * 16, tag, __ATOMIC_RELAXED, __HIP_MEMORY_SCOPE_AGENT);
  }
}

extern "C" void kernel_launch(void* const* d_in, const int* in_sizes, int n_in, void* d_out, int out_size, void* d_ws, size_t ws_size,
                              hipStream_t stream) {
  const __hip_bfloat16* x = (const __hip_bfloat16*)d_in[0];
  const __hip_bfloat16* kc = (const __hip_bfloat16*)d_in[1];
  const __hip_bfloat16* vc = (const __hip_bfloat16*)d_in[2];
  const __hip_bfloat16* wq = (const __hip_bfloat16*)d_in[3];
  const __hip_bfloat16* wo = (const __hip_bfloat16*)d_in[4];
  unsigned long long* diag = (unsigned long long*)((char*)d_ws + (64u << 20));
  mha_kernel<<<256, 768, 0, stream>>>(x, kc, vc, wq, wo, (__hip_bfloat16*)d_out, (char*)d_ws, diag);
}
```

```cpp
#include <hip/hip_runtime.h>
#include <hip/hip_bf16.h>
#include <type_traits>

typedef __bf16 bf16x8 __attribute__((ext_vector_type(8)));
typedef __bf16 bf16x4 __attribute__((ext_vector_type(4)));
typedef __bf16 bf16x2 __attribute__((ext_vector_type(2)));
typedef float f32x16 __attribute__((ext_vector_type(16)));
typedef float f32x4 __attribute__((ext_vector_type(4)));
typedef float f32x2 __attribute__((ext_vector_type(2)));
typedef unsigned u32x4 __attribute__((ext_vector_type(4)));
typedef unsigned u32x2 __attribute__((ext_vector_type(2)));
typedef __attribute__((address_space(3))) char lds_char;
typedef __attribute__((address_space(3))) bf16x4 lds_bf16x4;
typedef __attribute__((address_space(3))) bf16x8 lds_bf16x8;
typedef __attribute__((address_space(3))) f32x4 lds_f32x4;
typedef __attribute__((address_space(3))) f32x2 lds_f32x2;
typedef __attribute__((address_space(1))) unsigned long long gu64;

#define DI __device__ __forceinline__
#define MFMA32(a, b, c) __builtin_amdgcn_mfma_f32_32x32x16_bf16((a), (b), (c), 0, 0, 0)
#define MFMA16(a, b, c) __builtin_amdgcn_mfma_f32_16x16x32_bf16((a), (b), (c), 0, 0, 0)
#define SB() __builtin_amdgcn_sched_barrier(0)
template <int V> using IC = std::integral_constant<int, V>;
#define SG_MFMA(n) __builtin_amdgcn_sched_group_barrier(0x008, (n), 0)
#define SG_DSRD(n) __builtin_amdgcn_sched_group_barrier(0x100, (n), 0)

constexpr int NB = 8, SQ = 192, DM = 576, NH = 32, SK = 512, DH = 128;
constexpr float QSCALE = 0.08838834764831845f * 1.4426950408889634f;
constexpr float THR = 8.0f;

constexpr unsigned CODE_BYTES = 26 * 1024;
constexpr size_t WS_O = 0;
constexpr size_t WS_PART = 16u << 20;
constexpr size_t WS_FLAG = 32u << 20;
constexpr unsigned PART_BLK = 24 * 144 * 2;
constexpr unsigned O_BYTES = NB * NH * 6 * 8 * 1024, PART_BYTES = 8 * 8 * 4 * 4 * PART_BLK;

constexpr unsigned QS_BYTES = 40960, QS_W = 24576;
constexpr unsigned SPARE = 155648, LDS_TOTAL = 163840, ML_OFF = 98304, VOTE_OFF = 101376;
constexpr float L_MIN = 9.313225746154785e-10f, L_MAX = 1048576.0f;
DI constexpr unsigned kv_slot(int s) { return (unsigned)((s + 3) & 3) * QS_BYTES; }
DI unsigned qblk(unsigned blk) { return blk < 40 ? 2 * QS_BYTES + blk * 1024 : SPARE + (blk - 40) * 1024; }
constexpr unsigned P2_OSLOT = 12288, P2_WSLOT = 18432, P2_WBASE = 53248, IMG_STRIDE = 304, IMG_BYTES = 96 * IMG_STRIDE;
DI unsigned p2_oslot(unsigned i) { return (i & 3) * P2_OSLOT; }
DI unsigned p2_wslot(unsigned i) { return P2_WBASE + ((i + 3) % 6) * P2_WSLOT; }

DI unsigned rfl(unsigned x) { return __builtin_amdgcn_readfirstlane(x); }
DI unsigned cvtpk(float lo, float hi) { f32x2 v = {lo, hi}; bf16x2 b = __builtin_convertvector(v, bf16x2); return __builtin_bit_cast(unsigned, b); }
DI bf16x8 pack8(const f32x16& x, int s) {
  u32x4 p = {cvtpk(x[8 * s], x[8 * s + 1]), cvtpk(x[8 * s + 2], x[8 * s + 3]), cvtpk(x[8 * s + 4], x[8 * s + 5]), cvtpk(x[8 * s + 6], x[8 * s + 7])};
  return __builtin_bit_cast(bf16x8, p);
}
DI bf16x8 lds_rd128(lds_char* base, unsigned off) { return *(lds_bf16x8*)(base + off); }
DI bf16x8 lds_tr2(lds_char* base, unsigned off0, unsigned off1) {
  bf16x4 lo = __builtin_amdgcn_ds_read_tr16_b64_v4bf16((lds_bf16x4*)(base + off0));
  bf16x4 hi = __builtin_amdgcn_ds_read_tr16_b64_v4bf16((lds_bf16x4*)(base + off1));
  return __builtin_shufflevector(lo, hi, 0, 1, 2, 3, 4, 5, 6, 7);
}
template <int POL> DI void dma16(__amdgpu_buffer_rsrc_t r, unsigned voff, unsigned soff, unsigned m0v) {
  if constexpr (POL == 0) asm volatile("s_mov_b32 m0, %3\n\ts_nop 0\n\tbuffer_load_dwordx4 %0, %1, %2 offen lds" ::"v"(voff), "s"(r), "s"(soff), "s"(m0v) : "memory");
  else if constexpr (POL == 1) asm volatile("s_mov_b32 m0, %3\n\ts_nop 0\n\tbuffer_load_dwordx4 %0, %1, %2 offen nt lds" ::"v"(voff), "s"(r), "s"(soff), "s"(m0v) : "memory");
  else asm volatile("s_mov_b32 m0, %3\n\ts_nop 0\n\tbuffer_load_dwordx4 %0, %1, %2 offen sc1 lds" ::"v"(voff), "s"(r), "s"(soff), "s"(m0v) : "memory");
}
template <int N> DI void wait_vm() { asm volatile("s_waitcnt vmcnt(%0)" ::"i"(N) : "memory"); }
DI void bar() { SB(); asm volatile("s_waitcnt lgkmcnt(0)\n\ts_barrier" ::: "memory"); SB(); }
DI float pl32_max(float v) {
  auto r = __builtin_amdgcn_permlane32_swap(__float_as_uint(v), __float_as_uint(v), false, false);
  return fmaxf(__uint_as_float(r[0]), __uint_as_float(r[1]));
}
DI float pl32_sum(float v) {
  auto r = __builtin_amdgcn_permlane32_swap(__float_as_uint(v), __float_as_uint(v), false, false);
  return __uint_as_float(r[0]) + __uint_as_float(r[1]);
}
DI void add8(float (&v)[8], const u32x4& u) {
#pragma unroll
  for (int e = 0; e < 4; ++e) { v[2 * e] += __uint_as_float(u[e] << 16); v[2 * e + 1] += __uint_as_float(u[e] & 0xffff0000u); }
}
DI unsigned lane_id() { unsigned l; asm volatile("v_mbcnt_lo_u32_b32 %0, -1, 0\n\tv_mbcnt_hi_u32_b32 %0, -1, %0" : "=v"(l)); return l; }
DI void poll_flags(gu64* flags, unsigned idx0, unsigned stride, unsigned n, unsigned skip, unsigned long long tag, unsigned lane) {
  const bool mine = lane < n && lane != skip;
  gu64* p = flags + (size_t)(idx0 + (mine ? lane : 0) * stride) * 16;
  for (unsigned spins = 0; spins < 400000u; ++spins) {
    const unsigned long long v = __hip_atomic_load(p, __ATOMIC_RELAXED, __HIP_MEMORY_SCOPE_AGENT);
    if (__all(!mine || v == tag)) break;
    __builtin_amdgcn_s_sleep(1);
  }
}

__global__ __launch_bounds__(768, 3) void mha_kernel(const __hip_bfloat16* __restrict__ x, const __hip_bfloat16* __restrict__ kc,
                                                     const __hip_bfloat16* __restrict__ vc, const __hip_bfloat16* __restrict__ wq,
                                                     const __hip_bfloat16* __restrict__ wo, __hip_bfloat16* __restrict__ out,
                                                     char* __restrict__ ws) {
  __shared__ __attribute__((aligned(1024))) char smem[LDS_TOTAL];
  lds_char* lds = (lds_char*)smem;
  const unsigned ldsb = (unsigned)(size_t)smem;
  const unsigned tid = threadIdx.x, w = rfl(tid >> 6);
  const unsigned xcd = blockIdx.x & 7, jj = blockIdx.x >> 3;
  const unsigned b = 4 * (xcd & 1) + (jj & 3), hd = 8 * (xcd >> 1) + (jj >> 2);
  gu64* flags = (gu64*)(ws + WS_FLAG);
  unsigned long long epoch;
  u32x2 ev;
  auto epoch_issue = [&]() { asm volatile("global_load_dwordx2 %0, %1, off sc1" : "=v"(ev) : "v"((const char*)(ws + WS_FLAG) + (512 + b) * 128) : "memory"); };
  auto epoch_take = [&]() {
    asm volatile("" : "+v"(ev));
    epoch = ((unsigned long long)rfl(ev[1]) << 32) | rfl(ev[0]);
  };
  const __amdgpu_buffer_rsrc_t rwo = __builtin_amdgcn_make_buffer_rsrc((void*)wo, (short)0, NH * DH * DM * 2, 0x00020000);
  bf16x8 qf[8];
  f32x16 oacc[4];
  float m, l;
  {
    const unsigned lane = tid & 63, r = lane & 31, h = lane >> 5;
    const unsigned t = w >> 1, j = w & 1;
    const unsigned g1 = (lane >> 4) & 1, qq = (lane & 15) >> 2, pp = lane & 3, sp = ((pp & 1) << 1) | (pp >> 1);
    const __amdgpu_buffer_rsrc_t rk = __builtin_amdgcn_make_buffer_rsrc((void*)kc, (short)0, NB * NH * SK * DH * 2, 0x00020000);
    const __amdgpu_buffer_rsrc_t rv = __builtin_amdgcn_make_buffer_rsrc((void*)vc, (short)0, NB * NH * SK * DH * 2, 0x00020000);
    const unsigned skv = (b * NH + hd) * (SK * DH * 2);
    const unsigned vk = (lane >> 4) * 256 + ((lane & 15) ^ (lane >> 4)) * 16;
    const unsigned vv = (lane >> 4) * 256 + ((lane & 15) ^ ((lane >> 4) << 2)) * 16;

    auto kv4 = [&](int s) {
      const unsigned lw = w - 4;
      const unsigned dst = ldsb + kv_slot(s) + lw * 4096u;
      const unsigned so = skv + ((lw >> 1) & 1) * 65536u + (lw & 1) * 4096u + (unsigned)s * 8192u;
#pragma unroll
      for (int p = 0; p < 4; ++p) {
        if (lw < 4) dma16<1>(rk, vk ^ (p << 6), so + p * 1024u, dst + p * 1024u);
        else dma16<1>(rv, vv, so + p * 1024u, dst + p * 1024u);
      }
    };
    const unsigned koff = 3 * (((jj >> 2) + (jj & 3)) % 3);
    if (w >= 4) {
      const unsigned lw = w - 4;
      const __amdgpu_buffer_rsrc_t rx = __builtin_amdgcn_make_buffer_rsrc((void*)x, (short)0, NB * SQ * DM * 2, 0x00020000);
      const __amdgpu_buffer_rsrc_t rw = __builtin_amdgcn_make_buffer_rsrc((void*)wq, (short)0, DM * NH * DH * 2, 0x00020000);
      const unsigned vx = (lane >> 3) * 1152 + (((lane & 7) ^ (lane >> 4) ^ ((lw & 1) << 2)) * 16);
      const unsigned sx = (b * SQ + 8 * lw) * 1152u;
      const unsigned vw = (lane >> 4) * 8192 + (((lane & 15) ^ ((lane >> 4) << 2)) * 16);
      const unsigned sw = 4 * lw * 8192u + hd * 256u;
      auto issue_q = [&](int i) {
        unsigned st = koff + i; st = st >= 9 ? st - 9 : st;
        const unsigned slot = ldsb + (unsigned)(i % 3) * QS_BYTES;
#pragma unroll
        for (int p = 0; p < 3; ++p) dma16<0>(rx, vx, sx + (unsigned)(64 * p) * 1152u + st * 128u, slot + (lw + 8 * p) * 1024u);
#pragma unroll
        for (int p = 0; p < 2; ++p) dma16<0>(rw, vw, sw + (st * 64 + 32 * p) * 8192u, slot + QS_W + (lw + 8 * p) * 1024u);
      };
      epoch_issue();
      issue_q(0); issue_q(1);
      wait_vm<5>(); epoch_take(); bar(); issue_q(2);
      wait_vm<5>(); bar(); issue_q(3);
      wait_vm<5>(); bar(); issue_q(4); kv4(0);
      wait_vm<9>(); bar(); issue_q(5);
      wait_vm<9>(); bar(); issue_q(6);
      wait_vm<5>(); bar(); issue_q(7);
      wait_vm<5>(); bar(); issue_q(8);
      wait_vm<5>(); bar(); kv4(1);
      wait_vm<4>(); bar(); kv4(2);
      bar();
      bar();
    } else {
      epoch_issue(); wait_vm<0>(); epoch_take();
      if (w < 2) {
        unsigned long long pc;
        asm volatile("s_getpc_b64 %0" : "=s"(pc));
        const unsigned off = (jj * 2 + w) * 1024u;
        if (off + 1024u <= CODE_BYTES) {
          u32x4 t;
          const char* p = (const char*)(pc & ~127ull) + off + lane * 16;
          asm volatile("global_load_dwordx4 %0, %1, off\n\ts_waitcnt vmcnt(0)" : "=v"(t) : "v"(p) : "memory");
        }
      }
      if (w == 2) {
        const unsigned hg_ = hd >> 3, hl_ = hd & 7, wg_ = b * NH + hd;
        const char* p = (const char*)ws + WS_FLAG;
        if (lane == 0) p = (const char*)kc + (size_t)wg_ * (SK * DH * 2);
        if (lane == 1) p = (const char*)vc + (size_t)wg_ * (SK * DH * 2);
        if (lane == 2) p = (const char*)ws + WS_O + (size_t)wg_ * 49152;
        if (lane == 3) p = (const char*)ws + WS_O + (size_t)(b * NH + 8 * hg_) * 49152;
        if (lane == 4) p = (const char*)ws + WS_O + (size_t)(b * NH + 8 * hg_ + 8) * 49152 - 64;
        if (lane >= 5 && lane < 9) p = (const char*)ws + WS_PART + (size_t)(((b * 8 + hl_) * 4 + (lane - 5)) * 4) * PART_BLK;
        if (lane == 9) p = (const char*)wo + (size_t)hg_ * (1024 * 1152);
        if (lane == 10) p = (const char*)wo + (size_t)(hg_ + 1) * (1024 * 1152) - 64;
        if (lane == 11) p = (const char*)out + (size_t)(b * SQ) * (DM * 2);
        if (lane == 12) p = (const char*)kc + (size_t)(wg_ + 1) * (SK * DH * 2) - 64;
        if (lane == 13) p = (const char*)vc + (size_t)(wg_ + 1) * (SK * DH * 2) - 64;
        unsigned t;
        asm volatile("global_load_dword %0, %1, off\n\ts_waitcnt vmcnt(0)" : "=v"(t) : "v"(p) : "memory");
      }
      const unsigned qh = w >> 1, dh = w & 1;
      const unsigned xa0 = (96 * qh + r) * 128 + ((h ^ ((r >> 1) & 7)) << 4);
      const unsigned wa0 = QS_W + (8 * h + qq) * 256 + ((4 * ((2 * dh) ^ qq) + 2 * g1 + (sp >> 1)) << 4) + (sp & 1) * 8;
      f32x16 qacc[3][2];
#pragma unroll
      for (int a = 0; a < 3; ++a)
#pragma unroll
        for (int i = 0; i < 16; ++i) { qacc[a][0][i] = 0.f; qacc[a][1][i] = 0.f; }
      bf16x8 bA[3], aA[2], bB[3], aB[2];
      unsigned xb, wb;
      auto rd = [&](bf16x8 (&bb)[3], bf16x8 (&aa)[2], int s) {
        const unsigned xs = xb ^ (s << 5), wb1 = wb ^ 64;
        aa[0] = lds_tr2(lds, wb + s * 4096, wb + s * 4096 + 1024);
        bb[0] = lds_rd128(lds, xs); bb[1] = lds_rd128(lds, xs + 4096); bb[2] = lds_rd128(lds, xs + 8192);
        aa[1] = lds_tr2(lds, wb1 + s * 4096, wb1 + s * 4096 + 1024);
      };
      auto mm = [&](bf16x8 (&bb)[3], bf16x8 (&aa)[2]) {
        qacc[0][0] = MFMA32(aa[0], bb[0], qacc[0][0]);
        qacc[1][0] = MFMA32(aa[0], bb[1], qacc[1][0]);
        qacc[2][0] = MFMA32(aa[0], bb[2], qacc[2][0]);
        qacc[0][1] = MFMA32(aa[1], bb[0], qacc[0][1]);
        qacc[1][1] = MFMA32(aa[1], bb[1], qacc[1][1]);
        qacc[2][1] = MFMA32(aa[1], bb[2], qacc[2][1]);
      };
      bar();
      xb = xa0; wb = wa0;
      asm volatile("" : "+v"(xb), "+v"(wb));
      rd(bA, aA, 0);
      auto mix = [&]() { SG_MFMA(1); SG_DSRD(2); SG_MFMA(1); SG_DSRD(2); SG_MFMA(1); SG_DSRD(1); SG_MFMA(1); SG_DSRD(1); SG_MFMA(1); SG_DSRD(1); SG_MFMA(1); SB(); };
#pragma unroll 1
      for (int st = 0; st < 8; ++st) {
        SB(); rd(bB, aB, 1); mm(bA, aA); mix();
        rd(bA, aA, 2); mm(bB, aB); mix();
        rd(bB, aB, 3); mm(bA, aA); mix();
        bar();
        const unsigned so = (unsigned)((st + 1) % 3) * QS_BYTES;
        xb = xa0 + so; wb = wa0 + so;
        asm volatile("" : "+v"(xb), "+v"(wb));
        rd(bA, aA, 0); mm(bB, aB); mix();
      }
      SB(); rd(bB, aB, 1); mm(bA, aA); mix();
      rd(bA, aA, 2); mm(bB, aB); mix();
      rd(bB, aB, 3); mm(bA, aA); mix();
      mm(bB, aB);
      bar();
#pragma unroll
      for (int a = 0; a < 3; ++a)
#pragma unroll
        for (int d = 0; d < 2; ++d) {
#pragma unroll
          for (int i = 0; i < 16; ++i) qacc[a][d][i] *= QSCALE;
#pragma unroll
          for (int s2 = 0; s2 < 2; ++s2) {
            const unsigned blk = (3 * qh + a) * 8 + 4 * dh + 2 * d + s2;
            *(lds_bf16x8*)(lds + qblk(blk) + lane * 16) = pack8(qacc[a][d], s2);
          }
        }
      bar();
    }
#pragma unroll
    for (int i = 0; i < 8; ++i) qf[i] = lds_rd128(lds, qblk(t * 8 + i) + lane * 16);

    const unsigned grp = w >> 2;
    float l0, l1;
    bool safe = false;
    const unsigned ka0 = j * 8192 + r * 256 + ((h ^ (r & 15)) << 4);
    const unsigned va0 = (16384 + j * 8192 + (4 * h + qq) * 256 + ((4 * qq + 2 * g1 + (sp >> 1)) << 4) + (sp & 1) * 8) ^ (j << 7);
    const unsigned vkx = vk ^ ((w & 3) << 6);
    const unsigned sp0 = skv + ((w >> 3) & 1) * 65536u + (w & 7) * 1024u;
    const unsigned sp1 = skv + (((w + 12) >> 3) & 1) * 65536u + ((w + 12) & 7) * 1024u;
    const unsigned sp2 = skv + (((w + 24) >> 3) & 1) * 65536u + ((w + 24) & 7) * 1024u;

    bf16x8 kf[4];
    auto step = [&](auto G_, int s) {
      constexpr int G = decltype(G_)::value;
      constexpr int WGP = 2 - G;
      constexpr int NP = WGP == 2 ? 2 : 3;
      unsigned vb = va0 + kv_slot(s), kc0 = ka0 + kv_slot(s), kb = ka0 + kv_slot(s + 1);
      asm volatile("" : "+v"(kb), "+v"(vb), "+v"(kc0));
      __builtin_amdgcn_s_setprio(1);
      f32x16 sc;
      bf16x8 vf[4][2];
      {
        bf16x8 kg[4];
#pragma unroll
        for (int ks = 0; ks < 4; ++ks) kg[ks] = lds_rd128(lds, kc0 ^ ((ks + 4) << 5));
#pragma unroll
        for (int i = 0; i < 16; ++i) sc[i] = 0.f;
#pragma unroll
        for (int ks = 0; ks < 4; ++ks) sc = MFMA32(kf[ks], qf[ks], sc);
#pragma unroll
        for (int d = 0; d < 4; ++d) { const unsigned va = vb ^ (d << 6); vf[d][0] = lds_tr2(lds, va, va + 2048); }
#pragma unroll
        for (int ks = 0; ks < 4; ++ks) sc = MFMA32(kg[ks], qf[ks + 4], sc);
        SG_MFMA(1); SG_DSRD(2); SG_MFMA(1); SG_DSRD(2); SG_MFMA(1); SG_DSRD(2); SG_MFMA(1); SG_DSRD(2);
        SG_MFMA(1); SG_DSRD(2); SG_MFMA(1); SG_DSRD(2); SG_MFMA(2);
        SB();
        asm volatile("" : "+v"(sc));
      }
      __builtin_amdgcn_s_setprio(0);
      if constexpr (G == 1) wait_vm<NP>();
      bar();
#pragma unroll
      for (int d = 0; d < 4; ++d) { const unsigned va = vb ^ (d << 6); vf[d][1] = lds_tr2(lds, va + 4096, va + 4096 + 2048); }
      SB();
      if (safe) {
        const float t0 = fmaxf(fmaxf(sc[0], sc[1]), sc[2]), t1 = fmaxf(fmaxf(sc[3], sc[4]), sc[5]), t2 = fmaxf(fmaxf(sc[6], sc[7]), sc[8]);
        const float t3 = fmaxf(fmaxf(sc[9], sc[10]), sc[11]), t4 = fmaxf(fmaxf(sc[12], sc[13]), sc[14]);
        float tm = fmaxf(fmaxf(fmaxf(t0, t1), t2), fmaxf(fmaxf(t3, t4), sc[15]));
        tm = pl32_max(tm);
        if (__any(tm > m + THR)) {
          const float mn = fmaxf(m, tm);
          const float al = __builtin_amdgcn_exp2f(m - mn);
          l0 *= al; l1 *= al; m = mn;
#pragma unroll
          for (int d = 0; d < 4; ++d)
#pragma unroll
            for (int i = 0; i < 16; ++i) oacc[d][i] *= al;
        }
#pragma unroll
        for (int i = 0; i < 16; ++i) sc[i] -= m;
      }
      SB();
#pragma unroll
      for (int i = 0; i < 16; ++i) sc[i] = __builtin_amdgcn_exp2f(sc[i]);
      bf16x8 p0 = pack8(sc, 0), p1 = pack8(sc, 1);
#pragma unroll
      for (int i = 0; i < 16; i += 2) { l0 += sc[i]; l1 += sc[i + 1]; }
      asm volatile("" : "+v"(p0), "+v"(p1), "+v"(l0), "+v"(l1));
      if constexpr (G == 0) wait_vm<NP>();
      bar();
      __builtin_amdgcn_s_setprio(1);
#pragma unroll
      for (int d = 0; d < 4; ++d) oacc[d] = MFMA32(vf[d][0], p0, oacc[d]);
#pragma unroll
      for (int ks = 0; ks < 4; ++ks) kf[ks] = lds_rd128(lds, kb ^ (ks << 5));
      SG_MFMA(2); SG_DSRD(2); SG_MFMA(1); SG_DSRD(1); SG_MFMA(1); SG_DSRD(1);
      SB();
      if (s <= 4) {
        const unsigned dst = ldsb + kv_slot(s + 3) + w * 1024u, sofs = (unsigned)(s + 3) * 8192u;
        dma16<1>(rk, vkx, sp0 + sofs, dst);
        if constexpr (WGP == 0) dma16<1>(rk, vkx, sp1 + sofs, dst + 12288u); else dma16<1>(rv, vv, sp1 + sofs, dst + 12288u);
        if constexpr (WGP != 2) dma16<1>(rv, vv, sp2 + sofs, dst + 24576u);
      } else if (s == 5) {
        const unsigned pc = ((b & 3) * 8 + (hd & 7)) * 36 + (WGP == 2 ? 24 + 2 * (w - 8) : 3 * w);
        const unsigned so = (hd >> 3) * (1024u * 1152u) + pc * 1024u, vo = lane_id() * 16;
#pragma unroll
        for (int p = 0; p < NP; ++p) dma16<0>(rwo, vo, so + p * 1024u, ldsb + SPARE + (w & 7) * 1024u);
      }
      SB();
#pragma unroll
      for (int d = 0; d < 4; ++d) oacc[d] = MFMA32(vf[d][1], p1, oacc[d]);
      SB();
      asm volatile("" : "+v"(oacc[0]), "+v"(oacc[1]), "+v"(oacc[2]), "+v"(oacc[3]));
      __builtin_amdgcn_s_setprio(0);
      if constexpr (G == 2) wait_vm<NP>();
      bar();
    };
    auto run = [&](auto G_) {
      constexpr int G = decltype(G_)::value;
      {
        unsigned kb0 = ka0 + kv_slot(0);
        asm volatile("" : "+v"(kb0));
#pragma unroll
        for (int ks = 0; ks < 4; ++ks) kf[ks] = lds_rd128(lds, kb0 ^ (ks << 5));
      }
      if constexpr (G >= 1) bar();
      if constexpr (G == 2) bar();
#pragma unroll 1
      for (int s = 0; s < 8; ++s) step(G_, s);
      if constexpr (G == 0) {
        const unsigned pc = ((b & 3) * 8 + (hd & 7)) * 36 + 32 + (w & 3);
        dma16<0>(rwo, lane_id() * 16, (hd >> 3) * (1024u * 1152u) + pc * 1024u, ldsb + SPARE + (w & 3) * 1024u);
      }
      if constexpr (G == 0) bar();
      if constexpr (G <= 1) bar();
    };
#pragma unroll 1
    for (;;) {
#pragma unroll
      for (int d = 0; d < 4; ++d)
#pragma unroll
        for (int i = 0; i < 16; ++i) oacc[d][i] = 0.f;
      m = safe ? -__builtin_inff() : 0.f; l0 = 0.f; l1 = 0.f;
      bar();
      if (grp == 2) run(IC<0>{});
      else if (grp == 1) run(IC<1>{});
      else run(IC<2>{});
      const unsigned ln = lane_id();
      l = pl32_sum(l0 + l1);
      const unsigned xw = w * 4096 + ln * 16;
#pragma unroll
      for (int i = 0; i < 4; ++i) *(lds_bf16x8*)(lds + xw + i * 1024) = pack8(oacc[2 + (i >> 1)], i & 1);
      if (ln < 32) { f32x2 ml = {m, l}; *(lds_f32x2*)(lds + ML_OFF + w * 256 + ln * 8) = ml; }
      const bool bad = __any(!(l > L_MIN && l < L_MAX));
      if (ln == 0) *(__attribute__((address_space(3))) unsigned*)(lds + VOTE_OFF + w * 4) = bad ? 1u : 0u;
      bar();
      if (safe) break;
      const unsigned vote = *(__attribute__((address_space(3))) unsigned*)(lds + VOTE_OFF + (ln < 12 ? ln : 0) * 4);
      if (!__any(vote != 0)) break;
      safe = true;
      bar();
      if (w >= 4) {
        const unsigned lw = rfl(w - 4), sk = rfl((b * NH + hd) * (SK * DH * 2));
        const unsigned vo = (ln >> 4) * 256 + (lw < 4 ? ((ln & 15) ^ (ln >> 4)) : ((ln & 15) ^ ((ln >> 4) << 2))) * 16;
        const __amdgpu_buffer_rsrc_t rr = __builtin_amdgcn_make_buffer_rsrc((void*)(lw < 4 ? kc : vc), (short)0, NB * NH * SK * DH * 2, 0x00020000);
#pragma unroll 1
        for (unsigned q = 0; q < 12; ++q) {
          const unsigned st = q >> 2, p = q & 3;
          dma16<1>(rr, lw < 4 ? vo ^ (p << 6) : vo, rfl(sk + ((lw >> 1) & 1) * 65536u + (lw & 1) * 4096u + st * 8192u + p * 1024u), rfl(ldsb + kv_slot(st) + lw * 4096u + p * 1024u));
        }
        wait_vm<0>();
      }
    }
  }

  const unsigned lane = lane_id();
  const unsigned hg = hd >> 3, hl = hd & 7, rh = hl & 1, cq = hl >> 1;
  const unsigned long long tag = epoch + 1;
  const unsigned wg = b * NH + hd;
  auto wo_voff = [&](int i) {
    const unsigned n = 64 * i + lane, p = n / 18, ch = n - 18 * p;
    const unsigned k = (p & 19) | ((p & 4) << 1) | ((p & 8) >> 1);
    return k * 1152 + ch * 16;
  };
  auto wo_issue = [&](const unsigned (&vo)[9], unsigned i) {
    const unsigned kh = w & 1, hh = (hl + (i >> 1)) & 7;
    const unsigned so = ((8 * hg + hh) * 128 + (2 * (i & 1) + kh) * 32) * 1152u + cq * 288u;
    const unsigned dst = ldsb + p2_wslot(i) + kh * 9216u;
#pragma unroll
    for (int p = 0; p < 9; ++p) dma16<0>(rwo, vo[p], so, dst + p * 1024u);
  };
  unsigned wvo[9];
  if (w == 8 || w == 9) {
#pragma unroll
    for (int p = 0; p < 9; ++p) wvo[p] = wo_voff(p);
  }

  {
    const unsigned r = lane & 31, t = w >> 1, j = w & 1;
    f32x2 o = *(lds_f32x2*)(lds + ML_OFF + (w ^ 1) * 256 + r * 8);
    const float mn = fmaxf(m, o[0]);
    const float f = __builtin_amdgcn_exp2f(m - mn), fo = __builtin_amdgcn_exp2f(o[0] - mn);
    const float inv = 1.0f / (l * f + o[1] * fo);
    const float sa = f * inv, sb = fo * inv;
    const unsigned xr = (w ^ 1) * 4096 + lane * 16;
#pragma unroll
    for (int i = 0; i < 4; ++i) {
      const u32x4 v = __builtin_bit_cast(u32x4, lds_rd128(lds, xr + i * 1024));
#pragma unroll
      for (int e = 0; e < 4; ++e) {
        oacc[i >> 1][8 * (i & 1) + 2 * e] = oacc[i >> 1][8 * (i & 1) + 2 * e] * sa + __uint_as_float(v[e] << 16) * sb;
        oacc[i >> 1][8 * (i & 1) + 2 * e + 1] = oacc[i >> 1][8 * (i & 1) + 2 * e + 1] * sa + __uint_as_float(v[e] & 0xffff0000u) * sb;
      }
    }
    const __amdgpu_buffer_rsrc_t ro = __builtin_amdgcn_make_buffer_rsrc((void*)(ws + WS_O), (short)0, O_BYTES, 0x00020000);
    const unsigned so = ((wg * 6 + t) * 8 + 4 * j) * 1024u;
#pragma unroll
    for (int i = 0; i < 4; ++i)
      __builtin_amdgcn_raw_buffer_store_b128(__builtin_bit_cast(u32x4, pack8(oacc[i >> 1], i & 1)), ro, so + lane * 16 + i * 1024, 0, 16);
    if (w == 8 || w == 9) { wo_issue(wvo, 0); wo_issue(wvo, 1); wait_vm<18>(); } else wait_vm<0>();
    bar();
    if (tid == 0) __hip_atomic_store(flags + (size_t)wg * 16, tag, __ATOMIC_RELAXED, __HIP_MEMORY_SCOPE_AGENT);
  }
  f32x4 acc[3][5];
#pragma unroll
  for (int a = 0; a < 3; ++a)
#pragma unroll
    for (int n = 0; n < 5; ++n) acc[a][n] = f32x4{0.f, 0.f, 0.f, 0.f};
  const unsigned nh = (w >> 2) & 1, kh = w & 1, qh = (w >> 1) & 1;
  if (w >= 10) {
    const __amdgpu_buffer_rsrc_t ro = __builtin_amdgcn_make_buffer_rsrc((void*)(ws + WS_O), (short)0, O_BYTES, 0x00020000);
    auto o_issue = [&](unsigned i) {
      const unsigned hh = (hl + (i >> 1)) & 7;
      const unsigned so = (((b * NH + 8 * hg + hh) * 6 + 3 * rh) * 8 + 2 * (2 * (i & 1) + kh)) * 1024u;
      const unsigned dst = ldsb + p2_oslot(i) + kh * 6144u;
#pragma unroll
      for (int q = 0; q < 3; ++q) { dma16<2>(ro, lane * 16, so + q * 8192u, dst + q * 2048u); dma16<2>(ro, lane * 16, so + q * 8192u + 1024u, dst + q * 2048u + 1024u); }
    };
    gu64* fp = flags + (size_t)(b * NH + 8 * hg + (lane & 7)) * 16;
    unsigned long long pv;
    auto poll_issue = [&]() { asm volatile("global_load_dwordx2 %0, %1, off sc1" : "=v"(pv) : "v"(fp) : "memory"); };
    unsigned rdy = 1u << hl;
    auto poll_take = [&]() {
      asm volatile("s_waitcnt vmcnt(0)" : "+v"(pv) :: "memory");
      rdy |= (unsigned)__ballot(pv == tag) & 0xffu;
    };
    o_issue(0); o_issue(1);
    poll_issue();
    bool pending = true;
    unsigned spins = 0;
#pragma unroll 1
    for (unsigned i = 0; i < 16; ++i) {
      if (pending) wait_vm<7>(); else if (i <= 14) wait_vm<6>(); else wait_vm<0>();
      bar();
      if (i + 2 < 16) {
        const unsigned hh = (hl + ((i + 2) >> 1)) & 7;
        if (pending) { poll_take(); pending = false; }
        while (!((rdy >> hh) & 1) && spins < 400000u) { __builtin_amdgcn_s_sleep(1); poll_issue(); poll_take(); ++spins; }
        o_issue(i + 2);
      }
    }
  } else if (w >= 8) {
    wo_issue(wvo, 2); wo_issue(wvo, 3); wo_issue(wvo, 4);
#pragma unroll 1
    for (unsigned i = 0; i < 16; ++i) {
      if (i <= 11) wait_vm<36>(); else if (i == 12) wait_vm<27>(); else if (i == 13) wait_vm<18>(); else if (i == 14) wait_vm<9>(); else wait_vm<0>();
      bar();
      if (i + 5 < 16) wo_issue(wvo, i + 5);
    }
  } else {
    const unsigned c = lane & 15, g = lane >> 4;
    unsigned ba[3];
#pragma unroll
    for (int a = 0; a < 3; ++a) {
      const unsigned qi = 3 * qh + a;
      ba[a] = kh * 6144 + ((qi >> 1) * 2 + (g >> 1)) * 1024 + (16 * (qi & 1) + c + 32 * (g & 1)) * 16;
    }
    const unsigned aa = kh * 9216 + ((g >> 1) * 16 + (g & 1) * 4 + (c >> 2)) * 288 + 5 * nh * 32 + (c & 3) * 8;
    bf16x8 bX[3], aX[5], bY[3], aY[5];
    auto rd = [&](auto NT_, bf16x8 (&bb)[3], bf16x8 (&av)[5], unsigned i) {
      constexpr int NT = decltype(NT_)::value;
      unsigned ob = p2_oslot(i), wb = aa + p2_wslot(i);
      asm volatile("" : "+v"(wb));
#pragma unroll
      for (int a = 0; a < 3; ++a) bb[a] = lds_rd128(lds, ba[a] + ob);
#pragma unroll
      for (int n = 0; n < NT; ++n) av[n] = lds_tr2(lds, wb + n * 32, wb + n * 32 + 2304);
    };
    auto mm = [&](auto NT_, bf16x8 (&bb)[3], bf16x8 (&av)[5]) {
      constexpr int NT = decltype(NT_)::value;
#pragma unroll
      for (int n = 0; n < NT; ++n)
#pragma unroll
        for (int a = 0; a < 3; ++a) acc[a][n] = MFMA16(av[n], bb[a], acc[a][n]);
    };
    auto mix = [&](auto NT_) {
      constexpr int NT = decltype(NT_)::value;
#pragma unroll
      for (int k = 0; k < 3 + 2 * NT; ++k) {
        __builtin_amdgcn_sched_group_barrier(0x008, 1, 0);
        __builtin_amdgcn_sched_group_barrier(0x100, 1, 0);
      }
      __builtin_amdgcn_sched_group_barrier(0x008, 3 * NT, 0);
    };
    auto gemm = [&](auto NT_) {
      bar();
      rd(NT_, bX, aX, 0);
#pragma unroll 1
      for (unsigned i = 1; i < 15; i += 2) {
        bar(); rd(NT_, bY, aY, i); mm(NT_, bX, aX); mix(NT_);
        bar(); rd(NT_, bX, aX, i + 1); mm(NT_, bY, aY); mix(NT_);
      }
      bar(); rd(NT_, bY, aY, 15); mm(NT_, bX, aX); mix(NT_);
      SB(); mm(NT_, bY, aY);
    };
    if (nh == 0) gemm(IC<5>{}); else gemm(IC<4>{});
  }
  bar();
  if (w < 8) {
    const unsigned c = lane & 15, g = lane >> 4;
    const unsigned base = kh * IMG_BYTES + (48 * qh + c) * IMG_STRIDE + (80 * nh + 4 * g) * 2;
#pragma unroll
    for (int a = 0; a < 3; ++a)
#pragma unroll
      for (int n = 0; n < 5; ++n)
        if (n < 4 || nh == 0) {
          const u32x2 pk = {cvtpk(acc[a][n][0], acc[a][n][1]), cvtpk(acc[a][n][2], acc[a][n][3])};
          *(__attribute__((address_space(3))) u32x2*)(lds + base + a * 16 * IMG_STRIDE + n * 32) = pk;
        }
  }
  bar();
  {
    const __amdgpu_buffer_rsrc_t rp = __builtin_amdgcn_make_buffer_rsrc((void*)(ws + WS_PART), (short)0, PART_BYTES, 0x00020000);
    const unsigned pbase = ((b * 8 + hl) * 4 + hg) * 4 * PART_BLK;
#pragma unroll 1
    for (unsigned n = tid; n < 72 * 18; n += 768) {
      const unsigned rr = n / 18, ch = n - rr * 18;
      unsigned dst = rr / 24; const unsigned row = rr - dst * 24;
      dst += dst >= hg ? 1 : 0;
      const unsigned a0 = (24 * dst + row) * IMG_STRIDE + ch * 16;
      float v[8] = {0.f, 0.f, 0.f, 0.f, 0.f, 0.f, 0.f, 0.f};
      add8(v, __builtin_bit_cast(u32x4, lds_rd128(lds, a0))); add8(v, __builtin_bit_cast(u32x4, lds_rd128(lds, IMG_BYTES + a0)));
      const u32x4 pk = {cvtpk(v[0], v[1]), cvtpk(v[2], v[3]), cvtpk(v[4], v[5]), cvtpk(v[6], v[7])};
      __builtin_amdgcn_raw_buffer_store_b128(pk, rp, dst * PART_BLK + row * 288 + ch * 16 + pbase, 0, 16);
    }
    wait_vm<0>();
    bar();
    if (tid == 0) __hip_atomic_store(flags + (size_t)(256 + wg) * 16, tag, __ATOMIC_RELAXED, __HIP_MEMORY_SCOPE_AGENT);
    if (w == 0) poll_flags(flags, 256 + b * NH + hl, 8, 4, hg, tag, lane);
    bar();
    if (tid < 24 * 18) {
      const unsigned row = tid / 18, ch = tid - row * 18;
      u32x4 u[3];
#pragma unroll
      for (int k = 0; k < 3; ++k) {
        const unsigned src = k + (k >= (int)hg ? 1 : 0);
        u[k] = __builtin_amdgcn_raw_buffer_load_b128(rp, (((b * 8 + hl) * 4 + src) * 4 + hg) * PART_BLK + row * 288 + ch * 16, 0, 16);
      }
      const unsigned a0 = (24 * hg + row) * IMG_STRIDE + ch * 16;
      float v[8] = {0.f, 0.f, 0.f, 0.f, 0.f, 0.f, 0.f, 0.f};
      add8(v, __builtin_bit_cast(u32x4, lds_rd128(lds, a0))); add8(v, __builtin_bit_cast(u32x4, lds_rd128(lds, IMG_BYTES + a0)));
#pragma unroll
      for (int k = 0; k < 3; ++k) add8(v, u[k]);
      const u32x4 o = {cvtpk(v[0], v[1]), cvtpk(v[2], v[3]), cvtpk(v[4], v[5]), cvtpk(v[6], v[7])};
      const __amdgpu_buffer_rsrc_t rout = __builtin_amdgcn_make_buffer_rsrc((void*)out, (short)0, NB * SQ * DM * 2, 0x00020000);
      __builtin_amdgcn_raw_buffer_store_b128(o, rout, ((b * SQ + 96 * rh + 24 * hg + row) * DM + 144 * cq + 8 * ch) * 2, 0, 2);
    }
    if (tid == 0 && hd == 0) __hip_atomic_store(flags + (size_t)(512 + b) * 16, tag, __ATOMIC_RELAXED, __HIP_MEMORY_SCOPE_AGENT);
  }
}

extern "C" void kernel_launch(void* const* d_in, const int* in_sizes, int n_in, void* d_out, int out_size, void* d_ws, size_t ws_size,
                              hipStream_t stream) {
  const __hip_bfloat16* x = (const __hip_bfloat16*)d_in[0];
  const __hip_bfloat16* kc = (const __hip_bfloat16*)d_in[1];
  const __hip_bfloat16* vc = (const __hip_bfloat16*)d_in[2];
  const __hip_bfloat16* wq = (const __hip_bfloat16*)d_in[3];
  const __hip_bfloat16* wo = (const __hip_bfloat16*)d_in[4];
  mha_kernel<<<256, 768, 0, stream>>>(x, kc, vc, wq, wo, (__hip_bfloat16*)d_out, (char*)d_ws);
}
```

```cpp
#include <hip/hip_runtime.h>
#include <hip/hip_bf16.h>
#include <type_traits>

typedef __bf16 bf16x8 __attribute__((ext_vector_type(8)));
typedef __bf16 bf16x4 __attribute__((ext_vector_type(4)));
typedef __bf16 bf16x2 __attribute__((ext_vector_type(2)));
typedef float f32x16 __attribute__((ext_vector_type(16)));
typedef float f32x4 __attribute__((ext_vector_type(4)));
typedef float f32x2 __attribute__((ext_vector_type(2)));
typedef unsigned u32x4 __attribute__((ext_vector_type(4)));
typedef unsigned u32x2 __attribute__((ext_vector_type(2)));
typedef __attribute__((address_space(3))) char lds_char;
typedef __attribute__((address_space(3))) bf16x4 lds_bf16x4;
typedef __attribute__((address_space(3))) bf16x8 lds_bf16x8;
typedef __attribute__((address_space(3))) f32x4 lds_f32x4;
typedef __attribute__((address_space(3))) f32x2 lds_f32x2;
typedef __attribute__((address_space(1))) unsigned long long gu64;

#define DI __device__ __forceinline__
#define MFMA32(a, b, c) __builtin_amdgcn_mfma_f32_32x32x16_bf16((a), (b), (c), 0, 0, 0)
#define MFMA16(a, b, c) __builtin_amdgcn_mfma_f32_16x16x32_bf16((a), (b), (c), 0, 0, 0)
#define SB() __builtin_amdgcn_sched_barrier(0)
template <int V> using IC = std::integral_constant<int, V>;
#define SG_MFMA(n) __builtin_amdgcn_sched_group_barrier(0x008, (n), 0)
#define SG_DSRD(n) __builtin_amdgcn_sched_group_barrier(0x100, (n), 0)

constexpr int NB = 8, SQ = 192, DM = 576, NH = 32, SK = 512, DH = 128;
constexpr float QSCALE = 0.08838834764831845f * 1.4426950408889634f;
constexpr float THR = 8.0f;

constexpr unsigned CODE_BYTES = 28 * 1024;
constexpr size_t WS_O = 0;
constexpr size_t WS_PART = 16u << 20;
constexpr size_t WS_FLAG = 32u << 20;
constexpr unsigned PART_BLK = 24 * 144 * 2;
constexpr unsigned O_BYTES = NB * NH * 6 * 8 * 1024, PART_BYTES = 8 * 8 * 4 * 4 * PART_BLK;

constexpr unsigned QS_BYTES = 40960, QS_W = 24576;
constexpr unsigned SPARE = 155648, LDS_TOTAL = 163840, ML_OFF = 98304, VOTE_OFF = 101376;
constexpr float L_MIN = 9.313225746154785e-10f, L_MAX = 1048576.0f;
DI constexpr unsigned kv_slot(int s) { return (unsigned)((s + 3) & 3) * QS_BYTES; }
DI unsigned qblk(unsigned blk) { return blk < 40 ? 2 * QS_BYTES + blk * 1024 : SPARE + (blk - 40) * 1024; }
constexpr unsigned P2_OSLOT = 12288, P2_WSLOT = 18432, P2_WBASE = 53248, IMG_STRIDE = 304, IMG_BYTES = 96 * IMG_STRIDE;
DI unsigned p2_oslot(unsigned i) { return (i & 3) * P2_OSLOT; }
DI unsigned p2_wslot(unsigned i) { return P2_WBASE + ((i + 3) % 6) * P2_WSLOT; }

DI unsigned rfl(unsigned x) { return __builtin_amdgcn_readfirstlane(x); }
DI unsigned cvtpk(float lo, float hi) { f32x2 v = {lo, hi}; bf16x2 b = __builtin_convertvector(v, bf16x2); return __builtin_bit_cast(unsigned, b); }
DI bf16x8 pack8(const f32x16& x, int s) {
  u32x4 p = {cvtpk(x[8 * s], x[8 * s + 1]), cvtpk(x[8 * s + 2], x[8 * s + 3]), cvtpk(x[8 * s + 4], x[8 * s + 5]), cvtpk(x[8 * s + 6], x[8 * s + 7])};
  return __builtin_bit_cast(bf16x8, p);
}
DI bf16x8 lds_rd128(lds_char* base, unsigned off) { return *(lds_bf16x8*)(base + off); }
DI bf16x8 lds_tr2(lds_char* base, unsigned off0, unsigned off1) {
  bf16x4 lo = __builtin_amdgcn_ds_read_tr16_b64_v4bf16((lds_bf16x4*)(base + off0));
  bf16x4 hi = __builtin_amdgcn_ds_read_tr16_b64_v4bf16((lds_bf16x4*)(base + off1));
  return __builtin_shufflevector(lo, hi, 0, 1, 2, 3, 4, 5, 6, 7);
}
template <int POL> DI void dma16(__amdgpu_buffer_rsrc_t r, unsigned voff, unsigned soff, unsigned m0v) {
  if constexpr (POL == 0) asm volatile("s_mov_b32 m0, %3\n\ts_nop 0\n\tbuffer_load_dwordx4 %0, %1, %2 offen lds" ::"v"(voff), "s"(r), "s"(soff), "s"(m0v) : "memory");
  else if constexpr (POL == 1) asm volatile("s_mov_b32 m0, %3\n\ts_nop 0\n\tbuffer_load_dwordx4 %0, %1, %2 offen nt lds" ::"v"(voff), "s"(r), "s"(soff), "s"(m0v) : "memory");
  else asm volatile("s_mov_b32 m0, %3\n\ts_nop 0\n\tbuffer_load_dwordx4 %0, %1, %2 offen sc1 lds" ::"v"(voff), "s"(r), "s"(soff), "s"(m0v) : "memory");
}
template <int N> DI void wait_vm() { asm volatile("s_waitcnt vmcnt(%0)" ::"i"(N) : "memory"); }
DI void bar() { SB(); asm volatile("s_waitcnt lgkmcnt(0)\n\ts_barrier" ::: "memory"); SB(); }
DI float pl32_max(float v) {
  auto r = __builtin_amdgcn_permlane32_swap(__float_as_uint(v), __float_as_uint(v), false, false);
  return fmaxf(__uint_as_float(r[0]), __uint_as_float(r[1]));
}
DI float pl32_sum(float v) {
  auto r = __builtin_amdgcn_permlane32_swap(__float_as_uint(v), __float_as_uint(v), false, false);
  return __uint_as_float(r[0]) + __uint_as_float(r[1]);
}
DI void add8(float (&v)[8], const u32x4& u) {
#pragma unroll
  for (int e = 0; e < 4; ++e) { v[2 * e] += __uint_as_float(u[e] << 16); v[2 * e + 1] += __uint_as_float(u[e] & 0xffff0000u); }
}
DI unsigned lane_id() { unsigned l; asm volatile("v_mbcnt_lo_u32_b32 %0, -1, 0\n\tv_mbcnt_hi_u32_b32 %0, -1, %0" : "=v"(l)); return l; }
DI void poll_flags(gu64* flags, unsigned idx0, unsigned stride, unsigned n, unsigned skip, unsigned long long tag, unsigned lane) {
  const bool mine = lane < n && lane != skip;
  gu64* p = flags + (size_t)(idx0 + (mine ? lane : 0) * stride) * 16;
  for (unsigned spins = 0; spins < 400000u; ++spins) {
    const unsigned long long v = __hip_atomic_load(p, __ATOMIC_RELAXED, __HIP_MEMORY_SCOPE_AGENT);
    if (__all(!mine || v == tag)) break;
    __builtin_amdgcn_s_sleep(1);
  }
}

__global__ __launch_bounds__(768, 3) void mha_kernel(const __hip_bfloat16* __restrict__ x, const __hip_bfloat16* __restrict__ kc,
                                                     const __hip_bfloat16* __restrict__ vc, const __hip_bfloat16* __restrict__ wq,
                                                     const __hip_bfloat16* __restrict__ wo, __hip_bfloat16* __restrict__ out,
                                                     char* __restrict__ ws) {
  __shared__ __attribute__((aligned(1024))) char smem[LDS_TOTAL];
  lds_char* lds = (lds_char*)smem;
  const unsigned ldsb = (unsigned)(size_t)smem;
  const unsigned tid = threadIdx.x, w = rfl(tid >> 6);
  const unsigned xcd = blockIdx.x & 7, jj = blockIdx.x >> 3;
  const unsigned b = 4 * (xcd & 1) + (jj & 3), hd = 8 * (xcd >> 1) + (jj >> 2);
  gu64* flags = (gu64*)(ws + WS_FLAG);
  unsigned long long epoch;
  u32x2 ev;
  auto epoch_issue = [&]() { asm volatile("global_load_dwordx2 %0, %1, off sc1" : "=v"(ev) : "v"((const char*)(ws + WS_FLAG) + (512 + b) * 128) : "memory"); };
  auto epoch_take = [&]() {
    asm volatile("" : "+v"(ev));
    epoch = ((unsigned long long)rfl(ev[1]) << 32) | rfl(ev[0]);
  };
  const __amdgpu_buffer_rsrc_t rwo = __builtin_amdgcn_make_buffer_rsrc((void*)wo, (short)0, NH * DH * DM * 2, 0x00020000);
  bf16x8 qf[8];
  f32x16 oacc[4];
  float m, l;
  {
    const unsigned lane = tid & 63, r = lane & 31, h = lane >> 5;
    const unsigned t = w >> 1, j = w & 1;
    const unsigned g1 = (lane >> 4) & 1, qq = (lane & 15) >> 2, pp = lane & 3, sp = ((pp & 1) << 1) | (pp >> 1);
    const __amdgpu_buffer_rsrc_t rk = __builtin_amdgcn_make_buffer_rsrc((void*)kc, (short)0, NB * NH * SK * DH * 2, 0x00020000);
    const __amdgpu_buffer_rsrc_t rv = __builtin_amdgcn_make_buffer_rsrc((void*)vc, (short)0, NB * NH * SK * DH * 2, 0x00020000);
    const unsigned skv = (b * NH + hd) * (SK * DH * 2);
    const unsigned vk = (lane >> 4) * 256 + ((lane & 15) ^ (lane >> 4)) * 16;
    const unsigned vv = (lane >> 4) * 256 + ((lane & 15) ^ ((lane >> 4) << 2)) * 16;

    auto kv4 = [&](int s) {
      const unsigned lw = w - 4;
      const unsigned dst = ldsb + kv_slot(s) + lw * 4096u;
      const unsigned so = skv + ((lw >> 1) & 1) * 65536u + (lw & 1) * 4096u + (unsigned)s * 8192u;
#pragma unroll
      for (int p = 0; p < 4; ++p) {
        if (lw < 4) dma16<1>(rk, vk ^ (p << 6), so + p * 1024u, dst + p * 1024u);
        else dma16<1>(rv, vv, so + p * 1024u, dst + p * 1024u);
      }
    };
    const unsigned koff = 3 * (((jj >> 2) + (jj & 3)) % 3);
    if (w >= 4) {
      const unsigned lw = w - 4;
      const __amdgpu_buffer_rsrc_t rx = __builtin_amdgcn_make_buffer_rsrc((void*)x, (short)0, NB * SQ * DM * 2, 0x00020000);
      const __amdgpu_buffer_rsrc_t rw = __builtin_amdgcn_make_buffer_rsrc((void*)wq, (short)0, DM * NH * DH * 2, 0x00020000);
      const unsigned vx = (lane >> 3) * 1152 + (((lane & 7) ^ (lane >> 4) ^ ((lw & 1) << 2)) * 16);
      const unsigned sx = (b * SQ + 8 * lw) * 1152u;
      const unsigned vw = (lane >> 4) * 8192 + (((lane & 15) ^ ((lane >> 4) << 2)) * 16);
      const unsigned sw = 4 * lw * 8192u + hd * 256u;
      auto issue_q = [&](int i) {
        unsigned st = koff + i; st = st >= 9 ? st - 9 : st;
        const unsigned slot = ldsb + (unsigned)(i % 3) * QS_BYTES;
#pragma unroll
        for (int p = 0; p < 3; ++p) dma16<0>(rx, vx, sx + (unsigned)(64 * p) * 1152u + st * 128u, slot + (lw + 8 * p) * 1024u);
#pragma unroll
        for (int p = 0; p < 2; ++p) dma16<0>(rw, vw, sw + (st * 64 + 32 * p) * 8192u, slot + QS_W + (lw + 8 * p) * 1024u);
      };
      epoch_issue();
      issue_q(0); issue_q(1);
      wait_vm<5>(); epoch_take(); bar(); issue_q(2);
      wait_vm<5>(); bar(); issue_q(3);
      wait_vm<5>(); bar(); issue_q(4);
      wait_vm<5>(); bar(); issue_q(5);
      wait_vm<5>(); bar(); issue_q(6); kv4(0);
      wait_vm<9>(); bar(); issue_q(7);
      wait_vm<9>(); bar(); issue_q(8);
      wait_vm<5>(); bar(); kv4(1);
      wait_vm<4>(); bar(); kv4(2);
      bar();
      bar();
    } else {
      epoch_issue(); wait_vm<0>(); epoch_take();
      if (w < 2) {
        unsigned long long pc;
        asm volatile("s_getpc_b64 %0" : "=s"(pc));
        const unsigned off = (jj * 2 + w) * 1024u;
        if (off + 1024u <= CODE_BYTES) {
          u32x4 t;
          const char* p = (const char*)(pc & ~127ull) + off + lane * 16;
          asm volatile("global_load_dwordx4 %0, %1, off\n\ts_waitcnt vmcnt(0)" : "=v"(t) : "v"(p) : "memory");
        }
      }
      if (w == 2) {
        const unsigned hg_ = hd >> 3, hl_ = hd & 7, wg_ = b * NH + hd;
        const char* p = (const char*)ws + WS_FLAG;
        if (lane == 0) p = (const char*)kc + (size_t)wg_ * (SK * DH * 2);
        if (lane == 1) p = (const char*)vc + (size_t)wg_ * (SK * DH * 2);
        if (lane == 2) p = (const char*)ws + WS_O + (size_t)wg_ * 49152;
        if (lane == 3) p = (const char*)ws + WS_O + (size_t)(b * NH + 8 * hg_) * 49152;
        if (lane == 4) p = (const char*)ws + WS_O + (size_t)(b * NH + 8 * hg_ + 8) * 49152 - 64;
        if (lane >= 5 && lane < 9) p = (const char*)ws + WS_PART + (size_t)(((b * 8 + hl_) * 4 + (lane - 5)) * 4) * PART_BLK;
        if (lane == 9) p = (const char*)wo + (size_t)hg_ * (1024 * 1152);
        if (lane == 10) p = (const char*)wo + (size_t)(hg_ + 1) * (1024 * 1152) - 64;
        if (lane == 11) p = (const char*)out + (size_t)(b * SQ) * (DM * 2);
        if (lane == 12) p = (const char*)kc + (size_t)(wg_ + 1) * (SK * DH * 2) - 64;
        if (lane == 13) p = (const char*)vc + (size_t)(wg_ + 1) * (SK * DH * 2) - 64;
        unsigned t;
        asm volatile("global_load_dword %0, %1, off\n\ts_waitcnt vmcnt(0)" : "=v"(t) : "v"(p) : "memory");
      }
      const unsigned qh = w >> 1, dh = w & 1;
      const unsigned xa0 = (96 * qh + r) * 128 + ((h ^ ((r >> 1) & 7)) << 4);
      const unsigned wa0 = QS_W + (8 * h + qq) * 256 + ((4 * ((2 * dh) ^ qq) + 2 * g1 + (sp >> 1)) << 4) + (sp & 1) * 8;
      f32x16 qacc[3][2];
#pragma unroll
      for (int a = 0; a < 3; ++a)
#pragma unroll
        for (int i = 0; i < 16; ++i) { qacc[a][0][i] = 0.f; qacc[a][1][i] = 0.f; }
      bf16x8 bA[3], aA[2], bB[3], aB[2];
      unsigned xb, wb;
      auto rd = [&](bf16x8 (&bb)[3], bf16x8 (&aa)[2], int s) {
        const unsigned xs = xb ^ (s << 5), wb1 = wb ^ 64;
        aa[0] = lds_tr2(lds, wb + s * 4096, wb + s * 4096 + 1024);
        bb[0] = lds_rd128(lds, xs); bb[1] = lds_rd128(lds, xs + 4096); bb[2] = lds_rd128(lds, xs + 8192);
        aa[1] = lds_tr2(lds, wb1 + s * 4096, wb1 + s * 4096 + 1024);
      };
      auto mm = [&](bf16x8 (&bb)[3], bf16x8 (&aa)[2]) {
        qacc[0][0] = MFMA32(aa[0], bb[0], qacc[0][0]);
        qacc[1][0] = MFMA32(aa[0], bb[1], qacc[1][0]);
        qacc[2][0] = MFMA32(aa[0], bb[2], qacc[2][0]);
        qacc[0][1] = MFMA32(aa[1], bb[0], qacc[0][1]);
        qacc[1][1] = MFMA32(aa[1], bb[1], qacc[1][1]);
        qacc[2][1] = MFMA32(aa[1], bb[2], qacc[2][1]);
      };
      bar();
      xb = xa0; wb = wa0;
      asm volatile("" : "+v"(xb), "+v"(wb));
      rd(bA, aA, 0);
      auto mix = [&]() { SG_MFMA(1); SG_DSRD(2); SG_MFMA(1); SG_DSRD(2); SG_MFMA(1); SG_DSRD(1); SG_MFMA(1); SG_DSRD(1); SG_MFMA(1); SG_DSRD(1); SG_MFMA(1); SB(); };
#pragma unroll 1
      for (int st = 0; st < 8; ++st) {
        SB(); rd(bB, aB, 1); mm(bA, aA); mix();
        rd(bA, aA, 2); mm(bB, aB); mix();
        rd(bB, aB, 3); mm(bA, aA); mix();
        bar();
        const unsigned so = (unsigned)((st + 1) % 3) * QS_BYTES;
        xb = xa0 + so; wb = wa0 + so;
        asm volatile("" : "+v"(xb), "+v"(wb));
        rd(bA, aA, 0); mm(bB, aB); mix();
      }
      SB(); rd(bB, aB, 1); mm(bA, aA); mix();
      rd(bA, aA, 2); mm(bB, aB); mix();
      rd(bB, aB, 3); mm(bA, aA); mix();
      mm(bB, aB);
      bar();
#pragma unroll
      for (int a = 0; a < 3; ++a)
#pragma unroll
        for (int d = 0; d < 2; ++d) {
#pragma unroll
          for (int i = 0; i < 16; ++i) qacc[a][d][i] *= QSCALE;
#pragma unroll
          for (int s2 = 0; s2 < 2; ++s2) {
            const unsigned blk = (3 * qh + a) * 8 + 4 * dh + 2 * d + s2;
            *(lds_bf16x8*)(lds + qblk(blk) + lane * 16) = pack8(qacc[a][d], s2);
          }
        }
      bar();
    }
#pragma unroll
    for (int i = 0; i < 8; ++i) qf[i] = lds_rd128(lds, qblk(t * 8 + i) + lane * 16);

    const unsigned grp = w >> 2;
    float l0, l1;
    bool safe = false;
    const unsigned ka0 = j * 8192 + r * 256 + ((h ^ (r & 15)) << 4);
    const unsigned va0 = (16384 + j * 8192 + (4 * h + qq) * 256 + ((4 * qq + 2 * g1 + (sp >> 1)) << 4) + (sp & 1) * 8) ^ (j << 7);
    const unsigned vkx = vk ^ ((w & 3) << 6);
    const unsigned sp0 = skv + ((w >> 3) & 1) * 65536u + (w & 7) * 1024u;
    const unsigned sp1 = skv + (((w + 12) >> 3) & 1) * 65536u + ((w + 12) & 7) * 1024u;
    const unsigned sp2 = skv + (((w + 24) >> 3) & 1) * 65536u + ((w + 24) & 7) * 1024u;

    bf16x8 kf[4];
    auto step = [&](auto G_, int s) {
      constexpr int G = decltype(G_)::value;
      constexpr int WGP = 2 - G;
      constexpr int NP = WGP == 2 ? 2 : 3;
      unsigned vb = va0 + kv_slot(s), kc0 = ka0 + kv_slot(s), kb = ka0 + kv_slot(s + 1);
      asm volatile("" : "+v"(kb), "+v"(vb), "+v"(kc0));
      __builtin_amdgcn_s_setprio(1);
      f32x16 sc;
      bf16x8 vf[4][2];
      {
        bf16x8 kg[4];
#pragma unroll
        for (int ks = 0; ks < 4; ++ks) kg[ks] = lds_rd128(lds, kc0 ^ ((ks + 4) << 5));
#pragma unroll
        for (int i = 0; i < 16; ++i) sc[i] = 0.f;
#pragma unroll
        for (int ks = 0; ks < 4; ++ks) sc = MFMA32(kf[ks], qf[ks], sc);
#pragma unroll
        for (int d = 0; d < 4; ++d) { const unsigned va = vb ^ (d << 6); vf[d][0] = lds_tr2(lds, va, va + 2048); }
#pragma unroll
        for (int ks = 0; ks < 4; ++ks) sc = MFMA32(kg[ks], qf[ks + 4], sc);
        SG_MFMA(1); SG_DSRD(2); SG_MFMA(1); SG_DSRD(2); SG_MFMA(1); SG_DSRD(2); SG_MFMA(1); SG_DSRD(2);
        SG_MFMA(1); SG_DSRD(2); SG_MFMA(1); SG_DSRD(2); SG_MFMA(2);
        SB();
        asm volatile("" : "+v"(sc));
      }
      __builtin_amdgcn_s_setprio(0);
      if constexpr (G == 1) wait_vm<NP>();
      bar();
#pragma unroll
      for (int d = 0; d < 4; ++d) { const unsigned va = vb ^ (d << 6); vf[d][1] = lds_tr2(lds, va + 4096, va + 4096 + 2048); }
      SB();
      if (safe) {
        const float t0 = fmaxf(fmaxf(sc[0], sc[1]), sc[2]), t1 = fmaxf(fmaxf(sc[3], sc[4]), sc[5]), t2 = fmaxf(fmaxf(sc[6], sc[7]), sc[8]);
        const float t3 = fmaxf(fmaxf(sc[9], sc[10]), sc[11]), t4 = fmaxf(fmaxf(sc[12], sc[13]), sc[14]);
        float tm = fmaxf(fmaxf(fmaxf(t0, t1), t2), fmaxf(fmaxf(t3, t4), sc[15]));
        tm = pl32_max(tm);
        if (__any(tm > m + THR)) {
          const float mn = fmaxf(m, tm);
          const float al = __builtin_amdgcn_exp2f(m - mn);
          l0 *= al; l1 *= al; m = mn;
#pragma unroll
          for (int d = 0; d < 4; ++d)
#pragma unroll
            for (int i = 0; i < 16; ++i) oacc[d][i] *= al;
        }
#pragma unroll
        for (int i = 0; i < 16; ++i) sc[i] -= m;
      }
      SB();
#pragma unroll
      for (int i = 0; i < 16; ++i) sc[i] = __builtin_amdgcn_exp2f(sc[i]);
      bf16x8 p0 = pack8(sc, 0), p1 = pack8(sc, 1);
#pragma unroll
      for (int i = 0; i < 16; i += 2) { l0 += sc[i]; l1 += sc[i + 1]; }
      asm volatile("" : "+v"(p0), "+v"(p1), "+v"(l0), "+v"(l1));
      if constexpr (G == 0) wait_vm<NP>();
      bar();
      __builtin_amdgcn_s_setprio(1);
#pragma unroll
      for (int d = 0; d < 4; ++d) oacc[d] = MFMA32(vf[d][0], p0, oacc[d]);
#pragma unroll
      for (int ks = 0; ks < 4; ++ks) kf[ks] = lds_rd128(lds, kb ^ (ks << 5));
      SG_MFMA(2); SG_DSRD(2); SG_MFMA(1); SG_DSRD(1); SG_MFMA(1); SG_DSRD(1);
      SB();
      if (s <= 4) {
        const unsigned dst = ldsb + kv_slot(s + 3) + w * 1024u, sofs = (unsigned)(s + 3) * 8192u;
        dma16<1>(rk, vkx, sp0 + sofs, dst);
        if constexpr (WGP == 0) dma16<1>(rk, vkx, sp1 + sofs, dst + 12288u); else dma16<1>(rv, vv, sp1 + sofs, dst + 12288u);
        if constexpr (WGP != 2) dma16<1>(rv, vv, sp2 + sofs, dst + 24576u);
      } else if (s == 5) {
        const unsigned pc = ((b & 3) * 8 + (hd & 7)) * 36 + (WGP == 2 ? 24 + 2 * (w - 8) : 3 * w);
        const unsigned so = (hd >> 3) * (1024u * 1152u) + pc * 1024u, vo = lane_id() * 16;
#pragma unroll
        for (int p = 0; p < NP; ++p) dma16<0>(rwo, vo, so + p * 1024u, ldsb + SPARE + (w & 7) * 1024u);
      }
      SB();
#pragma unroll
      for (int d = 0; d < 4; ++d) oacc[d] = MFMA32(vf[d][1], p1, oacc[d]);
      SB();
      asm volatile("" : "+v"(oacc[0]), "+v"(oacc[1]), "+v"(oacc[2]), "+v"(oacc[3]));
      __builtin_amdgcn_s_setprio(0);
      if constexpr (G == 2) wait_vm<NP>();
      bar();
    };
    auto run = [&](auto G_) {
      constexpr int G = decltype(G_)::value;
      {
        unsigned kb0 = ka0 + kv_slot(0);
        asm volatile("" : "+v"(kb0));
#pragma unroll
        for (int ks = 0; ks < 4; ++ks) kf[ks] = lds_rd128(lds, kb0 ^ (ks << 5));
      }
      if constexpr (G >= 1) bar();
      if constexpr (G == 2) bar();
#pragma unroll 1
      for (int s = 0; s < 8; ++s) step(G_, s);
      if constexpr (G == 0) {
        const unsigned pc = ((b & 3) * 8 + (hd & 7)) * 36 + 32 + (w & 3);
        dma16<0>(rwo, lane_id() * 16, (hd >> 3) * (1024u * 1152u) + pc * 1024u, ldsb + SPARE + (w & 3) * 1024u);
      }
      if constexpr (G == 0) bar();
      if constexpr (G <= 1) bar();
    };
#pragma unroll 1
    for (;;) {
#pragma unroll
      for (int d = 0; d < 4; ++d)
#pragma unroll
        for (int i = 0; i < 16; ++i) oacc[d][i] = 0.f;
      m = safe ? -__builtin_inff() : 0.f; l0 = 0.f; l1 = 0.f;
      bar();
      if (grp == 2) run(IC<0>{});
      else if (grp == 1) run(IC<1>{});
      else run(IC<2>{});
      const unsigned ln = lane_id();
      l = pl32_sum(l0 + l1);
      const unsigned xw = w * 4096 + ln * 16;
#pragma unroll
      for (int i = 0; i < 4; ++i) *(lds_bf16x8*)(lds + xw + i * 1024) = pack8(oacc[2 + (i >> 1)], i & 1);
      if (ln < 32) { f32x2 ml = {m, l}; *(lds_f32x2*)(lds + ML_OFF + w * 256 + ln * 8) = ml; }
      const bool bad = __any(!(l > L_MIN && l < L_MAX));
      if (ln == 0) *(__attribute__((address_space(3))) unsigned*)(lds + VOTE_OFF + w * 4) = bad ? 1u : 0u;
      bar();
      if (safe) break;
      const unsigned vote = *(__attribute__((address_space(3))) unsigned*)(lds + VOTE_OFF + (ln < 12 ? ln : 0) * 4);
      if (!__any(vote != 0)) break;
      safe = true;
      bar();
      if (w >= 4) {
        const unsigned lw = rfl(w - 4), sk = rfl((b * NH + hd) * (SK * DH * 2));
        const unsigned vo = (ln >> 4) * 256 + (lw < 4 ? ((ln & 15) ^ (ln >> 4)) : ((ln & 15) ^ ((ln >> 4) << 2))) * 16;
        const __amdgpu_buffer_rsrc_t rr = __builtin_amdgcn_make_buffer_rsrc((void*)(lw < 4 ? kc : vc), (short)0, NB * NH * SK * DH * 2, 0x00020000);
#pragma unroll 1
        for (unsigned q = 0; q < 12; ++q) {
          const unsigned st = q >> 2, p = q & 3;
          dma16<1>(rr, lw < 4 ? vo ^ (p << 6) : vo, rfl(sk + ((lw >> 1) & 1) * 65536u + (lw & 1) * 4096u + st * 8192u + p * 1024u), rfl(ldsb + kv_slot(st) + lw * 4096u + p * 1024u));
        }
        wait_vm<0>();
      }
    }
  }

  const unsigned lane = lane_id();
  const unsigned hg = hd >> 3, hl = hd & 7, rh = hl & 1, cq = hl >> 1;
  const unsigned long long tag = epoch + 1;
  const unsigned wg = b * NH + hd;
  auto wo_voff = [&](int i) {
    const unsigned n = 64 * i + lane, p = n / 18, ch = n - 18 * p;
    const unsigned k = (p & 19) | ((p & 4) << 1) | ((p & 8) >> 1);
    return k * 1152 + ch * 16;
  };
  auto wo_issue = [&](const unsigned (&vo)[9], unsigned i) {
    const unsigned kh = w & 1, hh = (hl + (i >> 1)) & 7;
    const unsigned so = ((8 * hg + hh) * 128 + (2 * (i & 1) + kh) * 32) * 1152u + cq * 288u;
    const unsigned dst = ldsb + p2_wslot(i) + kh * 9216u;
#pragma unroll
    for (int p = 0; p < 9; ++p) dma16<0>(rwo, vo[p], so, dst + p * 1024u);
  };
  unsigned wvo[9];
  if (w == 8 || w == 9) {
#pragma unroll
    for (int p = 0; p < 9; ++p) wvo[p] = wo_voff(p);
  }

  {
    const unsigned r = lane & 31, t = w >> 1, j = w & 1;
    f32x2 o = *(lds_f32x2*)(lds + ML_OFF + (w ^ 1) * 256 + r * 8);
    const float mn = fmaxf(m, o[0]);
    const float f = __builtin_amdgcn_exp2f(m - mn), fo = __builtin_amdgcn_exp2f(o[0] - mn);
    const float inv = 1.0f / (l * f + o[1] * fo);
    const float sa = f * inv, sb = fo * inv;
    const unsigned xr = (w ^ 1) * 4096 + lane * 16;
#pragma unroll
    for (int i = 0; i < 4; ++i) {
      const u32x4 v = __builtin_bit_cast(u32x4, lds_rd128(lds, xr + i * 1024));
#pragma unroll
      for (int e = 0; e < 4; ++e) {
        oacc[i >> 1][8 * (i & 1) + 2 * e] = oacc[i >> 1][8 * (i & 1) + 2 * e] * sa + __uint_as_float(v[e] << 16) * sb;
        oacc[i >> 1][8 * (i & 1) + 2 * e + 1] = oacc[i >> 1][8 * (i & 1) + 2 * e + 1] * sa + __uint_as_float(v[e] & 0xffff0000u) * sb;
      }
    }
    const __amdgpu_buffer_rsrc_t ro = __builtin_amdgcn_make_buffer_rsrc((void*)(ws + WS_O), (short)0, O_BYTES, 0x00020000);
    const unsigned so = ((wg * 6 + t) * 8 + 4 * j) * 1024u;
#pragma unroll
    for (int i = 0; i < 4; ++i)
      __builtin_amdgcn_raw_buffer_store_b128(__builtin_bit_cast(u32x4, pack8(oacc[i >> 1], i & 1)), ro, so + lane * 16 + i * 1024, 0, 16);
    if (w == 8 || w == 9) { wo_issue(wvo, 0); wo_issue(wvo, 1); wait_vm<18>(); } else wait_vm<0>();
    bar();
    if (tid == 0) __hip_atomic_store(flags + (size_t)wg * 16, tag, __ATOMIC_RELAXED, __HIP_MEMORY_SCOPE_AGENT);
  }
  f32x4 acc[3][5];
#pragma unroll
  for (int a = 0; a < 3; ++a)
#pragma unroll
    for (int n = 0; n < 5; ++n) acc[a][n] = f32x4{0.f, 0.f, 0.f, 0.f};
  const unsigned nh = (w >> 2) & 1, kh = w & 1, qh = (w >> 1) & 1;
  if (w >= 10) {
    const __amdgpu_buffer_rsrc_t ro = __builtin_amdgcn_make_buffer_rsrc((void*)(ws + WS_O), (short)0, O_BYTES, 0x00020000);
    auto o_issue = [&](unsigned i) {
      const unsigned hh = (hl + (i >> 1)) & 7;
      const unsigned so = (((b * NH + 8 * hg + hh) * 6 + 3 * rh) * 8 + 2 * (2 * (i & 1) + kh)) * 1024u;
      const unsigned dst = ldsb + p2_oslot(i) + kh * 6144u;
#pragma unroll
      for (int q = 0; q < 3; ++q) { dma16<2>(ro, lane * 16, so + q * 8192u, dst + q * 2048u); dma16<2>(ro, lane * 16, so + q * 8192u + 1024u, dst + q * 2048u + 1024u); }
    };
    gu64* fp = flags + (size_t)(b * NH + 8 * hg + (lane & 7)) * 16;
    unsigned long long pv;
    auto poll_issue = [&]() { asm volatile("global_load_dwordx2 %0, %1, off sc1" : "=v"(pv) : "v"(fp) : "memory"); };
    unsigned rdy = 1u << hl;
    auto poll_take = [&]() {
      asm volatile("s_waitcnt vmcnt(0)" : "+v"(pv) :: "memory");
      rdy |= (unsigned)__ballot(pv == tag) & 0xffu;
    };
    o_issue(0); o_issue(1);
    poll_issue();
    bool pending = true;
    unsigned spins = 0;
#pragma unroll 1
    for (unsigned i = 0; i < 16; ++i) {
      if (pending) wait_vm<7>(); else if (i <= 14) wait_vm<6>(); else wait_vm<0>();
      bar();
      if (i + 2 < 16) {
        const unsigned hh = (hl + ((i + 2) >> 1)) & 7;
        if (pending) { poll_take(); pending = false; }
        while (!((rdy >> hh) & 1) && spins < 400000u) { __builtin_amdgcn_s_sleep(1); poll_issue(); poll_take(); ++spins; }
        o_issue(i + 2);
      }
    }
  } else if (w >= 8) {
    wo_issue(wvo, 2); wo_issue(wvo, 3); wo_issue(wvo, 4);
#pragma unroll 1
    for (unsigned i = 0; i < 16; ++i) {
      if (i <= 11) wait_vm<36>(); else if (i == 12) wait_vm<27>(); else if (i == 13) wait_vm<18>(); else if (i == 14) wait_vm<9>(); else wait_vm<0>();
      bar();
      if (i + 5 < 16) wo_issue(wvo, i + 5);
    }
  } else {
    const unsigned c = lane & 15, g = lane >> 4;
    unsigned ba[3];
#pragma unroll
    for (int a = 0; a < 3; ++a) {
      const unsigned qi = 3 * qh + a;
      ba[a] = kh * 6144 + ((qi >> 1) * 2 + (g >> 1)) * 1024 + (16 * (qi & 1) + c + 32 * (g & 1)) * 16;
    }
    const unsigned aa = kh * 9216 + ((g >> 1) * 16 + (g & 1) * 4 + (c >> 2)) * 288 + 5 * nh * 32 + (c & 3) * 8;
    bf16x8 bX[3], aX[5], bY[3], aY[5];
    auto rd = [&](auto NT_, bf16x8 (&bb)[3], bf16x8 (&av)[5], unsigned i) {
      constexpr int NT = decltype(NT_)::value;
      unsigned ob = p2_oslot(i), wb = aa + p2_wslot(i);
      asm volatile("" : "+v"(wb));
#pragma unroll
      for (int a = 0; a < 3; ++a) bb[a] = lds_rd128(lds, ba[a] + ob);
#pragma unroll
      for (int n = 0; n < NT; ++n) av[n] = lds_tr2(lds, wb + n * 32, wb + n * 32 + 2304);
    };
    auto mm = [&](auto NT_, bf16x8 (&bb)[3], bf16x8 (&av)[5]) {
      constexpr int NT = decltype(NT_)::value;
#pragma unroll
      for (int n = 0; n < NT; ++n)
#pragma unroll
        for (int a = 0; a < 3; ++a) acc[a][n] = MFMA16(av[n], bb[a], acc[a][n]);
    };
    auto mix = [&](auto NT_) {
      constexpr int NT = decltype(NT_)::value;
#pragma unroll
      for (int k = 0; k < 3 + 2 * NT; ++k) {
        __builtin_amdgcn_sched_group_barrier(0x008, 1, 0);
        __builtin_amdgcn_sched_group_barrier(0x100, 1, 0);
      }
      __builtin_amdgcn_sched_group_barrier(0x008, 3 * NT, 0);
    };
    auto gemm = [&](auto NT_) {
      bar();
      rd(NT_, bX, aX, 0);
#pragma unroll 1
      for (unsigned i = 1; i < 15; i += 2) {
        bar(); rd(NT_, bY, aY, i); mm(NT_, bX, aX); mix(NT_);
        bar(); rd(NT_, bX, aX, i + 1); mm(NT_, bY, aY); mix(NT_);
      }
      bar(); rd(NT_, bY, aY, 15); mm(NT_, bX, aX); mix(NT_);
      SB(); mm(NT_, bY, aY);
    };
    if (nh == 0) gemm(IC<5>{}); else gemm(IC<4>{});
  }
  bar();
  if (w < 8) {
    const unsigned c = lane & 15, g = lane >> 4;
    const unsigned base = kh * IMG_BYTES + (48 * qh + c) * IMG_STRIDE + (80 * nh + 4 * g) * 2;
#pragma unroll
    for (int a = 0; a < 3; ++a)
#pragma unroll
      for (int n = 0; n < 5; ++n)
        if (n < 4 || nh == 0) {
          const u32x2 pk = {cvtpk(acc[a][n][0], acc[a][n][1]), cvtpk(acc[a][n][2], acc[a][n][3])};
          *(__attribute__((address_space(3))) u32x2*)(lds + base + a * 16 * IMG_STRIDE + n * 32) = pk;
        }
  }
  bar();
  {
    const __amdgpu_buffer_rsrc_t rp = __builtin_amdgcn_make_buffer_rsrc((void*)(ws + WS_PART), (short)0, PART_BYTES, 0x00020000);
    const unsigned pbase = ((b * 8 + hl) * 4 + hg) * 4 * PART_BLK;
#pragma unroll 1
    for (unsigned n = tid; n < 72 * 18; n += 768) {
      const unsigned rr = n / 18, ch = n - rr * 18;
      unsigned dst = rr / 24; const unsigned row = rr - dst * 24;
      dst += dst >= hg ? 1 : 0;
      const unsigned a0 = (24 * dst + row) * IMG_STRIDE + ch * 16;
      float v[8] = {0.f, 0.f, 0.f, 0.f, 0.f, 0.f, 0.f, 0.f};
      add8(v, __builtin_bit_cast(u32x4, lds_rd128(lds, a0))); add8(v, __builtin_bit_cast(u32x4, lds_rd128(lds, IMG_BYTES + a0)));
      const u32x4 pk = {cvtpk(v[0], v[1]), cvtpk(v[2], v[3]), cvtpk(v[4], v[5]), cvtpk(v[6], v[7])};
      __builtin_amdgcn_raw_buffer_store_b128(pk, rp, dst * PART_BLK + row * 288 + ch * 16 + pbase, 0, 16);
    }
    wait_vm<0>();
    bar();
    if (tid == 0) __hip_atomic_store(flags + (size_t)(256 + wg) * 16, tag, __ATOMIC_RELAXED, __HIP_MEMORY_SCOPE_AGENT);
    if (w == 0) poll_flags(flags, 256 + b * NH + hl, 8, 4, hg, tag, lane);
    bar();
    if (tid < 24 * 18) {
      const unsigned row = tid / 18, ch = tid - row * 18;
      u32x4 u[3];
#pragma unroll
      for (int k = 0; k < 3; ++k) {
        const unsigned src = k + (k >= (int)hg ? 1 : 0);
        u[k] = __builtin_amdgcn_raw_buffer_load_b128(rp, (((b * 8 + hl) * 4 + src) * 4 + hg) * PART_BLK + row * 288 + ch * 16, 0, 16);
      }
      const unsigned a0 = (24 * hg + row) * IMG_STRIDE + ch * 16;
      float v[8] = {0.f, 0.f, 0.f, 0.f, 0.f, 0.f, 0.f, 0.f};
      add8(v, __builtin_bit_cast(u32x4, lds_rd128(lds, a0))); add8(v, __builtin_bit_cast(u32x4, lds_rd128(lds, IMG_BYTES + a0)));
#pragma unroll
      for (int k = 0; k < 3; ++k) add8(v, u[k]);
      const u32x4 o = {cvtpk(v[0], v[1]), cvtpk(v[2], v[3]), cvtpk(v[4], v[5]), cvtpk(v[6], v[7])};
      const __amdgpu_buffer_rsrc_t rout = __builtin_amdgcn_make_buffer_rsrc((void*)out, (short)0, NB * SQ * DM * 2, 0x00020000);
      __builtin_amdgcn_raw_buffer_store_b128(o, rout, ((b * SQ + 96 * rh + 24 * hg + row) * DM + 144 * cq + 8 * ch) * 2, 0, 2);
    }
    if (tid == 0 && hd == 0) __hip_atomic_store(flags + (size_t)(512 + b) * 16, tag, __ATOMIC_RELAXED, __HIP_MEMORY_SCOPE_AGENT);
  }
}

extern "C" void kernel_launch(void* const* d_in, const int* in_sizes, int n_in, void* d_out, int out_size, void* d_ws, size_t ws_size,
                              hipStream_t stream) {
  const __hip_bfloat16* x = (const __hip_bfloat16*)d_in[0];
  const __hip_bfloat16* kc = (const __hip_bfloat16*)d_in[1];
  const __hip_bfloat16* vc = (const __hip_bfloat16*)d_in[2];
  const __hip_bfloat16* wq = (const __hip_bfloat16*)d_in[3];
  const __hip_bfloat16* wo = (const __hip_bfloat16*)d_in[4];
  mha_kernel<<<256, 768, 0, stream>>>(x, kc, vc, wq, wo, (__hip_bfloat16*)d_out, (char*)d_ws);
}
```

```cpp
#include <hip/hip_runtime.h>
#include <hip/hip_bf16.h>
#include <type_traits>

typedef __bf16 bf16x8 __attribute__((ext_vector_type(8)));
typedef __bf16 bf16x4 __attribute__((ext_vector_type(4)));
typedef __bf16 bf16x2 __attribute__((ext_vector_type(2)));
typedef float f32x16 __attribute__((ext_vector_type(16)));
typedef float f32x4 __attribute__((ext_vector_type(4)));
typedef float f32x2 __attribute__((ext_vector_type(2)));
typedef unsigned u32x4 __attribute__((ext_vector_type(4)));
typedef unsigned u32x2 __attribute__((ext_vector_type(2)));
typedef __attribute__((address_space(3))) char lds_char;
typedef __attribute__((address_space(3))) bf16x4 lds_bf16x4;
typedef __attribute__((address_space(3))) bf16x8 lds_bf16x8;
typedef __attribute__((address_space(3))) f32x4 lds_f32x4;
typedef __attribute__((address_space(3))) f32x2 lds_f32x2;
typedef __attribute__((address_space(1))) unsigned long long gu64;

#define DI __device__ __forceinline__
#define MFMA32(a, b, c) __builtin_amdgcn_mfma_f32_32x32x16_bf16((a), (b), (c), 0, 0, 0)
#define MFMA16(a, b, c) __builtin_amdgcn_mfma_f32_16x16x32_bf16((a), (b), (c), 0, 0, 0)
#define SB() __builtin_amdgcn_sched_barrier(0)
template <int V> using IC = std::integral_constant<int, V>;
#define SG_MFMA(n) __builtin_amdgcn_sched_group_barrier(0x008, (n), 0)
#define SG_DSRD(n) __builtin_amdgcn_sched_group_barrier(0x100, (n), 0)

constexpr int NB = 8, SQ = 192, DM = 576, NH = 32, SK = 512, DH = 128;
constexpr float QSCALE = 0.08838834764831845f * 1.4426950408889634f;
constexpr float THR = 8.0f;

constexpr unsigned CODE_BYTES = 28 * 1024;
constexpr size_t WS_O = 0;
constexpr size_t WS_PART = 16u << 20;
constexpr size_t WS_FLAG = 32u << 20;
constexpr unsigned PART_BLK = 24 * 144 * 2;
constexpr unsigned O_BYTES = NB * NH * 6 * 8 * 1024, PART_BYTES = 8 * 8 * 4 * 4 * PART_BLK;

constexpr unsigned QS_BYTES = 40960, QS_W = 24576;
constexpr unsigned SPARE = 155648, LDS_TOTAL = 163840, ML_OFF = 98304, VOTE_OFF = 101376;
constexpr float L_MIN = 9.313225746154785e-10f, L_MAX = 1048576.0f;
DI constexpr unsigned kv_slot(int s) { return (unsigned)((s + 3) & 3) * QS_BYTES; }
DI unsigned qblk(unsigned blk) { return blk < 40 ? 2 * QS_BYTES + blk * 1024 : SPARE + (blk - 40) * 1024; }
constexpr unsigned P2_OSLOT = 12288, P2_WSLOT = 18432, P2_WBASE = 53248, IMG_STRIDE = 304, IMG_BYTES = 96 * IMG_STRIDE;
DI unsigned p2_oslot(unsigned i) { return (i & 3) * P2_OSLOT; }
DI unsigned p2_wslot(unsigned i) { return P2_WBASE + ((i + 3) % 6) * P2_WSLOT; }

DI unsigned rfl(unsigned x) { return __builtin_amdgcn_readfirstlane(x); }
DI unsigned cvtpk(float lo, float hi) { f32x2 v = {lo, hi}; bf16x2 b = __builtin_convertvector(v, bf16x2); return __builtin_bit_cast(unsigned, b); }
DI bf16x8 pack8(const f32x16& x, int s) {
  u32x4 p = {cvtpk(x[8 * s], x[8 * s + 1]), cvtpk(x[8 * s + 2], x[8 * s + 3]), cvtpk(x[8 * s + 4], x[8 * s + 5]), cvtpk(x[8 * s + 6], x[8 * s + 7])};
  return __builtin_bit_cast(bf16x8, p);
}
DI bf16x8 lds_rd128(lds_char* base, unsigned off) { return *(lds_bf16x8*)(base + off); }
DI bf16x8 lds_tr2(lds_char* base, unsigned off0, unsigned off1) {
  bf16x4 lo = __builtin_amdgcn_ds_read_tr16_b64_v4bf16((lds_bf16x4*)(base + off0));
  bf16x4 hi = __builtin_amdgcn_ds_read_tr16_b64_v4bf16((lds_bf16x4*)(base + off1));
  return __builtin_shufflevector(lo, hi, 0, 1, 2, 3, 4, 5, 6, 7);
}
template <int POL> DI void dma16(__amdgpu_buffer_rsrc_t r, unsigned voff, unsigned soff, unsigned m0v) {
  if constexpr (POL == 0) asm volatile("s_mov_b32 m0, %3\n\ts_nop 0\n\tbuffer_load_dwordx4 %0, %1, %2 offen lds" ::"v"(voff), "s"(r), "s"(soff), "s"(m0v) : "memory");
  else if constexpr (POL == 1) asm volatile("s_mov_b32 m0, %3\n\ts_nop 0\n\tbuffer_load_dwordx4 %0, %1, %2 offen nt lds" ::"v"(voff), "s"(r), "s"(soff), "s"(m0v) : "memory");
  else asm volatile("s_mov_b32 m0, %3\n\ts_nop 0\n\tbuffer_load_dwordx4 %0, %1, %2 offen sc1 lds" ::"v"(voff), "s"(r), "s"(soff), "s"(m0v) : "memory");
}
template <int N> DI void wait_vm() { asm volatile("s_waitcnt vmcnt(%0)" ::"i"(N) : "memory"); }
DI void bar() { SB(); asm volatile("s_waitcnt lgkmcnt(0)\n\ts_barrier" ::: "memory"); SB(); }
DI float pl32_max(float v) {
  auto r = __builtin_amdgcn_permlane32_swap(__float_as_uint(v), __float_as_uint(v), false, false);
  return fmaxf(__uint_as_float(r[0]), __uint_as_float(r[1]));
}
DI float pl32_sum(float v) {
  auto r = __builtin_amdgcn_permlane32_swap(__float_as_uint(v), __float_as_uint(v), false, false);
  return __uint_as_float(r[0]) + __uint_as_float(r[1]);
}
DI void add8(float (&v)[8], const u32x4& u) {
#pragma unroll
  for (int e = 0; e < 4; ++e) { v[2 * e] += __uint_as_float(u[e] << 16); v[2 * e + 1] += __uint_as_float(u[e] & 0xffff0000u); }
}
DI unsigned lane_id() { unsigned l; asm volatile("v_mbcnt_lo_u32_b32 %0, -1, 0\n\tv_mbcnt_hi_u32_b32 %0, -1, %0" : "=v"(l)); return l; }
DI void poll_flags(gu64* flags, unsigned idx0, unsigned stride, unsigned n, unsigned skip, unsigned long long tag, unsigned lane) {
  const bool mine = lane < n && lane != skip;
  gu64* p = flags + (size_t)(idx0 + (mine ? lane : 0) * stride) * 16;
  for (unsigned spins = 0; spins < 400000u; ++spins) {
    const unsigned long long v = __hip_atomic_load(p, __ATOMIC_RELAXED, __HIP_MEMORY_SCOPE_AGENT);
    if (__all(!mine || v == tag)) break;
    __builtin_amdgcn_s_sleep(1);
  }
}

__global__ __launch_bounds__(768, 3) void mha_kernel(const __hip_bfloat16* __restrict__ x, const __hip_bfloat16* __restrict__ kc,
                                                     const __hip_bfloat16* __restrict__ vc, const __hip_bfloat16* __restrict__ wq,
                                                     const __hip_bfloat16* __restrict__ wo, __hip_bfloat16* __restrict__ out,
                                                     char* __restrict__ ws) {
  __shared__ __attribute__((aligned(1024))) char smem[LDS_TOTAL];
  lds_char* lds = (lds_char*)smem;
  const unsigned ldsb = (unsigned)(size_t)smem;
  const unsigned tid = threadIdx.x, w = rfl(tid >> 6);
  const unsigned xcd = blockIdx.x & 7, jj = blockIdx.x >> 3;
  const unsigned b = 4 * (xcd & 1) + (jj & 3), hd = 8 * (xcd >> 1) + (jj >> 2);
  gu64* flags = (gu64*)(ws + WS_FLAG);
  unsigned long long epoch;
  u32x2 ev;
  auto epoch_issue = [&]() { asm volatile("global_load_dwordx2 %0, %1, off sc1" : "=v"(ev) : "v"((const char*)(ws + WS_FLAG) + (512 + b) * 128) : "memory"); };
  auto epoch_take = [&]() {
    asm volatile("" : "+v"(ev));
    epoch = ((unsigned long long)rfl(ev[1]) << 32) | rfl(ev[0]);
  };
  const __amdgpu_buffer_rsrc_t rwo = __builtin_amdgcn_make_buffer_rsrc((void*)wo, (short)0, NH * DH * DM * 2, 0x00020000);
  bf16x8 qf[8];
  f32x16 oacc[4];
  float m, l;
  {
    const unsigned lane = tid & 63, r = lane & 31, h = lane >> 5;
    const unsigned t = w >> 1, j = w & 1;
    const unsigned g1 = (lane >> 4) & 1, qq = (lane & 15) >> 2, pp = lane & 3, sp = ((pp & 1) << 1) | (pp >> 1);
    const __amdgpu_buffer_rsrc_t rk = __builtin_amdgcn_make_buffer_rsrc((void*)kc, (short)0, NB * NH * SK * DH * 2, 0x00020000);
    const __amdgpu_buffer_rsrc_t rv = __builtin_amdgcn_make_buffer_rsrc((void*)vc, (short)0, NB * NH * SK * DH * 2, 0x00020000);
    const unsigned skv = (b * NH + hd) * (SK * DH * 2);
    const unsigned vk = (lane >> 4) * 256 + ((lane & 15) ^ (lane >> 4)) * 16;
    const unsigned vv = (lane >> 4) * 256 + ((lane & 15) ^ ((lane >> 4) << 2)) * 16;

    auto kv4 = [&](int s) {
      const unsigned lw = w - 4;
      const unsigned dst = ldsb + kv_slot(s) + lw * 4096u;
      const unsigned so = skv + ((lw >> 1) & 1) * 65536u + (lw & 1) * 4096u + (unsigned)s * 8192u;
#pragma unroll
      for (int p = 0; p < 4; ++p) {
        if (lw < 4) dma16<1>(rk, vk ^ (p << 6), so + p * 1024u, dst + p * 1024u);
        else dma16<1>(rv, vv, so + p * 1024u, dst + p * 1024u);
      }
    };
    const unsigned koff = 3 * (((jj >> 2) + (jj & 3)) % 3);
    if (w >= 4) {
      const unsigned lw = w - 4;
      const __amdgpu_buffer_rsrc_t rx = __builtin_amdgcn_make_buffer_rsrc((void*)x, (short)0, NB * SQ * DM * 2, 0x00020000);
      const __amdgpu_buffer_rsrc_t rw = __builtin_amdgcn_make_buffer_rsrc((void*)wq, (short)0, DM * NH * DH * 2, 0x00020000);
      const unsigned vx = (lane >> 3) * 1152 + (((lane & 7) ^ (lane >> 4) ^ ((lw & 1) << 2)) * 16);
      const unsigned sx = (b * SQ + 8 * lw) * 1152u;
      const unsigned vw = (lane >> 4) * 8192 + (((lane & 15) ^ ((lane >> 4) << 2)) * 16);
      const unsigned sw = 4 * lw * 8192u + hd * 256u;
      auto issue_q = [&](int i) {
        unsigned st = koff + i; st = st >= 9 ? st - 9 : st;
        const unsigned slot = ldsb + (unsigned)(i % 3) * QS_BYTES;
#pragma unroll
        for (int p = 0; p < 3; ++p) dma16<0>(rx, vx, sx + (unsigned)(64 * p) * 1152u + st * 128u, slot + (lw + 8 * p) * 1024u);
#pragma unroll
        for (int p = 0; p < 2; ++p) dma16<0>(rw, vw, sw + (st * 64 + 32 * p) * 8192u, slot + QS_W + (lw + 8 * p) * 1024u);
      };
      epoch_issue();
      issue_q(0); issue_q(1);
      wait_vm<5>(); epoch_take(); bar(); issue_q(2);
      wait_vm<5>(); bar(); issue_q(3);
      wait_vm<5>(); bar(); issue_q(4); kv4(0);
      wait_vm<9>(); bar(); issue_q(5);
      wait_vm<9>(); bar(); issue_q(6);
      wait_vm<5>(); bar(); issue_q(7);
      wait_vm<5>(); bar(); issue_q(8);
      wait_vm<5>(); bar(); kv4(1);
      wait_vm<4>(); bar(); kv4(2);
      bar();
      bar();
    } else {
      epoch_issue(); wait_vm<0>(); epoch_take();
      if (w < 2) {
        unsigned long long pc;
        asm volatile("s_getpc_b64 %0" : "=s"(pc));
        const unsigned off = (jj * 2 + w) * 1024u;
        if (off + 1024u <= CODE_BYTES) {
          u32x4 t;
          const char* p = (const char*)(pc & ~127ull) + off + lane * 16;
          asm volatile("global_load_dwordx4 %0, %1, off\n\ts_waitcnt vmcnt(0)" : "=v"(t) : "v"(p) : "memory");
        }
      }
      if (w == 2) {
        const unsigned hg_ = hd >> 3, hl_ = hd & 7, wg_ = b * NH + hd;
        const char* p = (const char*)ws + WS_FLAG;
        if (lane == 0) p = (const char*)kc + (size_t)wg_ * (SK * DH * 2);
        if (lane == 1) p = (const char*)vc + (size_t)wg_ * (SK * DH * 2);
        if (lane == 2) p = (const char*)ws + WS_O + (size_t)wg_ * 49152;
        if (lane == 3) p = (const char*)ws + WS_O + (size_t)(b * NH + 8 * hg_) * 49152;
        if (lane == 4) p = (const char*)ws + WS_O + (size_t)(b * NH + 8 * hg_ + 8) * 49152 - 64;
        if (lane >= 5 && lane < 9) p = (const char*)ws + WS_PART + (size_t)(((b * 8 + hl_) * 4 + (lane - 5)) * 4) * PART_BLK;
        if (lane == 9) p = (const char*)wo + (size_t)hg_ * (1024 * 1152);
        if (lane == 10) p = (const char*)wo + (size_t)(hg_ + 1) * (1024 * 1152) - 64;
        if (lane == 11) p = (const char*)out + (size_t)(b * SQ) * (DM * 2);
        if (lane == 12) p = (const char*)kc + (size_t)(wg_ + 1) * (SK * DH * 2) - 64;
        if (lane == 13) p = (const char*)vc + (size_t)(wg_ + 1) * (SK * DH * 2) - 64;
        unsigned t;
        asm volatile("global_load_dword %0, %1, off\n\ts_waitcnt vmcnt(0)" : "=v"(t) : "v"(p) : "memory");
      }
      const unsigned qh = w >> 1, dh = w & 1;
      const unsigned xa0 = (96 * qh + r) * 128 + ((h ^ ((r >> 1) & 7)) << 4);
      const unsigned wa0 = QS_W + (8 * h + qq) * 256 + ((4 * ((2 * dh) ^ qq) + 2 * g1 + (sp >> 1)) << 4) + (sp & 1) * 8;
      f32x16 qacc[3][2];
#pragma unroll
      for (int a = 0; a < 3; ++a)
#pragma unroll
        for (int i = 0; i < 16; ++i) { qacc[a][0][i] = 0.f; qacc[a][1][i] = 0.f; }
      bf16x8 bA[3], aA[2], bB[3], aB[2];
      unsigned xb, wb;
      auto rd = [&](bf16x8 (&bb)[3], bf16x8 (&aa)[2], int s) {
        const unsigned xs = xb ^ (s << 5), wb1 = wb ^ 64;
        aa[0] = lds_tr2(lds, wb + s * 4096, wb + s * 4096 + 1024);
        bb[0] = lds_rd128(lds, xs); bb[1] = lds_rd128(lds, xs + 4096); bb[2] = lds_rd128(lds, xs + 8192);
        aa[1] = lds_tr2(lds, wb1 + s * 4096, wb1 + s * 4096 + 1024);
      };
      auto mm = [&](bf16x8 (&bb)[3], bf16x8 (&aa)[2]) {
        qacc[0][0] = MFMA32(aa[0], bb[0], qacc[0][0]);
        qacc[1][0] = MFMA32(aa[0], bb[1], qacc[1][0]);
        qacc[2][0] = MFMA32(aa[0], bb[2], qacc[2][0]);
        qacc[0][1] = MFMA32(aa[1], bb[0], qacc[0][1]);
        qacc[1][1] = MFMA32(aa[1], bb[1], qacc[1][1]);
        qacc[2][1] = MFMA32(aa[1], bb[2], qacc[2][1]);
      };
      bar();
      xb = xa0; wb = wa0;
      asm volatile("" : "+v"(xb), "+v"(wb));
      rd(bA, aA, 0);
      auto mix = [&]() { SG_MFMA(1); SG_DSRD(2); SG_MFMA(1); SG_DSRD(2); SG_MFMA(1); SG_DSRD(1); SG_MFMA(1); SG_DSRD(1); SG_MFMA(1); SG_DSRD(1); SG_MFMA(1); SB(); };
#pragma unroll 1
      for (int st = 0; st < 8; ++st) {
        SB(); rd(bB, aB, 1); mm(bA, aA); mix();
        rd(bA, aA, 2); mm(bB, aB); mix();
        rd(bB, aB, 3); mm(bA, aA); mix();
        bar();
        const unsigned so = (unsigned)((st + 1) % 3) * QS_BYTES;
        xb = xa0 + so; wb = wa0 + so;
        asm volatile("" : "+v"(xb), "+v"(wb));
        rd(bA, aA, 0); mm(bB, aB); mix();
      }
      SB(); rd(bB, aB, 1); mm(bA, aA); mix();
      rd(bA, aA, 2); mm(bB, aB); mix();
      rd(bB, aB, 3); mm(bA, aA); mix();
      mm(bB, aB);
      bar();
#pragma unroll
      for (int a = 0; a < 3; ++a)
#pragma unroll
        for (int d = 0; d < 2; ++d) {
#pragma unroll
          for (int i = 0; i < 16; ++i) qacc[a][d][i] *= QSCALE;
#pragma unroll
          for (int s2 = 0; s2 < 2; ++s2) {
            const unsigned blk = (3 * qh + a) * 8 + 4 * dh + 2 * d + s2;
            *(lds_bf16x8*)(lds + qblk(blk) + lane * 16) = pack8(qacc[a][d], s2);
          }
        }
      bar();
    }
#pragma unroll
    for (int i = 0; i < 8; ++i) qf[i] = lds_rd128(lds, qblk(t * 8 + i) + lane * 16);

    const unsigned grp = w >> 2;
    float l0, l1;
    bool safe = false;
    const unsigned ka0 = j * 8192 + r * 256 + ((h ^ (r & 15)) << 4);
    const unsigned va0 = (16384 + j * 8192 + (4 * h + qq) * 256 + ((4 * qq + 2 * g1 + (sp >> 1)) << 4) + (sp & 1) * 8) ^ (j << 7);
    const unsigned vkx = vk ^ ((w & 3) << 6);
    const unsigned sp0 = skv + ((w >> 3) & 1) * 65536u + (w & 7) * 1024u;
    const unsigned sp1 = skv + (((w + 12) >> 3) & 1) * 65536u + ((w + 12) & 7) * 1024u;
    const unsigned sp2 = skv + (((w + 24) >> 3) & 1) * 65536u + ((w + 24) & 7) * 1024u;

    bf16x8 kf[4];
    auto step = [&](auto G_, int s) {
      constexpr int G = decltype(G_)::value;
      constexpr int WGP = 2 - G;
      constexpr int NP = WGP == 2 ? 2 : 3;
      unsigned vb = va0 + kv_slot(s), kc0 = ka0 + kv_slot(s), kb = ka0 + kv_slot(s + 1);
      asm volatile("" : "+v"(kb), "+v"(vb), "+v"(kc0));
      __builtin_amdgcn_s_setprio(1);
      f32x16 sc;
      bf16x8 vf[4][2];
      {
        bf16x8 kg[4];
#pragma unroll
        for (int ks = 0; ks < 4; ++ks) kg[ks] = lds_rd128(lds, kc0 ^ ((ks + 4) << 5));
#pragma unroll
        for (int i = 0; i < 16; ++i) sc[i] = 0.f;
#pragma unroll
        for (int ks = 0; ks < 4; ++ks) sc = MFMA32(kf[ks], qf[ks], sc);
#pragma unroll
        for (int d = 0; d < 4; ++d) { const unsigned va = vb ^ (d << 6); vf[d][0] = lds_tr2(lds, va, va + 2048); }
#pragma unroll
        for (int ks = 0; ks < 4; ++ks) sc = MFMA32(kg[ks], qf[ks + 4], sc);
        SG_MFMA(1); SG_DSRD(2); SG_MFMA(1); SG_DSRD(2); SG_MFMA(1); SG_DSRD(2); SG_MFMA(1); SG_DSRD(2);
        SG_MFMA(1); SG_DSRD(2); SG_MFMA(1); SG_DSRD(2); SG_MFMA(2);
        SB();
        asm volatile("" : "+v"(sc));
      }
      __builtin_amdgcn_s_setprio(0);
      if constexpr (G == 1) wait_vm<NP>();
      bar();
#pragma unroll
      for (int d = 0; d < 4; ++d) { const unsigned va = vb ^ (d << 6); vf[d][1] = lds_tr2(lds, va + 4096, va + 4096 + 2048); }
      SB();
      if (safe) {
        const float t0 = fmaxf(fmaxf(sc[0], sc[1]), sc[2]), t1 = fmaxf(fmaxf(sc[3], sc[4]), sc[5]), t2 = fmaxf(fmaxf(sc[6], sc[7]), sc[8]);
        const float t3 = fmaxf(fmaxf(sc[9], sc[10]), sc[11]), t4 = fmaxf(fmaxf(sc[12], sc[13]), sc[14]);
        float tm = fmaxf(fmaxf(fmaxf(t0, t1), t2), fmaxf(fmaxf(t3, t4), sc[15]));
        tm = pl32_max(tm);
        if (__any(tm > m + THR)) {
          const float mn = fmaxf(m, tm);
          const float al = __builtin_amdgcn_exp2f(m - mn);
          l0 *= al; l1 *= al; m = mn;
#pragma unroll
          for (int d = 0; d < 4; ++d)
#pragma unroll
            for (int i = 0; i < 16; ++i) oacc[d][i] *= al;
        }
#pragma unroll
        for (int i = 0; i < 16; ++i) sc[i] -= m;
      }
      SB();
#pragma unroll
      for (int i = 0; i < 16; ++i) sc[i] = __builtin_amdgcn_exp2f(sc[i]);
      bf16x8 p0 = pack8(sc, 0), p1 = pack8(sc, 1);
#pragma unroll
      for (int i = 0; i < 16; i += 2) { l0 += sc[i]; l1 += sc[i + 1]; }
      asm volatile("" : "+v"(p0), "+v"(p1), "+v"(l0), "+v"(l1));
      if constexpr (G == 0) wait_vm<NP>();
      bar();
      __builtin_amdgcn_s_setprio(1);
#pragma unroll
      for (int d = 0; d < 4; ++d) oacc[d] = MFMA32(vf[d][0], p0, oacc[d]);
#pragma unroll
      for (int ks = 0; ks < 4; ++ks) kf[ks] = lds_rd128(lds, kb ^ (ks << 5));
      SG_MFMA(2); SG_DSRD(2); SG_MFMA(1); SG_DSRD(1); SG_MFMA(1); SG_DSRD(1);
      SB();
      if (s <= 4) {
        const unsigned dst = ldsb + kv_slot(s + 3) + w * 1024u, sofs = (unsigned)(s + 3) * 8192u;
        dma16<1>(rk, vkx, sp0 + sofs, dst);
        if constexpr (WGP == 0) dma16<1>(rk, vkx, sp1 + sofs, dst + 12288u); else dma16<1>(rv, vv, sp1 + sofs, dst + 12288u);
        if constexpr (WGP != 2) dma16<1>(rv, vv, sp2 + sofs, dst + 24576u);
      } else if (s == 5) {
        const unsigned pc = ((b & 3) * 8 + (hd & 7)) * 36 + (WGP == 2 ? 24 + 2 * (w - 8) : 3 * w);
        const unsigned so = (hd >> 3) * (1024u * 1152u) + pc * 1024u, vo = lane_id() * 16;
#pragma unroll
        for (int p = 0; p < NP; ++p) dma16<0>(rwo, vo, so + p * 1024u, ldsb + SPARE + (w & 7) * 1024u);
      }
      SB();
#pragma unroll
      for (int d = 0; d < 4; ++d) oacc[d] = MFMA32(vf[d][1], p1, oacc[d]);
      SB();
      asm volatile("" : "+v"(oacc[0]), "+v"(oacc[1]), "+v"(oacc[2]), "+v"(oacc[3]));
      __builtin_amdgcn_s_setprio(0);
      if constexpr (G == 2) wait_vm<NP>();
      bar();
    };
    auto run = [&](auto G_) {
      constexpr int G = decltype(G_)::value;
      {
        unsigned kb0 = ka0 + kv_slot(0);
        asm volatile("" : "+v"(kb0));
#pragma unroll
        for (int ks = 0; ks < 4; ++ks) kf[ks] = lds_rd128(lds, kb0 ^ (ks << 5));
      }
      if constexpr (G >= 1) bar();
      if constexpr (G == 2) bar();
#pragma unroll 1
      for (int s = 0; s < 8; ++s) step(G_, s);
      if constexpr (G == 0) {
        const unsigned pc = ((b & 3) * 8 + (hd & 7)) * 36 + 32 + (w & 3);
        dma16<0>(rwo, lane_id() * 16, (hd >> 3) * (1024u * 1152u) + pc * 1024u, ldsb + SPARE + (w & 3) * 1024u);
      }
      if constexpr (G == 0) bar();
      if constexpr (G <= 1) bar();
    };
#pragma unroll 1
    for (;;) {
#pragma unroll
      for (int d = 0; d < 4; ++d)
#pragma unroll
        for (int i = 0; i < 16; ++i) oacc[d][i] = 0.f;
      m = safe ? -__builtin_inff() : 0.f; l0 = 0.f; l1 = 0.f;
      bar();
      if (grp == 2) run(IC<0>{});
      else if (grp == 1) run(IC<1>{});
      else run(IC<2>{});
      const unsigned ln = lane_id();
      l = pl32_sum(l0 + l1);
      const unsigned xw = w * 4096 + ln * 16;
#pragma unroll
      for (int i = 0; i < 4; ++i) *(lds_bf16x8*)(lds + xw + i * 1024) = pack8(oacc[2 + (i >> 1)], i & 1);
      if (ln < 32) { f32x2 ml = {m, l}; *(lds_f32x2*)(lds + ML_OFF + w * 256 + ln * 8) = ml; }
      const bool bad = __any(!(l > L_MIN && l < L_MAX));
      if (ln == 0) *(__attribute__((address_space(3))) unsigned*)(lds + VOTE_OFF + w * 4) = bad ? 1u : 0u;
      bar();
      if (safe) break;
      const unsigned vote = *(__attribute__((address_space(3))) unsigned*)(lds + VOTE_OFF + (ln < 12 ? ln : 0) * 4);
      if (!__any(vote != 0)) break;
      safe = true;
      bar();
      if (w >= 4) {
        const unsigned lw = rfl(w - 4), sk = rfl((b * NH + hd) * (SK * DH * 2));
        const unsigned vo = (ln >> 4) * 256 + (lw < 4 ? ((ln & 15) ^ (ln >> 4)) : ((ln & 15) ^ ((ln >> 4) << 2))) * 16;
        const __amdgpu_buffer_rsrc_t rr = __builtin_amdgcn_make_buffer_rsrc((void*)(lw < 4 ? kc : vc), (short)0, NB * NH * SK * DH * 2, 0x00020000);
#pragma unroll 1
        for (unsigned q = 0; q < 12; ++q) {
          const unsigned st = q >> 2, p = q & 3;
          dma16<1>(rr, lw < 4 ? vo ^ (p << 6) : vo, rfl(sk + ((lw >> 1) & 1) * 65536u + (lw & 1) * 4096u + st * 8192u + p * 1024u), rfl(ldsb + kv_slot(st) + lw * 4096u + p * 1024u));
        }
        wait_vm<0>();
      }
    }
  }

  const unsigned lane = lane_id();
  const unsigned hg = hd >> 3, hl = hd & 7, rh = hl & 1, cq = hl >> 1;
  const unsigned long long tag = epoch + 1;
  const unsigned wg = b * NH + hd;
  auto wo_voff = [&](int i) {
    const unsigned n = 64 * i + lane, p = n / 18, ch = n - 18 * p;
    const unsigned k = (p & 19) | ((p & 4) << 1) | ((p & 8) >> 1);
    return k * 1152 + ch * 16;
  };
  auto wo_issue = [&](const unsigned (&vo)[9], unsigned i) {
    const unsigned kh = w & 1, hh = (hl + (i >> 1)) & 7;
    const unsigned so = ((8 * hg + hh) * 128 + (2 * (i & 1) + kh) * 32) * 1152u + cq * 288u;
    const unsigned dst = ldsb + p2_wslot(i) + kh * 9216u;
#pragma unroll
    for (int p = 0; p < 9; ++p) dma16<0>(rwo, vo[p], so, dst + p * 1024u);
  };
  unsigned wvo[9];
  if (w == 8 || w == 9) {
#pragma unroll
    for (int p = 0; p < 9; ++p) wvo[p] = wo_voff(p);
  }

  {
    const unsigned r = lane & 31, t = w >> 1, j = w & 1;
    f32x2 o = *(lds_f32x2*)(lds + ML_OFF + (w ^ 1) * 256 + r * 8);
    const float mn = fmaxf(m, o[0]);
    const float f = __builtin_amdgcn_exp2f(m - mn), fo = __builtin_amdgcn_exp2f(o[0] - mn);
    const float inv = 1.0f / (l * f + o[1] * fo);
    const float sa = f * inv, sb = fo * inv;
    const unsigned xr = (w ^ 1) * 4096 + lane * 16;
#pragma unroll
    for (int i = 0; i < 4; ++i) {
      const u32x4 v = __builtin_bit_cast(u32x4, lds_rd128(lds, xr + i * 1024));
#pragma unroll
      for (int e = 0; e < 4; ++e) {
        oacc[i >> 1][8 * (i & 1) + 2 * e] = oacc[i >> 1][8 * (i & 1) + 2 * e] * sa + __uint_as_float(v[e] << 16) * sb;
        oacc[i >> 1][8 * (i & 1) + 2 * e + 1] = oacc[i >> 1][8 * (i & 1) + 2 * e + 1] * sa + __uint_as_float(v[e] & 0xffff0000u) * sb;
      }
    }
    const __amdgpu_buffer_rsrc_t ro = __builtin_amdgcn_make_buffer_rsrc((void*)(ws + WS_O), (short)0, O_BYTES, 0x00020000);
    const unsigned so = ((wg * 6 + t) * 8 + 4 * j) * 1024u;
#pragma unroll
    for (int i = 0; i < 4; ++i)
      __builtin_amdgcn_raw_buffer_store_b128(__builtin_bit_cast(u32x4, pack8(oacc[i >> 1], i & 1)), ro, so + lane * 16 + i * 1024, 0, 16);
    if (w == 8 || w == 9) { wo_issue(wvo, 0); wo_issue(wvo, 1); wait_vm<18>(); } else wait_vm<0>();
    bar();
    if (tid == 0) __hip_atomic_store(flags + (size_t)wg * 16, tag, __ATOMIC_RELAXED, __HIP_MEMORY_SCOPE_AGENT);
  }
  f32x4 acc[3][5];
#pragma unroll
  for (int a = 0; a < 3; ++a)
#pragma unroll
    for (int n = 0; n < 5; ++n) acc[a][n] = f32x4{0.f, 0.f, 0.f, 0.f};
  const unsigned nh = (w >> 2) & 1, kh = w & 1, qh = (w >> 1) & 1;
  if (w >= 10) {
    const __amdgpu_buffer_rsrc_t ro = __builtin_amdgcn_make_buffer_rsrc((void*)(ws + WS_O), (short)0, O_BYTES, 0x00020000);
    auto o_issue = [&](unsigned i) {
      const unsigned hh = (hl + (i >> 1)) & 7;
      const unsigned so = (((b * NH + 8 * hg + hh) * 6 + 3 * rh) * 8 + 2 * (2 * (i & 1) + kh)) * 1024u;
      const unsigned dst = ldsb + p2_oslot(i) + kh * 6144u;
#pragma unroll
      for (int q = 0; q < 3; ++q) { dma16<2>(ro, lane * 16, so + q * 8192u, dst + q * 2048u); dma16<2>(ro, lane * 16, so + q * 8192u + 1024u, dst + q * 2048u + 1024u); }
    };
    gu64* fp = flags + (size_t)(b * NH + 8 * hg + (lane & 7)) * 16;
    unsigned long long pv;
    auto poll_issue = [&]() { asm volatile("global_load_dwordx2 %0, %1, off sc1" : "=v"(pv) : "v"(fp) : "memory"); };
    unsigned rdy = 1u << hl;
    auto poll_take = [&]() {
      asm volatile("s_waitcnt vmcnt(0)" : "+v"(pv) :: "memory");
      rdy |= (unsigned)__ballot(pv == tag) & 0xffu;
    };
    o_issue(0); o_issue(1);
    poll_issue();
    bool pending = true;
    unsigned spins = 0;
#pragma unroll 1
    for (unsigned i = 0; i < 16; ++i) {
      if (pending) wait_vm<7>(); else if (i <= 14) wait_vm<6>(); else wait_vm<0>();
      bar();
      if (i + 2 < 16) {
        const unsigned hh = (hl + ((i + 2) >> 1)) & 7;
        if (pending) { poll_take(); pending = false; }
        while (!((rdy >> hh) & 1) && spins < 400000u) { __builtin_amdgcn_s_sleep(1); poll_issue(); poll_take(); ++spins; }
        o_issue(i + 2);
      }
    }
  } else if (w >= 8) {
    wo_issue(wvo, 2); wo_issue(wvo, 3); wo_issue(wvo, 4);
#pragma unroll 1
    for (unsigned i = 0; i < 16; ++i) {
      if (i <= 11) wait_vm<36>(); else if (i == 12) wait_vm<27>(); else if (i == 13) wait_vm<18>(); else if (i == 14) wait_vm<9>(); else wait_vm<0>();
      bar();
      if (i + 5 < 16) wo_issue(wvo, i + 5);
    }
  } else {
    const unsigned c = lane & 15, g = lane >> 4;
    unsigned ba[3];
#pragma unroll
    for (int a = 0; a < 3; ++a) {
      const unsigned qi = 3 * qh + a;
      ba[a] = kh * 6144 + ((qi >> 1) * 2 + (g >> 1)) * 1024 + (16 * (qi & 1) + c + 32 * (g & 1)) * 16;
    }
    const unsigned aa = kh * 9216 + ((g >> 1) * 16 + (g & 1) * 4 + (c >> 2)) * 288 + 5 * nh * 32 + (c & 3) * 8;
    bf16x8 bX[3], aX[5], bY[3], aY[5];
    auto rd = [&](auto NT_, bf16x8 (&bb)[3], bf16x8 (&av)[5], unsigned i) {
      constexpr int NT = decltype(NT_)::value;
      unsigned ob = p2_oslot(i), wb = aa + p2_wslot(i);
      asm volatile("" : "+v"(wb));
#pragma unroll
      for (int a = 0; a < 3; ++a) bb[a] = lds_rd128(lds, ba[a] + ob);
#pragma unroll
      for (int n = 0; n < NT; ++n) av[n] = lds_tr2(lds, wb + n * 32, wb + n * 32 + 2304);
    };
    auto mm = [&](auto NT_, bf16x8 (&bb)[3], bf16x8 (&av)[5]) {
      constexpr int NT = decltype(NT_)::value;
#pragma unroll
      for (int n = 0; n < NT; ++n)
#pragma unroll
        for (int a = 0; a < 3; ++a) acc[a][n] = MFMA16(av[n], bb[a], acc[a][n]);
    };
    auto mix = [&](auto NT_) {
      constexpr int NT = decltype(NT_)::value;
#pragma unroll
      for (int k = 0; k < 3 + 2 * NT; ++k) {
        __builtin_amdgcn_sched_group_barrier(0x008, 1, 0);
        __builtin_amdgcn_sched_group_barrier(0x100, 1, 0);
      }
      __builtin_amdgcn_sched_group_barrier(0x008, 3 * NT, 0);
    };
    auto gemm = [&](auto NT_) {
      bar();
      rd(NT_, bX, aX, 0);
#pragma unroll 1
      for (unsigned i = 1; i < 15; i += 2) {
        bar(); rd(NT_, bY, aY, i); mm(NT_, bX, aX); mix(NT_);
        bar(); rd(NT_, bX, aX, i + 1); mm(NT_, bY, aY); mix(NT_);
      }
      bar(); rd(NT_, bY, aY, 15); mm(NT_, bX, aX); mix(NT_);
      SB(); mm(NT_, bY, aY);
    };
    if (nh == 0) gemm(IC<5>{}); else gemm(IC<4>{});
  }
  bar();
  if (w < 8) {
    const unsigned c = lane & 15, g = lane >> 4;
    const unsigned base = kh * IMG_BYTES + (48 * qh + c) * IMG_STRIDE + (80 * nh + 4 * g) * 2;
#pragma unroll
    for (int a = 0; a < 3; ++a)
#pragma unroll
      for (int n = 0; n < 5; ++n)
        if (n < 4 || nh == 0) {
          const u32x2 pk = {cvtpk(acc[a][n][0], acc[a][n][1]), cvtpk(acc[a][n][2], acc[a][n][3])};
          *(__attribute__((address_space(3))) u32x2*)(lds + base + a * 16 * IMG_STRIDE + n * 32) = pk;
        }
  }
  bar();
  {
    const __amdgpu_buffer_rsrc_t rp = __builtin_amdgcn_make_buffer_rsrc((void*)(ws + WS_PART), (short)0, PART_BYTES, 0x00020000);
    const unsigned pbase = ((b * 8 + hl) * 4 + hg) * 4 * PART_BLK;
#pragma unroll 1
    for (unsigned n = tid; n < 72 * 18; n += 768) {
      const unsigned rr = n / 18, ch = n - rr * 18;
      unsigned dst = rr / 24; const unsigned row = rr - dst * 24;
      dst += dst >= hg ? 1 : 0;
      const unsigned a0 = (24 * dst + row) * IMG_STRIDE + ch * 16;
      float v[8] = {0.f, 0.f, 0.f, 0.f, 0.f, 0.f, 0.f, 0.f};
      add8(v, __builtin_bit_cast(u32x4, lds_rd128(lds, a0))); add8(v, __builtin_bit_cast(u32x4, lds_rd128(lds, IMG_BYTES + a0)));
      const u32x4 pk = {cvtpk(v[0], v[1]), cvtpk(v[2], v[3]), cvtpk(v[4], v[5]), cvtpk(v[6], v[7])};
      __builtin_amdgcn_raw_buffer_store_b128(pk, rp, dst * PART_BLK + row * 288 + ch * 16 + pbase, 0, 16);
    }
    wait_vm<0>();
    bar();
    if (tid == 0) __hip_atomic_store(flags + (size_t)(256 + wg) * 16, tag, __ATOMIC_RELAXED, __HIP_MEMORY_SCOPE_AGENT);
    if (w == 0) poll_flags(flags, 256 + b * NH + hl, 8, 4, hg, tag, lane);
    bar();
    if (tid < 24 * 18) {
      const unsigned row = tid / 18, ch = tid - row * 18;
      u32x4 u[3];
#pragma unroll
      for (int k = 0; k < 3; ++k) {
        const unsigned src = k + (k >= (int)hg ? 1 : 0);
        u[k] = __builtin_amdgcn_raw_buffer_load_b128(rp, (((b * 8 + hl) * 4 + src) * 4 + hg) * PART_BLK + row * 288 + ch * 16, 0, 16);
      }
      const unsigned a0 = (24 * hg + row) * IMG_STRIDE + ch * 16;
      float v[8] = {0.f, 0.f, 0.f, 0.f, 0.f, 0.f, 0.f, 0.f};
      add8(v, __builtin_bit_cast(u32x4, lds_rd128(lds, a0))); add8(v, __builtin_bit_cast(u32x4, lds_rd128(lds, IMG_BYTES + a0)));
#pragma unroll
      for (int k = 0; k < 3; ++k) add8(v, u[k]);
      const u32x4 o = {cvtpk(v[0], v[1]), cvtpk(v[2], v[3]), cvtpk(v[4], v[5]), cvtpk(v[6], v[7])};
      const __amdgpu_buffer_rsrc_t rout = __builtin_amdgcn_make_buffer_rsrc((void*)out, (short)0, NB * SQ * DM * 2, 0x00020000);
      __builtin_amdgcn_raw_buffer_store_b128(o, rout, ((b * SQ + 96 * rh + 24 * hg + row) * DM + 144 * cq + 8 * ch) * 2, 0, 2);
    }
    if (tid == 0 && hd == 0) __hip_atomic_store(flags + (size_t)(512 + b) * 16, tag, __ATOMIC_RELAXED, __HIP_MEMORY_SCOPE_AGENT);
  }
}

extern "C" void kernel_launch(void* const* d_in, const int* in_sizes, int n_in, void* d_out, int out_size, void* d_ws, size_t ws_size,
                              hipStream_t stream) {
  const __hip_bfloat16* x = (const __hip_bfloat16*)d_in[0];
  const __hip_bfloat16* kc = (const __hip_bfloat16*)d_in[1];
  const __hip_bfloat16* vc = (const __hip_bfloat16*)d_in[2];
  const __hip_bfloat16* wq = (const __hip_bfloat16*)d_in[3];
  const __hip_bfloat16* wo = (const __hip_bfloat16*)d_in[4];
  mha_kernel<<<256, 768, 0, stream>>>(x, kc, vc, wq, wo, (__hip_bfloat16*)d_out, (char*)d_ws);
}
```
